```python
import torch

def _softmax(x, axis=-1):
    return torch.softmax(x, dim=axis)
def _gelu(x):
    return torch.nn.functional.gelu(x, approximate="tanh")
def _silu(x):
    return torch.nn.functional.silu(x)
def _sigmoid(x):
    return torch.sigmoid(x)
def _layernorm(x, axis=-1, eps=1e-5):
    m = x.mean(dim=axis, keepdim=True)
    v = x.var(dim=axis, keepdim=True, unbiased=False)
    return (x - m) / torch.sqrt(v + eps)
def _scatter_add(tgt, idx, src, axis=0):
    return tgt.clone().index_add_(axis, idx, src)
def _segment_sum(src, seg, n):
    out = torch.zeros((n,) + src.shape[1:], dtype=src.dtype)
    return out.index_add_(0, seg, src)
def _pad(x, pw):
    flat = tuple(v for pair in reversed(pw) for v in pair)
    return torch.nn.functional.pad(x, flat)

kernel_name = 'gated_mix__spectral_fnet__softmax_full_mha__prompt'

def setup_inputs():

    _w = (lambda a: (lambda t: t.to(torch.bfloat16) if t.is_floating_point() else t)(torch.as_tensor(a)))
    def _A_setup():
        B, S, D = 1, 2048, 640
        ns = torch.arange(S)[:, None] * torch.arange(S)[None, :]
        nd = torch.arange(D)[:, None] * torch.arange(D)[None, :]
        Ws_re = torch.cos(-2.0 * torch.pi * ns / S).to(float)
        Ws_im = torch.sin(-2.0 * torch.pi * ns / S).to(float)
        Wd_re = torch.cos(-2.0 * torch.pi * nd / D).to(float)
        Wd_im = torch.sin(-2.0 * torch.pi * nd / D).to(float)
        return {
            "x": _w((lambda *s: torch.randn(*s, dtype=torch.bfloat16) * 0.05)(B, S, D)),
            "dft_seq_re": _w(Ws_re),
            "dft_seq_im": _w(Ws_im),
            "dft_hid_re": _w(Wd_re),
            "dft_hid_im": _w(Wd_im),
        }
    _A_in = _A_setup()

    def _B_setup():
        B, S, D = 1, 2048, 640
        return {
            "x": _w((lambda *s: torch.randn(*s, dtype=torch.bfloat16) * 0.05)(B, S, D)),
            "wqkv": _w((lambda *s: torch.randn(*s, dtype=torch.bfloat16) * 1.6)(D, 3 * D)),
            "wo": _w((lambda *s: torch.randn(*s, dtype=torch.bfloat16) * 1.6)(D, D)),
        }
    _B_in = _B_setup()

    return {
        "A_x": _A_in["x"],
        "A_dft_seq_re": _A_in["dft_seq_re"],
        "A_dft_seq_im": _A_in["dft_seq_im"],
        "A_dft_hid_re": _A_in["dft_hid_re"],
        "A_dft_hid_im": _A_in["dft_hid_im"],
        "B_x": _B_in["x"],
        "B_wqkv": _B_in["wqkv"],
        "B_wo": _B_in["wo"],
        "gate_w": _w((lambda *s: torch.randn(*s, dtype=torch.bfloat16) * 0.05)(640) * 16.0),
    }

def reference(A_x, A_dft_seq_re, A_dft_seq_im, A_dft_hid_re, A_dft_hid_im, B_x, B_wqkv, B_wo, gate_w):
    def _A_ref(x, dft_seq_re, dft_seq_im, dft_hid_re, dft_hid_im):
        a = torch.einsum("bsd,ed->bse", x, dft_hid_re)
        b = torch.einsum("bsd,ed->bse", x, dft_hid_im)
        y = torch.einsum("ts,bse->bte", dft_seq_re, a) - torch.einsum("ts,bse->bte", dft_seq_im, b)
        return y

    def _B_ref(x, wqkv, wo):
        B, S, D = x.shape
        H = 32; Dh = D // H
        qkv = (lambda x, *d: x.permute(*d))((x @ wqkv).reshape(B, S, 3, H, Dh), 2, 0, 3, 1, 4)
        q, k, v = qkv[0], qkv[1], qkv[2]
        scale = Dh ** -0.5
        scores = torch.einsum("bhqd,bhkd->bhqk", q, k) * scale
        p = _softmax(scores, axis=-1)
        o = torch.einsum("bhqk,bhkd->bhqd", p, v)
        return (lambda x, *d: x.permute(*d))(o, 0, 2, 1, 3).reshape(B, S, D) @ wo

    x = A_x
    g = _sigmoid((x * gate_w).sum(-1, keepdims=True))
    a = _A_ref(x, A_dft_seq_re, A_dft_seq_im, A_dft_hid_re, A_dft_hid_im)
    b = _B_ref(x, B_wqkv, B_wo)
    return g * a + (1 - g) * b
```

```cpp
#include <hip/hip_runtime.h>
#include <hip/hip_bf16.h>
typedef __hip_bfloat16 bf16;
constexpr int S = 2048, D = 640, H = 32, DH = 20;

__device__ __forceinline__ float b2f(bf16 v) { return __bfloat162float(v); }

template <bool BT>
__global__ void gemm_naive(const bf16* A, const bf16* B, bf16* C, int M, int N, int K, int lda, int ldb, int ldc) {
    __shared__ float As[16][17], Bs[16][17];
    int tx = threadIdx.x & 15, ty = threadIdx.x >> 4;
    int m = blockIdx.y * 16 + ty, n = blockIdx.x * 16 + tx;
    float acc = 0.f;
    for (int k0 = 0; k0 < K; k0 += 16) {
        As[ty][tx] = b2f(A[(size_t)m * lda + k0 + tx]);
        if (BT) Bs[tx][ty] = b2f(B[(size_t)(blockIdx.x * 16 + ty) * ldb + k0 + tx]);
        else Bs[ty][tx] = b2f(B[(size_t)(k0 + ty) * ldb + n]);
        __syncthreads();
        for (int k = 0; k < 16; ++k) acc += As[ty][k] * Bs[k][tx];
        __syncthreads();
    }
    C[(size_t)m * ldc + n] = __float2bfloat16(acc);
}

__global__ void attn_naive(const bf16* qkv, bf16* o) {
    int h = blockIdx.y, q = blockIdx.x * 64 + threadIdx.x;
    float qv[DH], acc[DH];
    for (int d = 0; d < DH; ++d) { qv[d] = b2f(qkv[(size_t)q * 3 * D + h * DH + d]) * rsqrtf((float)DH); acc[d] = 0.f; }
    float m = -1e30f, l = 0.f;
    for (int k = 0; k < S; ++k) {
        float s = 0.f;
        for (int d = 0; d < DH; ++d) s += qv[d] * b2f(qkv[(size_t)k * 3 * D + D + h * DH + d]);
        float mn = fmaxf(m, s), a = __expf(m - mn), p = __expf(s - mn);
        l = l * a + p;
        for (int d = 0; d < DH; ++d) acc[d] = acc[d] * a + p * b2f(qkv[(size_t)k * 3 * D + 2 * D + h * DH + d]);
        m = mn;
    }
    for (int d = 0; d < DH; ++d) o[(size_t)q * D + h * DH + d] = __float2bfloat16(acc[d] / l);
}

__global__ void combine(const bf16* x, const bf16* gw, const bf16* yr, const bf16* yi, const bf16* bo, bf16* out) {
    int s = blockIdx.x;
    __shared__ float red[64];
    float t = 0.f;
    for (int d = threadIdx.x; d < D; d += 64) t += b2f(x[(size_t)s * D + d]) * b2f(gw[d]);
    red[threadIdx.x] = t;
    __syncthreads();
    float sum = 0.f;
    for (int i = 0; i < 64; ++i) sum += red[i];
    float g = 1.f / (1.f + __expf(-sum));
    for (int d = threadIdx.x; d < D; d += 64) {
        size_t i = (size_t)s * D + d;
        out[i] = __float2bfloat16(g * (b2f(yr[i]) - b2f(yi[i])) + (1.f - g) * b2f(bo[i]));
    }
}

extern "C" void kernel_launch(void* const* d_in, const int* in_sizes, int n_in, void* d_out, int out_size, void* d_ws, size_t ws_size, hipStream_t stream) {
    const bf16* x = (const bf16*)d_in[0];
    const bf16* sre = (const bf16*)d_in[1];
    const bf16* sim = (const bf16*)d_in[2];
    const bf16* hre = (const bf16*)d_in[3];
    const bf16* him = (const bf16*)d_in[4];
    const bf16* wqkv = (const bf16*)d_in[6];
    const bf16* wo = (const bf16*)d_in[7];
    const bf16* gw = (const bf16*)d_in[8];
    bf16* ws = (bf16*)d_ws;
    bf16* a = ws; bf16* b = a + S * D; bf16* yr = b + S * D; bf16* yi = yr + S * D; bf16* o = yi + S * D; bf16* bo = o + S * D; bf16* qkv = bo + S * D;
    gemm_naive<true><<<dim3(D / 16, S / 16), 256, 0, stream>>>(x, hre, a, S, D, D, D, D, D);
    gemm_naive<true><<<dim3(D / 16, S / 16), 256, 0, stream>>>(x, him, b, S, D, D, D, D, D);
    gemm_naive<false><<<dim3(D / 16, S / 16), 256, 0, stream>>>(sre, a, yr, S, D, S, S, D, D);
    gemm_naive<false><<<dim3(D / 16, S / 16), 256, 0, stream>>>(sim, b, yi, S, D, S, S, D, D);
    gemm_naive<false><<<dim3(3 * D / 16, S / 16), 256, 0, stream>>>(x, wqkv, qkv, S, 3 * D, D, D, 3 * D, 3 * D);
    attn_naive<<<dim3(S / 64, H), 64, 0, stream>>>(qkv, o);
    gemm_naive<false><<<dim3(D / 16, S / 16), 256, 0, stream>>>(o, wo, bo, S, D, D, D, D, D);
    combine<<<S, 64, 0, stream>>>(x, gw, yr, yi, bo, (bf16*)d_out);
}
```

```cpp
#include <hip/hip_runtime.h>
#include <hip/hip_bf16.h>
#include <stdint.h>

namespace {
typedef float f32x2 __attribute__((ext_vector_type(2)));
typedef float f32x8 __attribute__((ext_vector_type(8)));
typedef float f32x16 __attribute__((ext_vector_type(16)));
typedef unsigned u32x2 __attribute__((ext_vector_type(2)));
typedef unsigned u32x4 __attribute__((ext_vector_type(4)));
typedef unsigned u32x8 __attribute__((ext_vector_type(8)));
typedef unsigned u32x16 __attribute__((ext_vector_type(16)));
typedef unsigned u32x32 __attribute__((ext_vector_type(32)));
typedef short s16x4 __attribute__((ext_vector_type(4)));
typedef __bf16 b16x2 __attribute__((ext_vector_type(2)));
typedef __bf16 b16x8 __attribute__((ext_vector_type(8)));
typedef __bf16 b16x16 __attribute__((ext_vector_type(16)));
typedef unsigned long long u64;
typedef __amdgpu_buffer_rsrc_t rsrc_t;

constexpr unsigned WS_F1  = 128;
constexpr unsigned WS_F2  = WS_F1 + 256 * 128;
constexpr unsigned WS_F3  = WS_F2 + 256 * 128;
constexpr unsigned WS_G   = WS_F3 + 256 * 128;
constexpr unsigned WS_Q   = WS_G + 8192;
constexpr unsigned WS_KV  = WS_Q + 16 * 64 * 2560;
constexpr unsigned WS_AB  = WS_KV + 16 * 64 * 5120;
constexpr unsigned WS_O   = WS_AB + 16 * 64 * 5120;
constexpr unsigned WS_Y   = WS_O + 128 * 32 * 640;
constexpr unsigned WS_END = WS_Y + 1024 * 10240;
constexpr unsigned OOB = 0x7f000000u;


constexpr float QSCALE = 0.22360679774997896f * 1.4426950408889634f;

__device__ __forceinline__ unsigned pk(float lo, float hi) {
    const f32x2 v = {lo, hi};
    return __builtin_bit_cast(unsigned, __builtin_convertvector(v, b16x2));
}
__device__ __forceinline__ u32x4 ld16(rsrc_t rs, unsigned o) { return __builtin_amdgcn_raw_buffer_load_b128(rs, o, 0, 16); }
__device__ __forceinline__ u32x2 ld8(rsrc_t rs, unsigned o) { return __builtin_amdgcn_raw_buffer_load_b64(rs, o, 0, 16); }

__device__ __forceinline__ f32x16 attn_true_max(rsrc_t rs, unsigned qo, int head, int lane) {
    const int d = lane & 31, h = lane >> 5, par = head & 1;
    const unsigned ko = WS_KV + 40960 + (head >> 1) * 2560;
    const f32x16 zero = {};
    const u32x4 q16 = ld16(rs, qo + par * 1536 + lane * 16);
    const u32x2 q4 = ld8(rs, h ? OOB : qo + 1024 + d * 16 + par * 8);
    auto scores = [&](int kt) {
        const u32x4 k16 = ld16(rs, ko + kt * 81920 + par * 1536 + lane * 16);
        const u32x2 k4 = ld8(rs, ko + kt * 81920 + 1024 + d * 16 + par * 8);
        f32x16 s = __builtin_amdgcn_mfma_f32_32x32x16_bf16(__builtin_bit_cast(b16x8, k16), __builtin_bit_cast(b16x8, q16), zero, 0, 0, 0);
        return __builtin_amdgcn_mfma_f32_32x32x8bf16_1k(__builtin_bit_cast(s16x4, k4), __builtin_bit_cast(s16x4, q4), s, 0, 0, 0);
    };
    float m = -INFINITY;
#pragma unroll 1
    for (int kt = 0; kt < 64; ++kt) {
        const f32x16 s = scores(kt);
#pragma unroll
        for (int i = 0; i < 16; ++i) m = fmaxf(m, s[i]);
    }
    m = fmaxf(m, __shfl_xor(m, 32));
    const unsigned cv = (d == 20 || d == 24) ? 0x3f803f80u : 0u;
    f32x16 o = zero;
#pragma unroll 1
    for (int kt = 0; kt < 64; ++kt) {
        const f32x16 s = scores(kt);
#pragma unroll
        for (int u = 0; u < 2; ++u) {
            const u32x4 v = ld16(rs, d < 20 ? WS_KV + kt * 81920 + (u * 2 + h) * 10240 + (head * 20 + d) * 16 : OOB) | cv;
            f32x8 p;
#pragma unroll
            for (int i = 0; i < 8; ++i) p[i] = __builtin_amdgcn_exp2f((s[8 * u + i] - m) * QSCALE);
            o = __builtin_amdgcn_smfmac_f32_32x32x32_bf16(__builtin_bit_cast(b16x8, v), __builtin_bit_cast(b16x16, p), o, (int)0xdddddddd, 0, 0);
        }
    }
    return o;
}

__global__ __launch_bounds__(512, 2) __attribute__((aligned(4096))) void k_fused(const char* __restrict__ x, const char* __restrict__ wqkv, char* ws, const char* __restrict__ hre,
                                                  const char* __restrict__ him, const char* __restrict__ sre, const char* __restrict__ sim,
                                                  const char* __restrict__ wo, const char* __restrict__ gw, char* __restrict__ out) {
    __shared__ __attribute__((aligned(1024))) char lds[163840];
    const unsigned lb = (unsigned)(uintptr_t)lds;
    const int b = blockIdx.x;
    int lane = threadIdx.x & 63;
    const int w8 = __builtin_amdgcn_readfirstlane(threadIdx.x >> 6), w = w8 & 3;
    const char* pc; asm volatile("s_getpc_b64 %0\n .Lk_beg:" : "=s"(pc));

    if (w8 < 4) {
        asm volatile(
        "s_load_dwordx2 s[20:21], %[ka], 0x10\n"
        "s_load_dwordx4 s[24:27], %[ka], 0x40\n"
        "v_mov_b32 v49, 0\n"
        "v_mov_b32 v48, 116736\n"
        "s_mov_b64 exec, 0x1\n"
        "ds_write_b32 v48, v49\n"
        "s_mov_b64 exec, -1\n"
        "v_lshlrev_b32 v190, 4, %[ln]\n"
        "s_and_b32 s40, %[b], 7\n"
        "s_mul_i32 s40, s40, 4\n"
        "s_lshr_b32 s41, %[b], 6\n"
        "s_add_u32 s40, s40, s41\n"
        "s_mul_i32 s42, s40, 20\n"
        "s_add_u32 s43, s42, 4\n"
        "s_mul_i32 s43, s43, 16\n"
        "s_add_u32 s43, s43, 0x7800\n"
        "s_lshr_b32 s40, s40, 1\n"
        "s_mul_i32 s44, s40, 0xa00\n"
        "s_add_u32 s44, s44, 0xa000\n"
        "s_add_u32 s44, s44, 0x400\n"
        "s_add_u32 s44, s44, 0xffffff00\n"
        "v_mov_b32 v25, s43\n"
        "v_mov_b32 v26, s44\n"
        "v_cmp_gt_u32 vcc, 16, %[ln]\n"
        "s_nop 1\n"
        "v_cndmask_b32 v24, v26, v25, vcc\n"
        "v_lshlrev_b32 v191, 4, %[ln]\n"
        "v_add_u32 v191, v191, v24\n"
        "s_mul_i32 s40, s40, 5\n"
        "s_and_b32 s41, s41, 1\n"
        "s_mul_i32 s44, s41, 2\n"
        "s_add_u32 s44, s40, s44\n"
        "s_lshr_b32 s40, s44, 4\n"
        "s_mul_i32 s43, s40, 0x800\n"
        "s_lshr_b32 s45, %[w], 1\n"
        "s_mul_i32 s45, s45, 0x80\n"
        "s_add_u32 s45, s43, s45\n"
        "s_add_u32 s45, s45, 0x80\n"
        "v_and_b32 v24, 1, %[ln]\n"
        "s_add_u32 s44, s44, 2\n"
        "s_lshr_b32 s44, s44, 4\n"
        "s_sub_u32 s40, s44, s40\n"
        "v_mul_u32_u24 v24, s40, v24\n"
        "v_and_b32 v25, 2, %[ln]\n"
        "v_lshrrev_b32 v26, 2, %[ln]\n"
        "v_mul_u32_u24 v27, 40, v25\n"
        "v_lshl_add_u32 v27, v26, 1, v27\n"
        "v_add_u32 v27, 0x50, v27\n"
        "s_lshr_b32 s40, %[b], 3\n"
        "s_and_b32 s40, s40, 7\n"
        "s_mul_i32 s40, s40, 2\n"
        "v_mov_b32 v25, s40\n"
        "v_cmp_gt_u32 vcc, 8, %[ln]\n"
        "s_nop 1\n"
        "v_cndmask_b32 v26, v25, v27, vcc\n"
        "v_lshl_add_u32 v186, v24, 11, s45\n"
        "v_lshl_add_u32 v186, v26, 7, v186\n"
        "s_mul_i32 s42, s42, 16\n"
        "s_add_u32 s42, s42, 0x400\n"
        "v_mul_u32_u24 v26, 52, %[ln]\n"
        "v_lshrrev_b32 v26, 10, v26\n"
        "v_mul_u32_u24 v187, 0x26c0, v26\n"
        "v_lshl_add_u32 v187, %[ln], 4, v187\n"
        "v_add_u32 v187, s42, v187\n"
        "s_mul_i32 s41, s41, 0x600\n"
        "s_add_u32 s41, s41, 0x400\n"
        "v_lshl_add_u32 v188, %[ln], 4, s41\n"
        "v_lshrrev_b32 v26, 5, %[ln]\n"
        "v_and_b32 v24, 31, %[ln]\n"
        "v_mul_u32_u24 v189, 0xa000, v26\n"
        "v_lshl_add_u32 v189, v24, 4, v189\n"
        "s_lshr_b32 s40, %[b], 3\n"
        "s_and_b32 s40, s40, 7\n"
        "s_mul_i32 s41, s40, 16\n"
        "s_mul_i32 s41, s41, 0x5000\n"
        "s_mul_i32 s42, %[w], 4\n"
        "s_mul_i32 s42, s42, 0x5000\n"
        "s_add_u32 s42, s41, s42\n"
        "s_and_b32 s41, %[b], 7\n"
        "s_mul_i32 s41, s41, 4\n"
        "s_mul_i32 s41, s41, 0x280\n"
        "s_add_u32 s41, s42, s41\n"
        "s_lshr_b32 s42, %[b], 6\n"
        "s_mul_i32 s42, s42, 0x280\n"
        "s_add_u32 s42, s41, s42\n"
        "s_add_u32 s42, s42, 0xc9a080\n"
        "v_lshrrev_b32 v24, 4, %[ln]\n"
        "v_and_b32 v24, 1, v24\n"
        "v_lshrrev_b32 v25, 5, %[ln]\n"
        "v_and_b32 v26, 15, %[ln]\n"
        "v_mul_u32_u24 v252, 0x5000, v24\n"
        "v_lshl_add_u32 v252, v25, 8, v252\n"
        "v_lshl_add_u32 v252, v26, 4, v252\n"
        "v_add_u32 v252, s42, v252\n"
        "s_add_u32 s42, s42, 0x200\n"
        "v_mul_u32_u24 v25, 0x5000, v24\n"
        "v_lshl_add_u32 v25, v26, 3, v25\n"
        "v_add_u32 v25, s42, v25\n"
        "v_mov_b32 v26, 0x7f000000\n"
        "v_cmp_gt_u32 vcc, 32, %[ln]\n"
        "s_nop 1\n"
        "v_cndmask_b32 v253, v26, v25, vcc\n"
        "s_mul_i32 s40, s40, 0x100\n"
        "s_mul_i32 s40, s40, 4\n"
        "s_mul_i32 s42, %[w], 64\n"
        "s_mul_i32 s42, s42, 4\n"
        "s_add_u32 s42, s40, s42\n"
        "s_add_u32 s42, s42, 0x18080\n"
        "v_and_b32 v25, 31, %[ln]\n"
        "v_lshl_add_u32 v254, v25, 2, s42\n"
        "v_lshrrev_b32 v25, 1, %[ln]\n"
        "v_and_b32 v26, 1, %[ln]\n"
        "v_mul_u32_u24 v48, 0xb20, v25\n"
        "v_lshl_add_u32 v48, v26, 4, v48\n"
        "v_add_u32 v48, 0x1d900, v48\n"
        "v_mov_b32 v25, 0x3f803f80\n"
        "v_cmp_eq_u32 vcc, 0, v26\n"
        "s_nop 1\n"
        "v_cndmask_b32 v52, 0, v25, vcc\n"
        "v_mov_b32 v53, v52\n"
        "v_mov_b32 v54, v52\n"
        "v_mov_b32 v55, v52\n"
        "s_mov_b32 exec_hi, 0\n"
        "ds_write_b128 v48, v[52:55]\n"
        "s_mov_b32 exec_hi, -1\n"
        "s_mul_i32 s40, %[w], 0xc00\n"
        "s_add_u32 s40, s40, 0x5000\n"
        "v_lshl_add_u32 v156, %[ln], 4, s40\n"
        "s_add_u32 s40, s40, 0x800\n"
        "s_lshr_b32 s41, %[b], 6\n"
        "s_and_b32 s41, s41, 1\n"
        "s_mul_i32 s41, s41, 8\n"
        "s_add_u32 s40, s40, s41\n"
        "v_and_b32 v24, 31, %[ln]\n"
        "v_lshl_add_u32 v157, v24, 4, s40\n"
        "v_lshlrev_b32 v158, 4, %[ln]\n"
        "v_add_u32 v158, 0x1ce00, v158\n"
        "s_add_u32 s41, s41, 0x1d700\n"
        "v_lshl_add_u32 v159, v24, 4, s41\n"
        "v_lshrrev_b32 v25, 5, %[ln]\n"
        "v_mul_u32_u24 v26, 0x140, v25\n"
        "v_lshl_add_u32 v26, v24, 4, v26\n"
        "v_add_u32 v26, 0x1d200, v26\n"
        "v_add_u32 v27, 4, v24\n"
        "v_and_b32 v27, 27, v27\n"
        "v_mov_b32 v29, 0x1d900\n"
        "v_mov_b32 v30, 0x1d910\n"
        "v_cmp_eq_u32 vcc, 24, v27\n"
        "s_nop 1\n"
        "v_cndmask_b32 v28, v30, v29, vcc\n"
        "v_cmp_gt_u32 vcc, 20, v24\n"
        "s_nop 1\n"
        "v_cndmask_b32 v160, v28, v26, vcc\n"
        "v_mov_b32 v26, 0x280\n"
        "v_cmp_gt_u32 vcc, 20, v24\n"
        "s_nop 1\n"
        "v_cndmask_b32 v28, 0, v26, vcc\n"
        "v_mov_b32 v161, v160\n"
        "v_add_u32 v161, v161, v28\n"
        "s_mul_i32 s41, %[w], 0x1a00\n"
        "s_add_u32 s41, s41, 0x1b000\n"
        "v_lshl_add_u32 v162, %[ln], 4, s41\n"
        "s_lshr_b32 s40, %[w], 1\n"
        "s_mul_i32 s40, s40, 0x2000\n"
        "v_and_b32 v28, 15, %[ln]\n"
        "v_lshrrev_b32 v24, 4, %[ln]\n"
        "v_and_b32 v26, 7, %[ln]\n"
        "v_xor_b32 v26, v26, v24\n"
        "v_lshl_add_u32 v163, v28, 7, s40\n"
        "v_lshl_add_u32 v163, v26, 4, v163\n"
        "v_xor_b32 v164, 64, v163\n"
        "s_and_b32 s40, %[w], 1\n"
        "s_mul_i32 s40, s40, 0x1400\n"
        "s_add_u32 s40, s40, 0xc000\n"
        "v_lshl_add_u32 v165, %[ln], 4, s40\n"
        "s_lshr_b32 s40, %[b], 3\n"
        "s_and_b32 s40, s40, 7\n"
        "s_mul_i32 s40, s40, 2\n"
        "s_and_b32 s42, %[b], 7\n"
        "s_and_b32 s43, s42, 1\n"
        "s_add_u32 s43, s40, s43\n"
        "s_mul_i32 s43, s43, 2\n"
        "s_lshr_b32 s42, s42, 1\n"
        "s_lshr_b32 s42, s42, 1\n"
        "s_add_u32 s42, s43, s42\n"
        "s_mul_i32 s42, s42, 64\n"
        "s_mul_i32 s42, s42, 4\n"
        "s_add_u32 s42, s42, 0x18080\n"
        "v_lshl_add_u32 v166, v28, 2, s42\n"
        "s_mul_i32 s42, %[w], 0x6400\n"
        "s_add_u32 s42, s42, 0x4b00\n"
        "v_lshrrev_b32 v28, 2, v28\n"
        "v_and_b32 v24, 1, v24\n"
        "v_and_b32 v26, 3, %[ln]\n"
        "v_mul_u32_u24 v169, 0xa0, v28\n"
        "v_mul_u32_u24 v27, 0xc80, v25\n"
        "v_add_u32 v169, v169, v27\n"
        "v_mul_u32_u24 v27, 0x280, v24\n"
        "v_add_u32 v169, v169, v27\n"
        "v_lshl_add_u32 v169, v26, 3, v169\n"
        "v_add_u32 v169, s42, v169\n"
        "s_mul_i32 s42, %[w], 0x1400\n"
        "s_add_u32 s42, s42, 0x21800\n"
        "v_lshl_add_u32 v170, %[ln], 3, s42\n"
        "s_add_u32 s41, s41, 0xc00\n"
        "v_lshl_add_u32 v171, %[ln], 2, s41\n"
        "v_mov_b32 v176, v163\n"
        "v_mov_b32 v177, v164\n"
        "s_lshr_b32 s40, %[b], 6\n"
        "s_mul_i32 s40, s40, 4\n"
        "s_and_b32 s41, %[b], 7\n"
        "s_lshr_b32 s42, s41, 1\n"
        "s_add_u32 s40, s40, s42\n"
        "s_add_u32 s43, s40, 0xfffffffb\n"
        "s_lshr_b32 s44, %[b], 3\n"
        "s_and_b32 s44, s44, 7\n"
        "s_mul_i32 s44, s44, 2\n"
        "s_and_b32 s41, s41, 1\n"
        "s_add_u32 s41, s44, s41\n"
        "s_add_u32 s41, s41, 8\n"
        "s_cmp_lt_u32 s43, 10\n"
        "s_cselect_b32 s44, s41, 0\n"
        "s_lshr_b32 s72, s44, 4\n"
        "s_mov_b32 s73, 0x14000\n"
        "s_cmp_lt_u32 s40, 5\n"
        "s_cselect_b32 s73, 0xa000, s73\n"
        "s_mov_b32 s74, s42\n"
        "s_mov_b32 s75, s40\n"
        "v_lshrrev_b32 v24, 4, %[ln]\n"
        "v_and_b32 v25, 15, %[ln]\n"
        "v_lshrrev_b32 v25, 2, v25\n"
        "v_and_b32 v26, 3, %[ln]\n"
        "v_and_b32 v27, 1, v24\n"
        "v_mov_b32 v28, v25\n"
        "v_lshl_add_u32 v28, v27, 2, v28\n"
        "s_and_b32 s40, %[w], 1\n"
        "s_mul_i32 s40, s40, 4\n"
        "v_xor_b32 v28, s40, v28\n"
        "v_lshlrev_b32 v178, 11, v24\n"
        "v_lshl_add_u32 v178, v25, 8, v178\n"
        "v_lshl_add_u32 v178, v26, 3, v178\n"
        "v_lshl_add_u32 v178, v28, 5, v178\n"
        "v_add_u32 v178, 0x10000, v178\n"
        "v_xor_b32 v179, 32, v178\n"
        "v_xor_b32 v180, 64, v178\n"
        "v_xor_b32 v181, 0x60, v178\n"
        "s_mov_b64 exec, 0\n"
        "s_cmp_eq_u32 %[w], 0\n"
        "s_cbranch_scc1 .Lk_hop4\n"
        "s_cmp_eq_u32 %[w], 1\n"
        "s_cbranch_scc1 .Lk_hop5\n"
        "s_cmp_eq_u32 %[w], 2\n"
        "s_cbranch_scc1 .Lk_hop6\n"
        ".Lk_ret0:\n"
        "s_mov_b64 exec, -1\n"
        "s_waitcnt lgkmcnt(0)\n"
        "s_mov_b64 s[52:53], s[20:21]\n"
        "s_and_b32 s53, s53, 0xffff\n"
        "s_mov_b32 s54, %[wsn]\n"
        "s_mov_b32 s55, 0x20000\n"
        "s_cmp_lt_u32 s75, 10\n"
        "s_cbranch_scc1 40f\n"
        "s_cmp_eq_u32 s75, 15\n"
        "s_cbranch_scc1 42f\n"
        "s_lshr_b32 s40, %[b], 3\n"
        "s_and_b32 s40, s40, 7\n"
        "s_mul_i32 s40, s40, 2\n"
        "s_and_b32 s41, %[b], 7\n"
        "s_and_b32 s42, s41, 1\n"
        "s_add_u32 s42, s40, s42\n"
        "s_mul_i32 s42, s42, 4\n"
        "s_lshr_b32 s40, %[w], 1\n"
        "s_mul_i32 s40, s40, 2\n"
        "s_add_u32 s40, s42, s40\n"
        "s_mul_i32 s40, s40, 0x14000\n"
        "s_add_u32 s40, s40, 0x29a080\n"
        "s_lshr_b32 s42, %[b], 6\n"
        "s_mul_i32 s42, s42, 4\n"
        "s_lshr_b32 s41, s41, 1\n"
        "s_add_u32 s41, s42, s41\n"
        "s_add_u32 s41, s41, 0xfffffff6\n"
        "s_mul_i32 s41, s41, 0x80\n"
        "s_mul_i32 s41, s41, 16\n"
        "s_add_u32 s41, s40, s41\n"
        "s_and_b32 s40, %[w], 1\n"
        "s_mul_i32 s40, s40, 64\n"
        "s_mul_i32 s40, s40, 16\n"
        "s_add_u32 s40, s41, s40\n"
        "v_lshrrev_b32 v24, 4, %[ln]\n"
        "v_and_b32 v24, 1, v24\n"
        "v_lshrrev_b32 v25, 5, %[ln]\n"
        "v_and_b32 v26, 15, %[ln]\n"
        "v_mul_u32_u24 v182, 0x5000, v24\n"
        "v_mul_u32_u24 v27, 0x2800, v25\n"
        "v_add_u32 v182, v182, v27\n"
        "v_lshl_add_u32 v182, v26, 4, v182\n"
        "v_add_u32 v182, s40, v182\n"
        "v_add_u32 v183, 256, v182\n"
        "v_add_u32 v184, 512, v182\n"
        "v_add_u32 v185, 768, v182\n"
        "s_waitcnt lgkmcnt(0)\n"
        "s_barrier\n"
        "ds_read_b128 v[80:83], v176\n"
        "ds_read_b64_tr_b16 v[96:97], v178 offset:32768\n"
        "ds_read_b64_tr_b16 v[98:99], v178 offset:33792\n"
        "ds_read_b128 v[84:87], v176 offset:2048\n"
        "ds_read_b64_tr_b16 v[100:101], v179 offset:32768\n"
        "ds_read_b64_tr_b16 v[102:103], v179 offset:33792\n"
        "ds_read_b128 v[88:91], v176 offset:4096\n"
        "ds_read_b64_tr_b16 v[104:105], v180 offset:32768\n"
        "ds_read_b64_tr_b16 v[106:107], v180 offset:33792\n"
        "ds_read_b128 v[92:95], v176 offset:6144\n"
        "ds_read_b64_tr_b16 v[108:109], v181 offset:32768\n"
        "ds_read_b64_tr_b16 v[110:111], v181 offset:33792\n"
        "s_mov_b32 s40, 0\n"
        "s_cmp_eq_u32 s40, 1\n"
        "s_waitcnt lgkmcnt(5)\n"
        "v_mfma_f32_16x16x32_bf16 v[16:19], v[80:83], v[96:99], 0\n"
        "ds_read_b128 v[112:115], v177\n"
        "v_mfma_f32_16x16x32_bf16 v[20:23], v[84:87], v[96:99], 0\n"
        "ds_read_b64_tr_b16 v[128:129], v178 offset:40960\n"
        "v_mfma_f32_16x16x32_bf16 v[32:35], v[80:83], v[100:103], 0\n"
        "ds_read_b64_tr_b16 v[130:131], v178 offset:41984\n"
        "v_mfma_f32_16x16x32_bf16 v[36:39], v[84:87], v[100:103], 0\n"
        "ds_read_b128 v[116:119], v177 offset:2048\n"
        "v_mfma_f32_16x16x32_bf16 v[24:27], v[88:91], v[96:99], 0\n"
        "ds_read_b64_tr_b16 v[132:133], v179 offset:40960\n"
        "v_mfma_f32_16x16x32_bf16 v[40:43], v[88:91], v[100:103], 0\n"
        "ds_read_b64_tr_b16 v[134:135], v179 offset:41984\n"
        "s_waitcnt lgkmcnt(6)\n"
        "v_mfma_f32_16x16x32_bf16 v[48:51], v[80:83], v[104:107], 0\n"
        "ds_read_b128 v[120:123], v177 offset:4096\n"
        "v_mfma_f32_16x16x32_bf16 v[52:55], v[84:87], v[104:107], 0\n"
        "ds_read_b64_tr_b16 v[136:137], v180 offset:40960\n"
        "v_mfma_f32_16x16x32_bf16 v[56:59], v[88:91], v[104:107], 0\n"
        "ds_read_b64_tr_b16 v[138:139], v180 offset:41984\n"
        "v_mfma_f32_16x16x32_bf16 v[28:31], v[92:95], v[96:99], 0\n"
        "ds_read_b128 v[124:127], v177 offset:6144\n"
        "v_mfma_f32_16x16x32_bf16 v[44:47], v[92:95], v[100:103], 0\n"
        "ds_read_b64_tr_b16 v[140:141], v181 offset:40960\n"
        "v_mfma_f32_16x16x32_bf16 v[60:63], v[92:95], v[104:107], 0\n"
        "ds_read_b64_tr_b16 v[142:143], v181 offset:41984\n"
        "v_mfma_f32_16x16x32_bf16 v[64:67], v[80:83], v[108:111], 0\n"
        "v_add_u32 v176, 0x4000, v176\n"
        "v_mfma_f32_16x16x32_bf16 v[68:71], v[84:87], v[108:111], 0\n"
        "v_and_b32 v176, 0xffff, v176\n"
        "v_mfma_f32_16x16x32_bf16 v[72:75], v[88:91], v[108:111], 0\n"
        "v_mfma_f32_16x16x32_bf16 v[76:79], v[92:95], v[108:111], 0\n"
        "s_branch 4f\n"
        "1:\n"
        "s_cmp_eq_u32 s40, 3\n"
        "s_waitcnt lgkmcnt(5)\n"
        "v_mfma_f32_16x16x32_bf16 v[16:19], v[80:83], v[96:99], v[16:19]\n"
        "ds_read_b128 v[112:115], v177\n"
        "v_mfma_f32_16x16x32_bf16 v[20:23], v[84:87], v[96:99], v[20:23]\n"
        "ds_read_b64_tr_b16 v[128:129], v178 offset:40960\n"
        "v_mfma_f32_16x16x32_bf16 v[32:35], v[80:83], v[100:103], v[32:35]\n"
        "ds_read_b64_tr_b16 v[130:131], v178 offset:41984\n"
        "v_mfma_f32_16x16x32_bf16 v[36:39], v[84:87], v[100:103], v[36:39]\n"
        "ds_read_b128 v[116:119], v177 offset:2048\n"
        "v_mfma_f32_16x16x32_bf16 v[24:27], v[88:91], v[96:99], v[24:27]\n"
        "ds_read_b64_tr_b16 v[132:133], v179 offset:40960\n"
        "v_mfma_f32_16x16x32_bf16 v[40:43], v[88:91], v[100:103], v[40:43]\n"
        "ds_read_b64_tr_b16 v[134:135], v179 offset:41984\n"
        "s_waitcnt lgkmcnt(6)\n"
        "v_mfma_f32_16x16x32_bf16 v[48:51], v[80:83], v[104:107], v[48:51]\n"
        "ds_read_b128 v[120:123], v177 offset:4096\n"
        "v_mfma_f32_16x16x32_bf16 v[52:55], v[84:87], v[104:107], v[52:55]\n"
        "ds_read_b64_tr_b16 v[136:137], v180 offset:40960\n"
        "v_mfma_f32_16x16x32_bf16 v[56:59], v[88:91], v[104:107], v[56:59]\n"
        "ds_read_b64_tr_b16 v[138:139], v180 offset:41984\n"
        "v_mfma_f32_16x16x32_bf16 v[28:31], v[92:95], v[96:99], v[28:31]\n"
        "ds_read_b128 v[124:127], v177 offset:6144\n"
        "v_mfma_f32_16x16x32_bf16 v[44:47], v[92:95], v[100:103], v[44:47]\n"
        "ds_read_b64_tr_b16 v[140:141], v181 offset:40960\n"
        "v_mfma_f32_16x16x32_bf16 v[60:63], v[92:95], v[104:107], v[60:63]\n"
        "ds_read_b64_tr_b16 v[142:143], v181 offset:41984\n"
        "v_mfma_f32_16x16x32_bf16 v[64:67], v[80:83], v[108:111], v[64:67]\n"
        "v_add_u32 v176, 0x4000, v176\n"
        "v_mfma_f32_16x16x32_bf16 v[68:71], v[84:87], v[108:111], v[68:71]\n"
        "v_and_b32 v176, 0xffff, v176\n"
        "v_mfma_f32_16x16x32_bf16 v[72:75], v[88:91], v[108:111], v[72:75]\n"
        "v_mfma_f32_16x16x32_bf16 v[76:79], v[92:95], v[108:111], v[76:79]\n"
        "4:\n"
        "s_waitcnt lgkmcnt(1)\n"
        "v_mfma_f32_16x16x32_bf16 v[16:19], v[112:115], v[128:131], v[16:19]\n"
        "v_add_u32 v177, 0x4000, v177\n"
        "v_mfma_f32_16x16x32_bf16 v[20:23], v[116:119], v[128:131], v[20:23]\n"
        "v_and_b32 v177, 0xffff, v177\n"
        "v_mfma_f32_16x16x32_bf16 v[32:35], v[112:115], v[132:135], v[32:35]\n"
        "v_mfma_f32_16x16x32_bf16 v[36:39], v[116:119], v[132:135], v[36:39]\n"
        "s_waitcnt lgkmcnt(0)\n"
        "s_barrier\n"
        ".Lk_hop5:\n"
        "s_cbranch_execz .Lk_ret0\n"
        "v_mfma_f32_16x16x32_bf16 v[24:27], v[120:123], v[128:131], v[24:27]\n"
        "ds_read_b128 v[80:83], v176\n"
        "v_mfma_f32_16x16x32_bf16 v[40:43], v[120:123], v[132:135], v[40:43]\n"
        "ds_read_b64_tr_b16 v[96:97], v178\n"
        "v_mfma_f32_16x16x32_bf16 v[48:51], v[112:115], v[136:139], v[48:51]\n"
        "ds_read_b64_tr_b16 v[98:99], v178 offset:1024\n"
        "v_mfma_f32_16x16x32_bf16 v[52:55], v[116:119], v[136:139], v[52:55]\n"
        "ds_read_b128 v[84:87], v176 offset:2048\n"
        "v_mfma_f32_16x16x32_bf16 v[56:59], v[120:123], v[136:139], v[56:59]\n"
        "ds_read_b64_tr_b16 v[100:101], v179\n"
        "v_mfma_f32_16x16x32_bf16 v[28:31], v[124:127], v[128:131], v[28:31]\n"
        "ds_read_b64_tr_b16 v[102:103], v179 offset:1024\n"
        "v_mfma_f32_16x16x32_bf16 v[44:47], v[124:127], v[132:135], v[44:47]\n"
        "ds_read_b128 v[88:91], v176 offset:4096\n"
        "v_mfma_f32_16x16x32_bf16 v[60:63], v[124:127], v[136:139], v[60:63]\n"
        "ds_read_b64_tr_b16 v[104:105], v180\n"
        "v_mfma_f32_16x16x32_bf16 v[64:67], v[112:115], v[140:143], v[64:67]\n"
        "ds_read_b64_tr_b16 v[106:107], v180 offset:1024\n"
        "v_mfma_f32_16x16x32_bf16 v[68:71], v[116:119], v[140:143], v[68:71]\n"
        "ds_read_b128 v[92:95], v176 offset:6144\n"
        "v_mfma_f32_16x16x32_bf16 v[72:75], v[120:123], v[140:143], v[72:75]\n"
        "ds_read_b64_tr_b16 v[108:109], v181\n"
        "v_mfma_f32_16x16x32_bf16 v[76:79], v[124:127], v[140:143], v[76:79]\n"
        "ds_read_b64_tr_b16 v[110:111], v181 offset:1024\n"
        "s_cbranch_scc1 2f\n"
        "s_waitcnt lgkmcnt(5)\n"
        "v_mfma_f32_16x16x32_bf16 v[16:19], v[80:83], v[96:99], v[16:19]\n"
        "ds_read_b128 v[112:115], v177\n"
        "v_mfma_f32_16x16x32_bf16 v[20:23], v[84:87], v[96:99], v[20:23]\n"
        "ds_read_b64_tr_b16 v[128:129], v178 offset:8192\n"
        "v_mfma_f32_16x16x32_bf16 v[32:35], v[80:83], v[100:103], v[32:35]\n"
        "ds_read_b64_tr_b16 v[130:131], v178 offset:9216\n"
        "v_mfma_f32_16x16x32_bf16 v[36:39], v[84:87], v[100:103], v[36:39]\n"
        "ds_read_b128 v[116:119], v177 offset:2048\n"
        "v_mfma_f32_16x16x32_bf16 v[24:27], v[88:91], v[96:99], v[24:27]\n"
        "ds_read_b64_tr_b16 v[132:133], v179 offset:8192\n"
        "v_mfma_f32_16x16x32_bf16 v[40:43], v[88:91], v[100:103], v[40:43]\n"
        "ds_read_b64_tr_b16 v[134:135], v179 offset:9216\n"
        "s_waitcnt lgkmcnt(6)\n"
        "v_mfma_f32_16x16x32_bf16 v[48:51], v[80:83], v[104:107], v[48:51]\n"
        "ds_read_b128 v[120:123], v177 offset:4096\n"
        "v_mfma_f32_16x16x32_bf16 v[52:55], v[84:87], v[104:107], v[52:55]\n"
        "ds_read_b64_tr_b16 v[136:137], v180 offset:8192\n"
        "v_mfma_f32_16x16x32_bf16 v[56:59], v[88:91], v[104:107], v[56:59]\n"
        "ds_read_b64_tr_b16 v[138:139], v180 offset:9216\n"
        "v_mfma_f32_16x16x32_bf16 v[28:31], v[92:95], v[96:99], v[28:31]\n"
        "ds_read_b128 v[124:127], v177 offset:6144\n"
        "v_mfma_f32_16x16x32_bf16 v[44:47], v[92:95], v[100:103], v[44:47]\n"
        "ds_read_b64_tr_b16 v[140:141], v181 offset:8192\n"
        "v_mfma_f32_16x16x32_bf16 v[60:63], v[92:95], v[104:107], v[60:63]\n"
        "ds_read_b64_tr_b16 v[142:143], v181 offset:9216\n"
        "v_mfma_f32_16x16x32_bf16 v[64:67], v[80:83], v[108:111], v[64:67]\n"
        "v_add_u32 v176, 0x4000, v176\n"
        "v_mfma_f32_16x16x32_bf16 v[68:71], v[84:87], v[108:111], v[68:71]\n"
        "v_and_b32 v176, 0xffff, v176\n"
        "v_mfma_f32_16x16x32_bf16 v[72:75], v[88:91], v[108:111], v[72:75]\n"
        "v_mfma_f32_16x16x32_bf16 v[76:79], v[92:95], v[108:111], v[76:79]\n"
        "s_waitcnt lgkmcnt(1)\n"
        "v_mfma_f32_16x16x32_bf16 v[16:19], v[112:115], v[128:131], v[16:19]\n"
        "v_add_u32 v177, 0x4000, v177\n"
        "v_mfma_f32_16x16x32_bf16 v[20:23], v[116:119], v[128:131], v[20:23]\n"
        "v_and_b32 v177, 0xffff, v177\n"
        "v_mfma_f32_16x16x32_bf16 v[32:35], v[112:115], v[132:135], v[32:35]\n"
        "v_mfma_f32_16x16x32_bf16 v[36:39], v[116:119], v[132:135], v[36:39]\n"
        "s_waitcnt lgkmcnt(0)\n"
        "s_barrier\n"
        "v_mfma_f32_16x16x32_bf16 v[24:27], v[120:123], v[128:131], v[24:27]\n"
        "ds_read_b128 v[80:83], v176\n"
        "v_mfma_f32_16x16x32_bf16 v[40:43], v[120:123], v[132:135], v[40:43]\n"
        "ds_read_b64_tr_b16 v[96:97], v178 offset:16384\n"
        "v_mfma_f32_16x16x32_bf16 v[48:51], v[112:115], v[136:139], v[48:51]\n"
        "ds_read_b64_tr_b16 v[98:99], v178 offset:17408\n"
        "v_mfma_f32_16x16x32_bf16 v[52:55], v[116:119], v[136:139], v[52:55]\n"
        "ds_read_b128 v[84:87], v176 offset:2048\n"
        "v_mfma_f32_16x16x32_bf16 v[56:59], v[120:123], v[136:139], v[56:59]\n"
        "ds_read_b64_tr_b16 v[100:101], v179 offset:16384\n"
        "v_mfma_f32_16x16x32_bf16 v[28:31], v[124:127], v[128:131], v[28:31]\n"
        "ds_read_b64_tr_b16 v[102:103], v179 offset:17408\n"
        "v_mfma_f32_16x16x32_bf16 v[44:47], v[124:127], v[132:135], v[44:47]\n"
        "ds_read_b128 v[88:91], v176 offset:4096\n"
        "v_mfma_f32_16x16x32_bf16 v[60:63], v[124:127], v[136:139], v[60:63]\n"
        "ds_read_b64_tr_b16 v[104:105], v180 offset:16384\n"
        "v_mfma_f32_16x16x32_bf16 v[64:67], v[112:115], v[140:143], v[64:67]\n"
        "ds_read_b64_tr_b16 v[106:107], v180 offset:17408\n"
        "v_mfma_f32_16x16x32_bf16 v[68:71], v[116:119], v[140:143], v[68:71]\n"
        "ds_read_b128 v[92:95], v176 offset:6144\n"
        "v_mfma_f32_16x16x32_bf16 v[72:75], v[120:123], v[140:143], v[72:75]\n"
        "ds_read_b64_tr_b16 v[108:109], v181 offset:16384\n"
        "v_mfma_f32_16x16x32_bf16 v[76:79], v[124:127], v[140:143], v[76:79]\n"
        "ds_read_b64_tr_b16 v[110:111], v181 offset:17408\n"
        "s_waitcnt lgkmcnt(5)\n"
        "v_mfma_f32_16x16x32_bf16 v[16:19], v[80:83], v[96:99], v[16:19]\n"
        "ds_read_b128 v[112:115], v177\n"
        "v_mfma_f32_16x16x32_bf16 v[20:23], v[84:87], v[96:99], v[20:23]\n"
        "ds_read_b64_tr_b16 v[128:129], v178 offset:24576\n"
        "v_mfma_f32_16x16x32_bf16 v[32:35], v[80:83], v[100:103], v[32:35]\n"
        "ds_read_b64_tr_b16 v[130:131], v178 offset:25600\n"
        "v_mfma_f32_16x16x32_bf16 v[36:39], v[84:87], v[100:103], v[36:39]\n"
        "ds_read_b128 v[116:119], v177 offset:2048\n"
        "v_mfma_f32_16x16x32_bf16 v[24:27], v[88:91], v[96:99], v[24:27]\n"
        "ds_read_b64_tr_b16 v[132:133], v179 offset:24576\n"
        "v_mfma_f32_16x16x32_bf16 v[40:43], v[88:91], v[100:103], v[40:43]\n"
        "ds_read_b64_tr_b16 v[134:135], v179 offset:25600\n"
        "s_waitcnt lgkmcnt(6)\n"
        "v_mfma_f32_16x16x32_bf16 v[48:51], v[80:83], v[104:107], v[48:51]\n"
        "ds_read_b128 v[120:123], v177 offset:4096\n"
        "v_mfma_f32_16x16x32_bf16 v[52:55], v[84:87], v[104:107], v[52:55]\n"
        "ds_read_b64_tr_b16 v[136:137], v180 offset:24576\n"
        "v_mfma_f32_16x16x32_bf16 v[56:59], v[88:91], v[104:107], v[56:59]\n"
        "ds_read_b64_tr_b16 v[138:139], v180 offset:25600\n"
        "v_mfma_f32_16x16x32_bf16 v[28:31], v[92:95], v[96:99], v[28:31]\n"
        "ds_read_b128 v[124:127], v177 offset:6144\n"
        "v_mfma_f32_16x16x32_bf16 v[44:47], v[92:95], v[100:103], v[44:47]\n"
        "ds_read_b64_tr_b16 v[140:141], v181 offset:24576\n"
        "v_mfma_f32_16x16x32_bf16 v[60:63], v[92:95], v[104:107], v[60:63]\n"
        "ds_read_b64_tr_b16 v[142:143], v181 offset:25600\n"
        "v_mfma_f32_16x16x32_bf16 v[64:67], v[80:83], v[108:111], v[64:67]\n"
        "v_add_u32 v176, 0x4000, v176\n"
        "v_mfma_f32_16x16x32_bf16 v[68:71], v[84:87], v[108:111], v[68:71]\n"
        "v_and_b32 v176, 0xffff, v176\n"
        "v_mfma_f32_16x16x32_bf16 v[72:75], v[88:91], v[108:111], v[72:75]\n"
        "v_mfma_f32_16x16x32_bf16 v[76:79], v[92:95], v[108:111], v[76:79]\n"
        "s_waitcnt lgkmcnt(1)\n"
        "v_mfma_f32_16x16x32_bf16 v[16:19], v[112:115], v[128:131], v[16:19]\n"
        "v_add_u32 v177, 0x4000, v177\n"
        "v_mfma_f32_16x16x32_bf16 v[20:23], v[116:119], v[128:131], v[20:23]\n"
        "v_and_b32 v177, 0xffff, v177\n"
        "v_mfma_f32_16x16x32_bf16 v[32:35], v[112:115], v[132:135], v[32:35]\n"
        "v_mfma_f32_16x16x32_bf16 v[36:39], v[116:119], v[132:135], v[36:39]\n"
        "s_waitcnt lgkmcnt(0)\n"
        "s_barrier\n"
        "v_mfma_f32_16x16x32_bf16 v[24:27], v[120:123], v[128:131], v[24:27]\n"
        "ds_read_b128 v[80:83], v176\n"
        "v_mfma_f32_16x16x32_bf16 v[40:43], v[120:123], v[132:135], v[40:43]\n"
        "ds_read_b64_tr_b16 v[96:97], v178 offset:32768\n"
        "v_mfma_f32_16x16x32_bf16 v[48:51], v[112:115], v[136:139], v[48:51]\n"
        "ds_read_b64_tr_b16 v[98:99], v178 offset:33792\n"
        "v_mfma_f32_16x16x32_bf16 v[52:55], v[116:119], v[136:139], v[52:55]\n"
        "ds_read_b128 v[84:87], v176 offset:2048\n"
        "v_mfma_f32_16x16x32_bf16 v[56:59], v[120:123], v[136:139], v[56:59]\n"
        "ds_read_b64_tr_b16 v[100:101], v179 offset:32768\n"
        "v_mfma_f32_16x16x32_bf16 v[28:31], v[124:127], v[128:131], v[28:31]\n"
        "ds_read_b64_tr_b16 v[102:103], v179 offset:33792\n"
        "v_mfma_f32_16x16x32_bf16 v[44:47], v[124:127], v[132:135], v[44:47]\n"
        "ds_read_b128 v[88:91], v176 offset:4096\n"
        "v_mfma_f32_16x16x32_bf16 v[60:63], v[124:127], v[136:139], v[60:63]\n"
        "ds_read_b64_tr_b16 v[104:105], v180 offset:32768\n"
        "v_mfma_f32_16x16x32_bf16 v[64:67], v[112:115], v[140:143], v[64:67]\n"
        "ds_read_b64_tr_b16 v[106:107], v180 offset:33792\n"
        "v_mfma_f32_16x16x32_bf16 v[68:71], v[116:119], v[140:143], v[68:71]\n"
        "ds_read_b128 v[92:95], v176 offset:6144\n"
        "v_mfma_f32_16x16x32_bf16 v[72:75], v[120:123], v[140:143], v[72:75]\n"
        "ds_read_b64_tr_b16 v[108:109], v181 offset:32768\n"
        "v_mfma_f32_16x16x32_bf16 v[76:79], v[124:127], v[140:143], v[76:79]\n"
        "ds_read_b64_tr_b16 v[110:111], v181 offset:33792\n"
        "s_add_u32 s40, s40, 1\n"
        "s_branch 1b\n"
        "2:\n"
        "s_branch 41f\n"
        "40:\n"
        "s_lshr_b32 s40, %[b], 6\n"
        "s_mul_i32 s40, s40, 4\n"
        "s_and_b32 s41, %[b], 7\n"
        "s_lshr_b32 s42, s41, 1\n"
        "s_add_u32 s42, s40, s42\n"
        "s_mov_b32 s40, 0x29a080\n"
        "s_cmp_lt_u32 s42, 5\n"
        "s_cselect_b32 s40, 0x1a080, s40\n"
        "s_lshr_b32 s43, %[b], 3\n"
        "s_and_b32 s43, s43, 7\n"
        "s_mul_i32 s43, s43, 2\n"
        "s_and_b32 s41, s41, 1\n"
        "s_add_u32 s41, s43, s41\n"
        "s_mul_i32 s41, s41, 4\n"
        "s_lshr_b32 s43, %[w], 1\n"
        "s_mul_i32 s43, s43, 2\n"
        "s_add_u32 s43, s41, s43\n"
        "s_mov_b32 s41, 0x14000\n"
        "s_cmp_lt_u32 s42, 5\n"
        "s_cselect_b32 s41, 0xa000, s41\n"
        "s_mul_i32 s41, s43, s41\n"
        "s_add_u32 s41, s40, s41\n"
        "s_mul_i32 s42, s42, 0x2000\n"
        "s_add_u32 s42, s41, s42\n"
        "s_and_b32 s41, %[w], 1\n"
        "s_mul_i32 s41, s41, 0x1000\n"
        "s_add_u32 s41, s42, s41\n"
        "v_lshl_add_u32 v182, %[ln], 4, s41\n"
        "v_add_u32 v183, 1024, v182\n"
        "v_add_u32 v184, 2048, v182\n"
        "v_add_u32 v185, 3072, v182\n"
        "s_waitcnt lgkmcnt(0)\n"
        "s_barrier\n"
        "ds_read_b128 v[80:83], v176\n"
        "ds_read_b64_tr_b16 v[96:97], v178 offset:32768\n"
        "ds_read_b64_tr_b16 v[98:99], v178 offset:33792\n"
        "ds_read_b128 v[84:87], v176 offset:2048\n"
        "ds_read_b64_tr_b16 v[100:101], v179 offset:32768\n"
        "ds_read_b64_tr_b16 v[102:103], v179 offset:33792\n"
        "ds_read_b128 v[88:91], v176 offset:4096\n"
        "ds_read_b64_tr_b16 v[104:105], v180 offset:32768\n"
        "ds_read_b64_tr_b16 v[106:107], v180 offset:33792\n"
        "ds_read_b128 v[92:95], v176 offset:6144\n"
        "ds_read_b64_tr_b16 v[108:109], v181 offset:32768\n"
        "ds_read_b64_tr_b16 v[110:111], v181 offset:33792\n"
        "s_mov_b32 s40, 0\n"
        "s_cmp_eq_u32 s40, 1\n"
        "s_waitcnt lgkmcnt(5)\n"
        "v_mfma_f32_16x16x32_bf16 v[16:19], v[96:99], v[80:83], 0\n"
        "ds_read_b128 v[112:115], v177\n"
        "v_mfma_f32_16x16x32_bf16 v[20:23], v[96:99], v[84:87], 0\n"
        "ds_read_b64_tr_b16 v[128:129], v178 offset:40960\n"
        "v_mfma_f32_16x16x32_bf16 v[32:35], v[100:103], v[80:83], 0\n"
        "ds_read_b64_tr_b16 v[130:131], v178 offset:41984\n"
        "v_mfma_f32_16x16x32_bf16 v[36:39], v[100:103], v[84:87], 0\n"
        "ds_read_b128 v[116:119], v177 offset:2048\n"
        "v_mfma_f32_16x16x32_bf16 v[24:27], v[96:99], v[88:91], 0\n"
        "ds_read_b64_tr_b16 v[132:133], v179 offset:40960\n"
        "v_mfma_f32_16x16x32_bf16 v[40:43], v[100:103], v[88:91], 0\n"
        "ds_read_b64_tr_b16 v[134:135], v179 offset:41984\n"
        "s_waitcnt lgkmcnt(6)\n"
        "v_mfma_f32_16x16x32_bf16 v[48:51], v[104:107], v[80:83], 0\n"
        "ds_read_b128 v[120:123], v177 offset:4096\n"
        "v_mfma_f32_16x16x32_bf16 v[52:55], v[104:107], v[84:87], 0\n"
        "ds_read_b64_tr_b16 v[136:137], v180 offset:40960\n"
        "v_mfma_f32_16x16x32_bf16 v[56:59], v[104:107], v[88:91], 0\n"
        "ds_read_b64_tr_b16 v[138:139], v180 offset:41984\n"
        "v_mfma_f32_16x16x32_bf16 v[28:31], v[96:99], v[92:95], 0\n"
        "ds_read_b128 v[124:127], v177 offset:6144\n"
        "v_mfma_f32_16x16x32_bf16 v[44:47], v[100:103], v[92:95], 0\n"
        "ds_read_b64_tr_b16 v[140:141], v181 offset:40960\n"
        "v_mfma_f32_16x16x32_bf16 v[60:63], v[104:107], v[92:95], 0\n"
        "ds_read_b64_tr_b16 v[142:143], v181 offset:41984\n"
        "v_mfma_f32_16x16x32_bf16 v[64:67], v[108:111], v[80:83], 0\n"
        "v_add_u32 v176, 0x4000, v176\n"
        "v_mfma_f32_16x16x32_bf16 v[68:71], v[108:111], v[84:87], 0\n"
        "v_and_b32 v176, 0xffff, v176\n"
        "v_mfma_f32_16x16x32_bf16 v[72:75], v[108:111], v[88:91], 0\n"
        "v_mfma_f32_16x16x32_bf16 v[76:79], v[108:111], v[92:95], 0\n"
        "s_branch 4f\n"
        "1:\n"
        "s_cmp_eq_u32 s40, 3\n"
        "s_waitcnt lgkmcnt(5)\n"
        "v_mfma_f32_16x16x32_bf16 v[16:19], v[96:99], v[80:83], v[16:19]\n"
        "ds_read_b128 v[112:115], v177\n"
        "v_mfma_f32_16x16x32_bf16 v[20:23], v[96:99], v[84:87], v[20:23]\n"
        "ds_read_b64_tr_b16 v[128:129], v178 offset:40960\n"
        "v_mfma_f32_16x16x32_bf16 v[32:35], v[100:103], v[80:83], v[32:35]\n"
        "ds_read_b64_tr_b16 v[130:131], v178 offset:41984\n"
        "v_mfma_f32_16x16x32_bf16 v[36:39], v[100:103], v[84:87], v[36:39]\n"
        "ds_read_b128 v[116:119], v177 offset:2048\n"
        "v_mfma_f32_16x16x32_bf16 v[24:27], v[96:99], v[88:91], v[24:27]\n"
        "ds_read_b64_tr_b16 v[132:133], v179 offset:40960\n"
        "v_mfma_f32_16x16x32_bf16 v[40:43], v[100:103], v[88:91], v[40:43]\n"
        "ds_read_b64_tr_b16 v[134:135], v179 offset:41984\n"
        "s_waitcnt lgkmcnt(6)\n"
        "v_mfma_f32_16x16x32_bf16 v[48:51], v[104:107], v[80:83], v[48:51]\n"
        "ds_read_b128 v[120:123], v177 offset:4096\n"
        "v_mfma_f32_16x16x32_bf16 v[52:55], v[104:107], v[84:87], v[52:55]\n"
        "ds_read_b64_tr_b16 v[136:137], v180 offset:40960\n"
        "v_mfma_f32_16x16x32_bf16 v[56:59], v[104:107], v[88:91], v[56:59]\n"
        "ds_read_b64_tr_b16 v[138:139], v180 offset:41984\n"
        "v_mfma_f32_16x16x32_bf16 v[28:31], v[96:99], v[92:95], v[28:31]\n"
        "ds_read_b128 v[124:127], v177 offset:6144\n"
        "v_mfma_f32_16x16x32_bf16 v[44:47], v[100:103], v[92:95], v[44:47]\n"
        "ds_read_b64_tr_b16 v[140:141], v181 offset:40960\n"
        "v_mfma_f32_16x16x32_bf16 v[60:63], v[104:107], v[92:95], v[60:63]\n"
        "ds_read_b64_tr_b16 v[142:143], v181 offset:41984\n"
        "v_mfma_f32_16x16x32_bf16 v[64:67], v[108:111], v[80:83], v[64:67]\n"
        "v_add_u32 v176, 0x4000, v176\n"
        "v_mfma_f32_16x16x32_bf16 v[68:71], v[108:111], v[84:87], v[68:71]\n"
        "v_and_b32 v176, 0xffff, v176\n"
        "v_mfma_f32_16x16x32_bf16 v[72:75], v[108:111], v[88:91], v[72:75]\n"
        "v_mfma_f32_16x16x32_bf16 v[76:79], v[108:111], v[92:95], v[76:79]\n"
        "4:\n"
        "s_waitcnt lgkmcnt(1)\n"
        "v_mfma_f32_16x16x32_bf16 v[16:19], v[128:131], v[112:115], v[16:19]\n"
        "v_add_u32 v177, 0x4000, v177\n"
        "v_mfma_f32_16x16x32_bf16 v[20:23], v[128:131], v[116:119], v[20:23]\n"
        "v_and_b32 v177, 0xffff, v177\n"
        "v_mfma_f32_16x16x32_bf16 v[32:35], v[132:135], v[112:115], v[32:35]\n"
        "v_mfma_f32_16x16x32_bf16 v[36:39], v[132:135], v[116:119], v[36:39]\n"
        "s_waitcnt lgkmcnt(0)\n"
        "s_barrier\n"
        "v_mfma_f32_16x16x32_bf16 v[24:27], v[128:131], v[120:123], v[24:27]\n"
        "ds_read_b128 v[80:83], v176\n"
        "v_mfma_f32_16x16x32_bf16 v[40:43], v[132:135], v[120:123], v[40:43]\n"
        "ds_read_b64_tr_b16 v[96:97], v178\n"
        "v_mfma_f32_16x16x32_bf16 v[48:51], v[136:139], v[112:115], v[48:51]\n"
        "ds_read_b64_tr_b16 v[98:99], v178 offset:1024\n"
        "v_mfma_f32_16x16x32_bf16 v[52:55], v[136:139], v[116:119], v[52:55]\n"
        "ds_read_b128 v[84:87], v176 offset:2048\n"
        "v_mfma_f32_16x16x32_bf16 v[56:59], v[136:139], v[120:123], v[56:59]\n"
        "ds_read_b64_tr_b16 v[100:101], v179\n"
        "v_mfma_f32_16x16x32_bf16 v[28:31], v[128:131], v[124:127], v[28:31]\n"
        "ds_read_b64_tr_b16 v[102:103], v179 offset:1024\n"
        "v_mfma_f32_16x16x32_bf16 v[44:47], v[132:135], v[124:127], v[44:47]\n"
        "ds_read_b128 v[88:91], v176 offset:4096\n"
        "v_mfma_f32_16x16x32_bf16 v[60:63], v[136:139], v[124:127], v[60:63]\n"
        "ds_read_b64_tr_b16 v[104:105], v180\n"
        "v_mfma_f32_16x16x32_bf16 v[64:67], v[140:143], v[112:115], v[64:67]\n"
        "ds_read_b64_tr_b16 v[106:107], v180 offset:1024\n"
        "v_mfma_f32_16x16x32_bf16 v[68:71], v[140:143], v[116:119], v[68:71]\n"
        "ds_read_b128 v[92:95], v176 offset:6144\n"
        "v_mfma_f32_16x16x32_bf16 v[72:75], v[140:143], v[120:123], v[72:75]\n"
        "ds_read_b64_tr_b16 v[108:109], v181\n"
        "v_mfma_f32_16x16x32_bf16 v[76:79], v[140:143], v[124:127], v[76:79]\n"
        "ds_read_b64_tr_b16 v[110:111], v181 offset:1024\n"
        "s_cbranch_scc1 2f\n"
        "s_waitcnt lgkmcnt(5)\n"
        "v_mfma_f32_16x16x32_bf16 v[16:19], v[96:99], v[80:83], v[16:19]\n"
        "ds_read_b128 v[112:115], v177\n"
        "v_mfma_f32_16x16x32_bf16 v[20:23], v[96:99], v[84:87], v[20:23]\n"
        "ds_read_b64_tr_b16 v[128:129], v178 offset:8192\n"
        "v_mfma_f32_16x16x32_bf16 v[32:35], v[100:103], v[80:83], v[32:35]\n"
        "ds_read_b64_tr_b16 v[130:131], v178 offset:9216\n"
        "v_mfma_f32_16x16x32_bf16 v[36:39], v[100:103], v[84:87], v[36:39]\n"
        "ds_read_b128 v[116:119], v177 offset:2048\n"
        "v_mfma_f32_16x16x32_bf16 v[24:27], v[96:99], v[88:91], v[24:27]\n"
        "ds_read_b64_tr_b16 v[132:133], v179 offset:8192\n"
        "v_mfma_f32_16x16x32_bf16 v[40:43], v[100:103], v[88:91], v[40:43]\n"
        "ds_read_b64_tr_b16 v[134:135], v179 offset:9216\n"
        "s_waitcnt lgkmcnt(6)\n"
        "v_mfma_f32_16x16x32_bf16 v[48:51], v[104:107], v[80:83], v[48:51]\n"
        "ds_read_b128 v[120:123], v177 offset:4096\n"
        "v_mfma_f32_16x16x32_bf16 v[52:55], v[104:107], v[84:87], v[52:55]\n"
        "ds_read_b64_tr_b16 v[136:137], v180 offset:8192\n"
        "v_mfma_f32_16x16x32_bf16 v[56:59], v[104:107], v[88:91], v[56:59]\n"
        "ds_read_b64_tr_b16 v[138:139], v180 offset:9216\n"
        "v_mfma_f32_16x16x32_bf16 v[28:31], v[96:99], v[92:95], v[28:31]\n"
        "ds_read_b128 v[124:127], v177 offset:6144\n"
        "v_mfma_f32_16x16x32_bf16 v[44:47], v[100:103], v[92:95], v[44:47]\n"
        "ds_read_b64_tr_b16 v[140:141], v181 offset:8192\n"
        "v_mfma_f32_16x16x32_bf16 v[60:63], v[104:107], v[92:95], v[60:63]\n"
        "ds_read_b64_tr_b16 v[142:143], v181 offset:9216\n"
        "v_mfma_f32_16x16x32_bf16 v[64:67], v[108:111], v[80:83], v[64:67]\n"
        "v_add_u32 v176, 0x4000, v176\n"
        "v_mfma_f32_16x16x32_bf16 v[68:71], v[108:111], v[84:87], v[68:71]\n"
        "v_and_b32 v176, 0xffff, v176\n"
        "v_mfma_f32_16x16x32_bf16 v[72:75], v[108:111], v[88:91], v[72:75]\n"
        "v_mfma_f32_16x16x32_bf16 v[76:79], v[108:111], v[92:95], v[76:79]\n"
        "s_waitcnt lgkmcnt(1)\n"
        "v_mfma_f32_16x16x32_bf16 v[16:19], v[128:131], v[112:115], v[16:19]\n"
        "v_add_u32 v177, 0x4000, v177\n"
        "v_mfma_f32_16x16x32_bf16 v[20:23], v[128:131], v[116:119], v[20:23]\n"
        "v_and_b32 v177, 0xffff, v177\n"
        "v_mfma_f32_16x16x32_bf16 v[32:35], v[132:135], v[112:115], v[32:35]\n"
        "v_mfma_f32_16x16x32_bf16 v[36:39], v[132:135], v[116:119], v[36:39]\n"
        "s_waitcnt lgkmcnt(0)\n"
        "s_barrier\n"
        "v_mfma_f32_16x16x32_bf16 v[24:27], v[128:131], v[120:123], v[24:27]\n"
        "ds_read_b128 v[80:83], v176\n"
        "v_mfma_f32_16x16x32_bf16 v[40:43], v[132:135], v[120:123], v[40:43]\n"
        "ds_read_b64_tr_b16 v[96:97], v178 offset:16384\n"
        "v_mfma_f32_16x16x32_bf16 v[48:51], v[136:139], v[112:115], v[48:51]\n"
        "ds_read_b64_tr_b16 v[98:99], v178 offset:17408\n"
        "v_mfma_f32_16x16x32_bf16 v[52:55], v[136:139], v[116:119], v[52:55]\n"
        "ds_read_b128 v[84:87], v176 offset:2048\n"
        "v_mfma_f32_16x16x32_bf16 v[56:59], v[136:139], v[120:123], v[56:59]\n"
        "ds_read_b64_tr_b16 v[100:101], v179 offset:16384\n"
        "v_mfma_f32_16x16x32_bf16 v[28:31], v[128:131], v[124:127], v[28:31]\n"
        "ds_read_b64_tr_b16 v[102:103], v179 offset:17408\n"
        "v_mfma_f32_16x16x32_bf16 v[44:47], v[132:135], v[124:127], v[44:47]\n"
        "ds_read_b128 v[88:91], v176 offset:4096\n"
        "v_mfma_f32_16x16x32_bf16 v[60:63], v[136:139], v[124:127], v[60:63]\n"
        "ds_read_b64_tr_b16 v[104:105], v180 offset:16384\n"
        "v_mfma_f32_16x16x32_bf16 v[64:67], v[140:143], v[112:115], v[64:67]\n"
        "ds_read_b64_tr_b16 v[106:107], v180 offset:17408\n"
        "v_mfma_f32_16x16x32_bf16 v[68:71], v[140:143], v[116:119], v[68:71]\n"
        "ds_read_b128 v[92:95], v176 offset:6144\n"
        "v_mfma_f32_16x16x32_bf16 v[72:75], v[140:143], v[120:123], v[72:75]\n"
        "ds_read_b64_tr_b16 v[108:109], v181 offset:16384\n"
        "v_mfma_f32_16x16x32_bf16 v[76:79], v[140:143], v[124:127], v[76:79]\n"
        "ds_read_b64_tr_b16 v[110:111], v181 offset:17408\n"
        "s_waitcnt lgkmcnt(5)\n"
        "v_mfma_f32_16x16x32_bf16 v[16:19], v[96:99], v[80:83], v[16:19]\n"
        "ds_read_b128 v[112:115], v177\n"
        "v_mfma_f32_16x16x32_bf16 v[20:23], v[96:99], v[84:87], v[20:23]\n"
        "ds_read_b64_tr_b16 v[128:129], v178 offset:24576\n"
        "v_mfma_f32_16x16x32_bf16 v[32:35], v[100:103], v[80:83], v[32:35]\n"
        "ds_read_b64_tr_b16 v[130:131], v178 offset:25600\n"
        "v_mfma_f32_16x16x32_bf16 v[36:39], v[100:103], v[84:87], v[36:39]\n"
        "ds_read_b128 v[116:119], v177 offset:2048\n"
        "v_mfma_f32_16x16x32_bf16 v[24:27], v[96:99], v[88:91], v[24:27]\n"
        "ds_read_b64_tr_b16 v[132:133], v179 offset:24576\n"
        "v_mfma_f32_16x16x32_bf16 v[40:43], v[100:103], v[88:91], v[40:43]\n"
        "ds_read_b64_tr_b16 v[134:135], v179 offset:25600\n"
        "s_waitcnt lgkmcnt(6)\n"
        "v_mfma_f32_16x16x32_bf16 v[48:51], v[104:107], v[80:83], v[48:51]\n"
        "ds_read_b128 v[120:123], v177 offset:4096\n"
        "v_mfma_f32_16x16x32_bf16 v[52:55], v[104:107], v[84:87], v[52:55]\n"
        "ds_read_b64_tr_b16 v[136:137], v180 offset:24576\n"
        "v_mfma_f32_16x16x32_bf16 v[56:59], v[104:107], v[88:91], v[56:59]\n"
        "ds_read_b64_tr_b16 v[138:139], v180 offset:25600\n"
        "v_mfma_f32_16x16x32_bf16 v[28:31], v[96:99], v[92:95], v[28:31]\n"
        "ds_read_b128 v[124:127], v177 offset:6144\n"
        "v_mfma_f32_16x16x32_bf16 v[44:47], v[100:103], v[92:95], v[44:47]\n"
        "ds_read_b64_tr_b16 v[140:141], v181 offset:24576\n"
        "v_mfma_f32_16x16x32_bf16 v[60:63], v[104:107], v[92:95], v[60:63]\n"
        "ds_read_b64_tr_b16 v[142:143], v181 offset:25600\n"
        "v_mfma_f32_16x16x32_bf16 v[64:67], v[108:111], v[80:83], v[64:67]\n"
        "v_add_u32 v176, 0x4000, v176\n"
        "v_mfma_f32_16x16x32_bf16 v[68:71], v[108:111], v[84:87], v[68:71]\n"
        "v_and_b32 v176, 0xffff, v176\n"
        "v_mfma_f32_16x16x32_bf16 v[72:75], v[108:111], v[88:91], v[72:75]\n"
        "v_mfma_f32_16x16x32_bf16 v[76:79], v[108:111], v[92:95], v[76:79]\n"
        "s_waitcnt lgkmcnt(1)\n"
        "v_mfma_f32_16x16x32_bf16 v[16:19], v[128:131], v[112:115], v[16:19]\n"
        "v_add_u32 v177, 0x4000, v177\n"
        "v_mfma_f32_16x16x32_bf16 v[20:23], v[128:131], v[116:119], v[20:23]\n"
        "v_and_b32 v177, 0xffff, v177\n"
        "v_mfma_f32_16x16x32_bf16 v[32:35], v[132:135], v[112:115], v[32:35]\n"
        "v_mfma_f32_16x16x32_bf16 v[36:39], v[132:135], v[116:119], v[36:39]\n"
        "s_waitcnt lgkmcnt(0)\n"
        "s_barrier\n"
        "v_mfma_f32_16x16x32_bf16 v[24:27], v[128:131], v[120:123], v[24:27]\n"
        "ds_read_b128 v[80:83], v176\n"
        "v_mfma_f32_16x16x32_bf16 v[40:43], v[132:135], v[120:123], v[40:43]\n"
        "ds_read_b64_tr_b16 v[96:97], v178 offset:32768\n"
        "v_mfma_f32_16x16x32_bf16 v[48:51], v[136:139], v[112:115], v[48:51]\n"
        "ds_read_b64_tr_b16 v[98:99], v178 offset:33792\n"
        "v_mfma_f32_16x16x32_bf16 v[52:55], v[136:139], v[116:119], v[52:55]\n"
        "ds_read_b128 v[84:87], v176 offset:2048\n"
        "v_mfma_f32_16x16x32_bf16 v[56:59], v[136:139], v[120:123], v[56:59]\n"
        "ds_read_b64_tr_b16 v[100:101], v179 offset:32768\n"
        "v_mfma_f32_16x16x32_bf16 v[28:31], v[128:131], v[124:127], v[28:31]\n"
        "ds_read_b64_tr_b16 v[102:103], v179 offset:33792\n"
        "v_mfma_f32_16x16x32_bf16 v[44:47], v[132:135], v[124:127], v[44:47]\n"
        "ds_read_b128 v[88:91], v176 offset:4096\n"
        "v_mfma_f32_16x16x32_bf16 v[60:63], v[136:139], v[124:127], v[60:63]\n"
        "ds_read_b64_tr_b16 v[104:105], v180 offset:32768\n"
        "v_mfma_f32_16x16x32_bf16 v[64:67], v[140:143], v[112:115], v[64:67]\n"
        "ds_read_b64_tr_b16 v[106:107], v180 offset:33792\n"
        "v_mfma_f32_16x16x32_bf16 v[68:71], v[140:143], v[116:119], v[68:71]\n"
        "ds_read_b128 v[92:95], v176 offset:6144\n"
        "v_mfma_f32_16x16x32_bf16 v[72:75], v[140:143], v[120:123], v[72:75]\n"
        "ds_read_b64_tr_b16 v[108:109], v181 offset:32768\n"
        "v_mfma_f32_16x16x32_bf16 v[76:79], v[140:143], v[124:127], v[76:79]\n"
        "ds_read_b64_tr_b16 v[110:111], v181 offset:33792\n"
        "s_add_u32 s40, s40, 1\n"
        "s_branch 1b\n"
        "2:\n"
        "41:\n"
        "s_nop 7\n"
        "s_cmp_lg_u32 s72, 0\n"
        "s_cbranch_scc0 6f\n"
        "v_cvt_pk_bf16_f32 v112, v16, v17\n"
        "v_cvt_pk_bf16_f32 v113, v18, v19\n"
        "v_cvt_pk_bf16_f32 v114, v20, v21\n"
        "v_cvt_pk_bf16_f32 v115, v22, v23\n"
        "v_cvt_pk_bf16_f32 v116, v24, v25\n"
        "v_cvt_pk_bf16_f32 v117, v26, v27\n"
        "v_cvt_pk_bf16_f32 v118, v28, v29\n"
        "v_cvt_pk_bf16_f32 v119, v30, v31\n"
        "v_permlane16_swap_b32 v112, v114\n"
        "v_permlane16_swap_b32 v113, v115\n"
        "v_cvt_pk_bf16_f32 v120, v32, v33\n"
        "v_cvt_pk_bf16_f32 v121, v34, v35\n"
        "v_cvt_pk_bf16_f32 v122, v36, v37\n"
        "v_cvt_pk_bf16_f32 v123, v38, v39\n"
        "v_permlane16_swap_b32 v116, v118\n"
        "v_permlane16_swap_b32 v117, v119\n"
        "v_cvt_pk_bf16_f32 v124, v40, v41\n"
        "v_cvt_pk_bf16_f32 v125, v42, v43\n"
        "v_cvt_pk_bf16_f32 v126, v44, v45\n"
        "v_cvt_pk_bf16_f32 v127, v46, v47\n"
        "v_permlane16_swap_b32 v120, v122\n"
        "v_permlane16_swap_b32 v121, v123\n"
        "v_cvt_pk_bf16_f32 v128, v48, v49\n"
        "v_cvt_pk_bf16_f32 v129, v50, v51\n"
        "v_cvt_pk_bf16_f32 v130, v52, v53\n"
        "v_cvt_pk_bf16_f32 v131, v54, v55\n"
        "v_permlane16_swap_b32 v124, v126\n"
        "v_permlane16_swap_b32 v125, v127\n"
        "v_cvt_pk_bf16_f32 v132, v56, v57\n"
        "v_cvt_pk_bf16_f32 v133, v58, v59\n"
        "v_cvt_pk_bf16_f32 v134, v60, v61\n"
        "v_cvt_pk_bf16_f32 v135, v62, v63\n"
        "v_permlane16_swap_b32 v128, v130\n"
        "v_permlane16_swap_b32 v129, v131\n"
        "v_cvt_pk_bf16_f32 v136, v64, v65\n"
        "v_cvt_pk_bf16_f32 v137, v66, v67\n"
        "v_cvt_pk_bf16_f32 v138, v68, v69\n"
        "v_cvt_pk_bf16_f32 v139, v70, v71\n"
        "v_permlane16_swap_b32 v132, v134\n"
        "v_permlane16_swap_b32 v133, v135\n"
        "v_cvt_pk_bf16_f32 v140, v72, v73\n"
        "v_cvt_pk_bf16_f32 v141, v74, v75\n"
        "v_cvt_pk_bf16_f32 v142, v76, v77\n"
        "v_cvt_pk_bf16_f32 v143, v78, v79\n"
        "v_permlane16_swap_b32 v136, v138\n"
        "v_permlane16_swap_b32 v137, v139\n"
        "s_nop 1\n"
        "v_permlane16_swap_b32 v140, v142\n"
        "v_permlane16_swap_b32 v141, v143\n"
        "s_waitcnt lgkmcnt(0)\n"
        "s_barrier\n"
        "s_branch 43f\n"
        "42:\n"
        "v_and_b32 v120, 48, %[ln]\n"
        "global_load_dwordx4 v[16:19], v120, s[24:25]\n"
        "global_load_dwordx4 v[20:23], v120, s[24:25] offset:64\n"
        "global_load_dwordx4 v[24:27], v120, s[24:25] offset:128\n"
        "global_load_dwordx4 v[28:31], v120, s[24:25] offset:192\n"
        "global_load_dwordx4 v[32:35], v120, s[24:25] offset:256\n"
        "global_load_dwordx4 v[36:39], v120, s[24:25] offset:320\n"
        "global_load_dwordx4 v[40:43], v120, s[24:25] offset:384\n"
        "global_load_dwordx4 v[44:47], v120, s[24:25] offset:448\n"
        "global_load_dwordx4 v[48:51], v120, s[24:25] offset:512\n"
        "global_load_dwordx4 v[52:55], v120, s[24:25] offset:576\n"
        "global_load_dwordx4 v[56:59], v120, s[24:25] offset:640\n"
        "global_load_dwordx4 v[60:63], v120, s[24:25] offset:704\n"
        "global_load_dwordx4 v[64:67], v120, s[24:25] offset:768\n"
        "global_load_dwordx4 v[68:71], v120, s[24:25] offset:832\n"
        "global_load_dwordx4 v[72:75], v120, s[24:25] offset:896\n"
        "global_load_dwordx4 v[76:79], v120, s[24:25] offset:960\n"
        "global_load_dwordx4 v[80:83], v120, s[24:25] offset:1024\n"
        "global_load_dwordx4 v[84:87], v120, s[24:25] offset:1088\n"
        "global_load_dwordx4 v[88:91], v120, s[24:25] offset:1152\n"
        "global_load_dwordx4 v[92:95], v120, s[24:25] offset:1216\n"
        "s_mul_i32 s40, %[w], 0x1000\n"
        "v_and_b32 v124, 15, %[ln]\n"
        "v_lshrrev_b32 v125, 4, %[ln]\n"
        "v_and_b32 v126, 7, %[ln]\n"
        "v_xor_b32 v126, v126, v125\n"
        "v_lshl_add_u32 v176, v124, 7, s40\n"
        "v_lshl_add_u32 v176, v126, 4, v176\n"
        "v_xor_b32 v177, 64, v176\n"
        "s_lshr_b32 s40, %[b], 3\n"
        "s_and_b32 s40, s40, 7\n"
        "s_mul_i32 s40, s40, 2\n"
        "s_and_b32 s41, %[b], 7\n"
        "s_and_b32 s41, s41, 1\n"
        "s_add_u32 s41, s40, s41\n"
        "s_mul_i32 s41, s41, 0x80\n"
        "s_mul_i32 s41, s41, 4\n"
        "s_mul_i32 s40, %[w], 32\n"
        "s_mul_i32 s40, s40, 4\n"
        "s_add_u32 s40, s41, s40\n"
        "s_add_u32 s40, s40, 0x18080\n"
        "v_lshl_add_u32 v126, %[ln], 2, s40\n"
        "v_mov_b32 v124, 0x7f000000\n"
        "v_cmp_gt_u32 vcc, 32, %[ln]\n"
        "s_nop 1\n"
        "v_cndmask_b32 v182, v124, v126, vcc\n"
        "s_barrier\n"
        "ds_read_b128 v[96:99], v176\n"
        "ds_read_b128 v[100:103], v176 offset:2048\n"
        "ds_read_b128 v[104:107], v177\n"
        "ds_read_b128 v[108:111], v177 offset:2048\n"
        "s_waitcnt vmcnt(0) lgkmcnt(0)\n"
        "v_mfma_f32_16x16x32_bf16 v[112:115], v[16:19], v[96:99], 0\n"
        "v_mfma_f32_16x16x32_bf16 v[116:119], v[16:19], v[100:103], 0\n"
        "v_mfma_f32_16x16x32_bf16 v[112:115], v[20:23], v[104:107], v[112:115]\n"
        "v_mfma_f32_16x16x32_bf16 v[116:119], v[20:23], v[108:111], v[116:119]\n"
        "s_barrier\n"
        "ds_read_b128 v[96:99], v176 offset:16384\n"
        "ds_read_b128 v[100:103], v176 offset:18432\n"
        "ds_read_b128 v[104:107], v177 offset:16384\n"
        "ds_read_b128 v[108:111], v177 offset:18432\n"
        "s_waitcnt vmcnt(0) lgkmcnt(0)\n"
        "v_mfma_f32_16x16x32_bf16 v[112:115], v[24:27], v[96:99], v[112:115]\n"
        "v_mfma_f32_16x16x32_bf16 v[116:119], v[24:27], v[100:103], v[116:119]\n"
        "v_mfma_f32_16x16x32_bf16 v[112:115], v[28:31], v[104:107], v[112:115]\n"
        "v_mfma_f32_16x16x32_bf16 v[116:119], v[28:31], v[108:111], v[116:119]\n"
        "s_barrier\n"
        "ds_read_b128 v[96:99], v176 offset:32768\n"
        "ds_read_b128 v[100:103], v176 offset:34816\n"
        "ds_read_b128 v[104:107], v177 offset:32768\n"
        "ds_read_b128 v[108:111], v177 offset:34816\n"
        "s_waitcnt vmcnt(0) lgkmcnt(0)\n"
        "v_mfma_f32_16x16x32_bf16 v[112:115], v[32:35], v[96:99], v[112:115]\n"
        "v_mfma_f32_16x16x32_bf16 v[116:119], v[32:35], v[100:103], v[116:119]\n"
        ".Lk_hop6:\n"
        "s_cbranch_execz .Lk_ret0\n"
        "v_mfma_f32_16x16x32_bf16 v[112:115], v[36:39], v[104:107], v[112:115]\n"
        "v_mfma_f32_16x16x32_bf16 v[116:119], v[36:39], v[108:111], v[116:119]\n"
        "s_barrier\n"
        "ds_read_b128 v[96:99], v176 offset:49152\n"
        "ds_read_b128 v[100:103], v176 offset:51200\n"
        "ds_read_b128 v[104:107], v177 offset:49152\n"
        "ds_read_b128 v[108:111], v177 offset:51200\n"
        "s_waitcnt vmcnt(0) lgkmcnt(0)\n"
        "v_mfma_f32_16x16x32_bf16 v[112:115], v[40:43], v[96:99], v[112:115]\n"
        "v_mfma_f32_16x16x32_bf16 v[116:119], v[40:43], v[100:103], v[116:119]\n"
        "v_mfma_f32_16x16x32_bf16 v[112:115], v[44:47], v[104:107], v[112:115]\n"
        "v_mfma_f32_16x16x32_bf16 v[116:119], v[44:47], v[108:111], v[116:119]\n"
        "s_barrier\n"
        "ds_read_b128 v[96:99], v176\n"
        "ds_read_b128 v[100:103], v176 offset:2048\n"
        "ds_read_b128 v[104:107], v177\n"
        "ds_read_b128 v[108:111], v177 offset:2048\n"
        "s_waitcnt vmcnt(0) lgkmcnt(0)\n"
        "v_mfma_f32_16x16x32_bf16 v[112:115], v[48:51], v[96:99], v[112:115]\n"
        "v_mfma_f32_16x16x32_bf16 v[116:119], v[48:51], v[100:103], v[116:119]\n"
        "v_mfma_f32_16x16x32_bf16 v[112:115], v[52:55], v[104:107], v[112:115]\n"
        "v_mfma_f32_16x16x32_bf16 v[116:119], v[52:55], v[108:111], v[116:119]\n"
        "s_barrier\n"
        "ds_read_b128 v[96:99], v176 offset:16384\n"
        "ds_read_b128 v[100:103], v176 offset:18432\n"
        "ds_read_b128 v[104:107], v177 offset:16384\n"
        "ds_read_b128 v[108:111], v177 offset:18432\n"
        "s_waitcnt vmcnt(0) lgkmcnt(0)\n"
        "v_mfma_f32_16x16x32_bf16 v[112:115], v[56:59], v[96:99], v[112:115]\n"
        "v_mfma_f32_16x16x32_bf16 v[116:119], v[56:59], v[100:103], v[116:119]\n"
        "v_mfma_f32_16x16x32_bf16 v[112:115], v[60:63], v[104:107], v[112:115]\n"
        "v_mfma_f32_16x16x32_bf16 v[116:119], v[60:63], v[108:111], v[116:119]\n"
        "s_barrier\n"
        "ds_read_b128 v[96:99], v176 offset:32768\n"
        "ds_read_b128 v[100:103], v176 offset:34816\n"
        "ds_read_b128 v[104:107], v177 offset:32768\n"
        "ds_read_b128 v[108:111], v177 offset:34816\n"
        "s_waitcnt vmcnt(0) lgkmcnt(0)\n"
        "v_mfma_f32_16x16x32_bf16 v[112:115], v[64:67], v[96:99], v[112:115]\n"
        "v_mfma_f32_16x16x32_bf16 v[116:119], v[64:67], v[100:103], v[116:119]\n"
        "v_mfma_f32_16x16x32_bf16 v[112:115], v[68:71], v[104:107], v[112:115]\n"
        "v_mfma_f32_16x16x32_bf16 v[116:119], v[68:71], v[108:111], v[116:119]\n"
        "s_barrier\n"
        "ds_read_b128 v[96:99], v176 offset:49152\n"
        "ds_read_b128 v[100:103], v176 offset:51200\n"
        "ds_read_b128 v[104:107], v177 offset:49152\n"
        "ds_read_b128 v[108:111], v177 offset:51200\n"
        "s_waitcnt vmcnt(0) lgkmcnt(0)\n"
        "v_mfma_f32_16x16x32_bf16 v[112:115], v[72:75], v[96:99], v[112:115]\n"
        "v_mfma_f32_16x16x32_bf16 v[116:119], v[72:75], v[100:103], v[116:119]\n"
        "v_mfma_f32_16x16x32_bf16 v[112:115], v[76:79], v[104:107], v[112:115]\n"
        "v_mfma_f32_16x16x32_bf16 v[116:119], v[76:79], v[108:111], v[116:119]\n"
        "s_barrier\n"
        "ds_read_b128 v[96:99], v176\n"
        "ds_read_b128 v[100:103], v176 offset:2048\n"
        "ds_read_b128 v[104:107], v177\n"
        "ds_read_b128 v[108:111], v177 offset:2048\n"
        "s_waitcnt vmcnt(0) lgkmcnt(0)\n"
        "v_mfma_f32_16x16x32_bf16 v[112:115], v[80:83], v[96:99], v[112:115]\n"
        "v_mfma_f32_16x16x32_bf16 v[116:119], v[80:83], v[100:103], v[116:119]\n"
        "v_mfma_f32_16x16x32_bf16 v[112:115], v[84:87], v[104:107], v[112:115]\n"
        "v_mfma_f32_16x16x32_bf16 v[116:119], v[84:87], v[108:111], v[116:119]\n"
        "s_barrier\n"
        "ds_read_b128 v[96:99], v176 offset:16384\n"
        "ds_read_b128 v[100:103], v176 offset:18432\n"
        "ds_read_b128 v[104:107], v177 offset:16384\n"
        "ds_read_b128 v[108:111], v177 offset:18432\n"
        "s_waitcnt vmcnt(0) lgkmcnt(0)\n"
        "v_mfma_f32_16x16x32_bf16 v[112:115], v[88:91], v[96:99], v[112:115]\n"
        "v_mfma_f32_16x16x32_bf16 v[116:119], v[88:91], v[100:103], v[116:119]\n"
        "v_mfma_f32_16x16x32_bf16 v[112:115], v[92:95], v[104:107], v[112:115]\n"
        "v_mfma_f32_16x16x32_bf16 v[116:119], v[92:95], v[108:111], v[116:119]\n"
        "s_barrier\n"
        "s_mov_b32 vcc_lo, 0xffff0000\n"
        "s_mov_b32 vcc_hi, 0xffff0000\n"
        "s_nop 4\n"
        "v_cndmask_b32 v112, v112, v116, vcc\n"
        "v_mul_f32 v112, 0xbfb8aa3b, v112\n"
        "v_exp_f32 v112, v112\n"
        "s_nop 0\n"
        "v_add_f32 v112, 1.0, v112\n"
        "v_rcp_f32 v112, v112\n"
        "s_nop 0\n"
        "buffer_store_dword v112, v182, s[52:55], 0 offen sc1\n"
        "s_barrier\n"
        "s_branch 43f\n"
        "6:\n"
        "v_cvt_pk_bf16_f32 v112, v16, v17\n"
        "v_cvt_pk_bf16_f32 v113, v18, v19\n"
        "v_cvt_pk_bf16_f32 v114, v20, v21\n"
        "v_cvt_pk_bf16_f32 v115, v22, v23\n"
        "v_cvt_pk_bf16_f32 v116, v24, v25\n"
        "v_cvt_pk_bf16_f32 v117, v26, v27\n"
        "v_cvt_pk_bf16_f32 v118, v28, v29\n"
        "v_cvt_pk_bf16_f32 v119, v30, v31\n"
        "v_permlane16_swap_b32 v112, v114\n"
        "v_permlane16_swap_b32 v113, v115\n"
        "buffer_store_dwordx4 v[112:115], v182, s[52:55], 0 offen sc1\n"
        "v_cvt_pk_bf16_f32 v120, v32, v33\n"
        "v_cvt_pk_bf16_f32 v121, v34, v35\n"
        "v_cvt_pk_bf16_f32 v122, v36, v37\n"
        "v_cvt_pk_bf16_f32 v123, v38, v39\n"
        "v_permlane16_swap_b32 v116, v118\n"
        "v_permlane16_swap_b32 v117, v119\n"
        "buffer_store_dwordx4 v[116:119], v182, s[52:55], s73 offen sc1\n"
        "v_cvt_pk_bf16_f32 v124, v40, v41\n"
        "v_cvt_pk_bf16_f32 v125, v42, v43\n"
        "v_cvt_pk_bf16_f32 v126, v44, v45\n"
        "v_cvt_pk_bf16_f32 v127, v46, v47\n"
        "v_permlane16_swap_b32 v120, v122\n"
        "v_permlane16_swap_b32 v121, v123\n"
        "buffer_store_dwordx4 v[120:123], v183, s[52:55], 0 offen sc1\n"
        "v_cvt_pk_bf16_f32 v128, v48, v49\n"
        "v_cvt_pk_bf16_f32 v129, v50, v51\n"
        "v_cvt_pk_bf16_f32 v130, v52, v53\n"
        "v_cvt_pk_bf16_f32 v131, v54, v55\n"
        "v_permlane16_swap_b32 v124, v126\n"
        "v_permlane16_swap_b32 v125, v127\n"
        "buffer_store_dwordx4 v[124:127], v183, s[52:55], s73 offen sc1\n"
        "v_cvt_pk_bf16_f32 v132, v56, v57\n"
        "v_cvt_pk_bf16_f32 v133, v58, v59\n"
        "v_cvt_pk_bf16_f32 v134, v60, v61\n"
        "v_cvt_pk_bf16_f32 v135, v62, v63\n"
        "v_permlane16_swap_b32 v128, v130\n"
        "v_permlane16_swap_b32 v129, v131\n"
        "buffer_store_dwordx4 v[128:131], v184, s[52:55], 0 offen sc1\n"
        "v_cvt_pk_bf16_f32 v136, v64, v65\n"
        "v_cvt_pk_bf16_f32 v137, v66, v67\n"
        "v_cvt_pk_bf16_f32 v138, v68, v69\n"
        "v_cvt_pk_bf16_f32 v139, v70, v71\n"
        "v_permlane16_swap_b32 v132, v134\n"
        "v_permlane16_swap_b32 v133, v135\n"
        "buffer_store_dwordx4 v[132:135], v184, s[52:55], s73 offen sc1\n"
        "v_cvt_pk_bf16_f32 v140, v72, v73\n"
        "v_cvt_pk_bf16_f32 v141, v74, v75\n"
        "v_cvt_pk_bf16_f32 v142, v76, v77\n"
        "v_cvt_pk_bf16_f32 v143, v78, v79\n"
        "v_permlane16_swap_b32 v136, v138\n"
        "v_permlane16_swap_b32 v137, v139\n"
        "buffer_store_dwordx4 v[136:139], v185, s[52:55], 0 offen sc1\n"
        "s_nop 1\n"
        "v_permlane16_swap_b32 v140, v142\n"
        "v_permlane16_swap_b32 v141, v143\n"
        "buffer_store_dwordx4 v[140:143], v185, s[52:55], s73 offen sc1\n"
        "s_waitcnt lgkmcnt(0)\n"
        "s_barrier\n"
        "43:\n"
        "s_mov_b32 s80, m0\n"
        "buffer_load_dwordx2 v[20:21], off, s[52:55], 0 sc1\n"
        "v_mov_b32 v16, 116736\n"
        "v_mov_b32 v17, 1\n"
        "s_lshr_b32 s40, %[b], 6\n"
        "s_mul_i32 s41, s40, 4\n"
        "s_and_b32 s42, %[b], 7\n"
        "s_lshr_b32 s43, s42, 1\n"
        "s_add_u32 s43, s41, s43\n"
        "s_mul_i32 s43, s43, 16\n"
        "s_lshr_b32 s41, %[b], 3\n"
        "s_and_b32 s41, s41, 7\n"
        "s_mul_i32 s44, s41, 2\n"
        "s_and_b32 s45, s42, 1\n"
        "s_add_u32 s45, s44, s45\n"
        "s_add_u32 s45, s43, s45\n"
        "s_mul_i32 s45, s45, 0x80\n"
        "s_add_u32 s64, s45, 0x80\n"
        "s_mul_i32 s41, s41, 8\n"
        "s_mul_i32 s45, %[w], 2\n"
        "s_add_u32 s45, s41, s45\n"
        "s_mul_i32 s45, s45, 0xa000\n"
        "s_add_u32 s45, s45, 0x19c80\n"
        "s_mul_i32 s42, s42, 4\n"
        "s_add_u32 s42, s42, s40\n"
        "s_lshr_b32 s42, s42, 1\n"
        "s_mul_i32 s42, s42, 0xa00\n"
        "s_add_u32 s65, s45, s42\n"
        "s_mul_i32 s45, %[w], 0xc00\n"
        "s_add_u32 s66, s45, 0x5000\n"
        "s_mul_i32 s45, %[w], 0x28000\n"
        "s_add_u32 s70, s45, 0x299880\n"
        "s_mul_i32 s45, %[w], 0x1640\n"
        "s_add_u32 s71, s45, 0x1ce00\n"
        "s_add_u32 s42, s42, 0xa800\n"
        "s_and_b32 s40, s40, 1\n"
        "s_mul_i32 s40, s40, 0x600\n"
        "s_add_u32 s83, s42, s40\n"
        "s_mov_b64 exec, 0\n"
        "s_cmp_eq_u32 %[w], 0\n"
        "s_cbranch_scc1 .Lk_hop0\n"
        "s_cmp_eq_u32 %[w], 1\n"
        "s_cbranch_scc1 .Lk_hop1\n"
        "s_cmp_eq_u32 %[w], 2\n"
        "s_cbranch_scc1 .Lk_hop2\n"
        "s_branch .Lk_hop3\n"
        ".Lk_ret:\n"
        "s_mov_b64 exec, -1\n"
        "s_waitcnt vmcnt(0)\n"
        "v_readfirstlane_b32 s60, v20\n"
        "v_readfirstlane_b32 s61, v21\n"
        "s_nop 0\n"
        "s_add_u32 s60, s60, 0x7f4a7c15\n"
        "s_addc_u32 s61, s61, 0x9e3779b9\n"
        "s_cmp_lg_u32 s72, 0\n"
        "s_cbranch_scc1 4f\n"
        "s_mov_b64 exec, 0x1\n"
        "ds_add_rtn_u32 v18, v16, v17\n"
        "s_mov_b64 exec, -1\n"
        "s_waitcnt lgkmcnt(0)\n"
        "v_readfirstlane_b32 s56, v18\n"
        "s_nop 1\n"
        "s_cmp_lg_u32 s56, 3\n"
        "s_cbranch_scc1 4f\n"
        "v_mov_b32 v18, s60\n"
        "v_mov_b32 v19, s61\n"
        "s_mov_b64 exec, 1\n"
        "buffer_store_dwordx2 v[18:19], v190, s[52:55], s64 offen sc1\n"
        "s_mov_b64 exec, -1\n"
        "4:\n"
        "s_barrier\n"
        "s_mov_b64 exec, 0x3ff\n"
        "buffer_load_dwordx2 v[20:21], v186, s[52:55], 0 offen sc1\n"
        "s_mov_b64 exec, -1\n"
        "v_mov_b32 v152, 0xdddddddd\n"
        "s_mov_b32 s40, 0\n"
        "v_mov_b32 v22, 0\n"
        "v_mov_b32 v23, 0\n"
        "v_mov_b32 v24, 0\n"
        "v_mov_b32 v25, 0\n"
        "v_mov_b32 v26, 0\n"
        "v_mov_b32 v27, 0\n"
        "v_mov_b32 v28, 0\n"
        "v_mov_b32 v29, 0\n"
        "v_mov_b32 v30, 0\n"
        "v_mov_b32 v31, 0\n"
        "v_mov_b32 v32, 0\n"
        "v_mov_b32 v33, 0\n"
        "v_mov_b32 v34, 0\n"
        "v_mov_b32 v35, 0\n"
        "v_mov_b32 v36, 0\n"
        "v_mov_b32 v37, 0\n"
        "v_mov_b32 v38, 0\n"
        "v_mov_b32 v39, 0\n"
        "v_mov_b32 v40, 0\n"
        "v_mov_b32 v41, 0\n"
        "v_mov_b32 v42, 0\n"
        "v_mov_b32 v43, 0\n"
        "v_mov_b32 v44, 0\n"
        "v_mov_b32 v45, 0\n"
        "v_mov_b32 v46, 0\n"
        "v_mov_b32 v47, 0\n"
        "v_mov_b32 v104, 0\n"
        "v_mov_b32 v105, 0\n"
        "v_mov_b32 v106, 0\n"
        "v_mov_b32 v107, 0\n"
        "v_mov_b32 v108, 0\n"
        "v_mov_b32 v109, 0\n"
        "v_mov_b32 v110, 0\n"
        "v_mov_b32 v111, 0\n"
        "v_mov_b32 v148, 0\n"
        "v_mov_b32 v149, 0\n"
        "v_mov_b32 v150, 0\n"
        "v_mov_b32 v151, 0\n"
        "s_mov_b64 exec, 0x3ff\n"
        "s_waitcnt vmcnt(0)\n"
        "s_mov_b32 s58, 0\n"
        "5:\n"
        "v_cmp_eq_u64 vcc, s[60:61], v[20:21]\n"
        "s_nop 1\n"
        "s_cmp_eq_u64 vcc, exec\n"
        "s_cbranch_scc1 6f\n"
        "s_sleep 2\n"
        "buffer_load_dwordx2 v[20:21], v186, s[52:55], 0 offen sc1\n"
        "s_add_u32 s58, s58, 1\n"
        "s_cmp_lt_u32 s58, 262144\n"
        "s_waitcnt vmcnt(0)\n"
        "s_cbranch_scc1 5b\n"
        "6:\n"
        "s_mov_b64 exec, -1\n"
        "s_mov_b32 m0, s66\n"
        "s_add_u32 s62, s65, 39936\n"
        "buffer_load_dwordx4 v188, s[52:55], s65 offen sc1 lds\n"
        "buffer_load_dwordx4 v188, s[52:55], s62 offen offset:1024 sc1 lds\n"
        "buffer_load_dwordx4 v189, s[52:55], s65 offen offset:2048 sc1 lds\n"
        "s_add_u32 m0, s71, 0\n"
        "s_add_u32 s56, s70, 0\n"
        "s_add_u32 s57, s56, s83\n"
        "buffer_load_dwordx4 v190, s[52:55], s57 offen sc1 lds\n"
        "buffer_load_dwordx4 v187, s[52:55], s56 offen offset:1024 sc1 lds\n"
        "s_mov_b32 exec_hi, 0xffff\n"
        "buffer_load_dwordx4 v191, s[52:55], s56 offen offset:2048 sc1 lds\n"
        "s_mov_b32 exec_hi, -1\n"
        "s_add_u32 m0, s71, 2848\n"
        "s_add_u32 s56, s56, 81920\n"
        "s_add_u32 s57, s56, s83\n"
        "buffer_load_dwordx4 v190, s[52:55], s57 offen sc1 lds\n"
        "buffer_load_dwordx4 v187, s[52:55], s56 offen offset:1024 sc1 lds\n"
        "s_mov_b32 exec_hi, 0xffff\n"
        "buffer_load_dwordx4 v191, s[52:55], s56 offen offset:2048 sc1 lds\n"
        "s_mov_b32 exec_hi, -1\n"
        "s_mov_b32 m0, s80\n"
        "v_mov_b32 v16, 0\n"
        "v_mov_b32 v17, 0\n"
        "v_mov_b32 v18, 0\n"
        "v_mov_b32 v19, 0\n"
        "v_mov_b32 v20, 0\n"
        "v_mov_b32 v21, 0\n"
        "s_mov_b32 s62, 81920\n"
        "s_mov_b32 s56, 0x3ea52b5c\n"
        "s_cmp_lg_u32 s72, 0\n"
        "s_cbranch_scc0 8f\n"
        "buffer_store_dwordx4 v[112:115], v182, s[52:55], 0 offen sc1\n"
        "buffer_store_dwordx4 v[116:119], v182, s[52:55], s62 offen sc1\n"
        "buffer_store_dwordx4 v[120:123], v183, s[52:55], 0 offen sc1\n"
        "buffer_store_dwordx4 v[124:127], v183, s[52:55], s62 offen sc1\n"
        "s_waitcnt vmcnt(10)\n"
        "ds_read_b128 v[48:51], v156\n"
        "ds_read_b128 v[52:55], v156 offset:1024\n"
        "ds_read_b128 v[56:59], v156 offset:2048\n"
        "buffer_store_dwordx4 v[128:131], v184, s[52:55], 0 offen sc1\n"
        "s_waitcnt lgkmcnt(2)\n"
        "v_lshlrev_b32 v64, 16, v48\n"
        "v_and_b32 v65, 0xffff0000, v48\n"
        "v_lshlrev_b32 v66, 16, v49\n"
        "v_and_b32 v67, 0xffff0000, v49\n"
        "v_lshlrev_b32 v68, 16, v50\n"
        "v_and_b32 v69, 0xffff0000, v50\n"
        "v_lshlrev_b32 v70, 16, v51\n"
        "v_and_b32 v71, 0xffff0000, v51\n"
        "v_mul_f32 v64, s56, v64\n"
        "v_mul_f32 v65, s56, v65\n"
        "v_mul_f32 v66, s56, v66\n"
        "v_mul_f32 v67, s56, v67\n"
        "v_mul_f32 v68, s56, v68\n"
        "v_mul_f32 v69, s56, v69\n"
        "v_mul_f32 v70, s56, v70\n"
        "v_mul_f32 v71, s56, v71\n"
        "v_cvt_pk_bf16_f32 v48, v64, v65\n"
        "v_cvt_pk_bf16_f32 v49, v66, v67\n"
        "v_cvt_pk_bf16_f32 v50, v68, v69\n"
        "v_cvt_pk_bf16_f32 v51, v70, v71\n"
        "ds_write_b128 v156, v[48:51]\n"
        "buffer_store_dwordx4 v[132:135], v184, s[52:55], s62 offen sc1\n"
        "s_waitcnt lgkmcnt(2)\n"
        "v_lshlrev_b32 v64, 16, v52\n"
        "v_and_b32 v65, 0xffff0000, v52\n"
        "v_lshlrev_b32 v66, 16, v53\n"
        "v_and_b32 v67, 0xffff0000, v53\n"
        "v_lshlrev_b32 v68, 16, v54\n"
        "v_and_b32 v69, 0xffff0000, v54\n"
        "v_lshlrev_b32 v70, 16, v55\n"
        "v_and_b32 v71, 0xffff0000, v55\n"
        "v_mul_f32 v64, s56, v64\n"
        "v_mul_f32 v65, s56, v65\n"
        "v_mul_f32 v66, s56, v66\n"
        "v_mul_f32 v67, s56, v67\n"
        "v_mul_f32 v68, s56, v68\n"
        "v_mul_f32 v69, s56, v69\n"
        "v_mul_f32 v70, s56, v70\n"
        "v_mul_f32 v71, s56, v71\n"
        "v_cvt_pk_bf16_f32 v52, v64, v65\n"
        "v_cvt_pk_bf16_f32 v53, v66, v67\n"
        "v_cvt_pk_bf16_f32 v54, v68, v69\n"
        "v_cvt_pk_bf16_f32 v55, v70, v71\n"
        "ds_write_b128 v156, v[52:55] offset:1024\n"
        "buffer_store_dwordx4 v[136:139], v185, s[52:55], 0 offen sc1\n"
        "s_waitcnt lgkmcnt(2)\n"
        "v_lshlrev_b32 v64, 16, v56\n"
        "v_and_b32 v65, 0xffff0000, v56\n"
        "v_lshlrev_b32 v66, 16, v57\n"
        "v_and_b32 v67, 0xffff0000, v57\n"
        "v_lshlrev_b32 v68, 16, v58\n"
        "v_and_b32 v69, 0xffff0000, v58\n"
        "v_lshlrev_b32 v70, 16, v59\n"
        "v_and_b32 v71, 0xffff0000, v59\n"
        "v_mul_f32 v64, s56, v64\n"
        "v_mul_f32 v65, s56, v65\n"
        "v_mul_f32 v66, s56, v66\n"
        "v_mul_f32 v67, s56, v67\n"
        "v_mul_f32 v68, s56, v68\n"
        "v_mul_f32 v69, s56, v69\n"
        "v_mul_f32 v70, s56, v70\n"
        "v_mul_f32 v71, s56, v71\n"
        "v_cvt_pk_bf16_f32 v56, v64, v65\n"
        "v_cvt_pk_bf16_f32 v57, v66, v67\n"
        "v_cvt_pk_bf16_f32 v58, v68, v69\n"
        "v_cvt_pk_bf16_f32 v59, v70, v71\n"
        "ds_write_b128 v156, v[56:59] offset:2048\n"
        "buffer_store_dwordx4 v[140:143], v185, s[52:55], s62 offen sc1\n"
        "s_waitcnt vmcnt(8) lgkmcnt(0)\n"
        "s_branch 9f\n"
        "8:\n"
        "s_waitcnt vmcnt(6)\n"
        "ds_read_b128 v[48:51], v156\n"
        "ds_read_b128 v[52:55], v156 offset:1024\n"
        "ds_read_b128 v[56:59], v156 offset:2048\n"
        "s_waitcnt lgkmcnt(2)\n"
        "v_lshlrev_b32 v64, 16, v48\n"
        "v_and_b32 v65, 0xffff0000, v48\n"
        "v_lshlrev_b32 v66, 16, v49\n"
        "v_and_b32 v67, 0xffff0000, v49\n"
        "v_lshlrev_b32 v68, 16, v50\n"
        "v_and_b32 v69, 0xffff0000, v50\n"
        "v_lshlrev_b32 v70, 16, v51\n"
        "v_and_b32 v71, 0xffff0000, v51\n"
        "v_mul_f32 v64, s56, v64\n"
        "v_mul_f32 v65, s56, v65\n"
        "v_mul_f32 v66, s56, v66\n"
        "v_mul_f32 v67, s56, v67\n"
        "v_mul_f32 v68, s56, v68\n"
        "v_mul_f32 v69, s56, v69\n"
        "v_mul_f32 v70, s56, v70\n"
        "v_mul_f32 v71, s56, v71\n"
        "v_cvt_pk_bf16_f32 v48, v64, v65\n"
        "v_cvt_pk_bf16_f32 v49, v66, v67\n"
        "v_cvt_pk_bf16_f32 v50, v68, v69\n"
        "v_cvt_pk_bf16_f32 v51, v70, v71\n"
        "ds_write_b128 v156, v[48:51]\n"
        "s_waitcnt lgkmcnt(2)\n"
        "v_lshlrev_b32 v64, 16, v52\n"
        "v_and_b32 v65, 0xffff0000, v52\n"
        "v_lshlrev_b32 v66, 16, v53\n"
        "v_and_b32 v67, 0xffff0000, v53\n"
        "v_lshlrev_b32 v68, 16, v54\n"
        "v_and_b32 v69, 0xffff0000, v54\n"
        "v_lshlrev_b32 v70, 16, v55\n"
        "v_and_b32 v71, 0xffff0000, v55\n"
        "v_mul_f32 v64, s56, v64\n"
        "v_mul_f32 v65, s56, v65\n"
        "v_mul_f32 v66, s56, v66\n"
        "v_mul_f32 v67, s56, v67\n"
        "v_mul_f32 v68, s56, v68\n"
        "v_mul_f32 v69, s56, v69\n"
        "v_mul_f32 v70, s56, v70\n"
        "v_mul_f32 v71, s56, v71\n"
        "v_cvt_pk_bf16_f32 v52, v64, v65\n"
        "v_cvt_pk_bf16_f32 v53, v66, v67\n"
        "v_cvt_pk_bf16_f32 v54, v68, v69\n"
        "v_cvt_pk_bf16_f32 v55, v70, v71\n"
        "ds_write_b128 v156, v[52:55] offset:1024\n"
        "s_waitcnt lgkmcnt(2)\n"
        "v_lshlrev_b32 v64, 16, v56\n"
        "v_and_b32 v65, 0xffff0000, v56\n"
        "v_lshlrev_b32 v66, 16, v57\n"
        "v_and_b32 v67, 0xffff0000, v57\n"
        "v_lshlrev_b32 v68, 16, v58\n"
        "v_and_b32 v69, 0xffff0000, v58\n"
        "v_lshlrev_b32 v70, 16, v59\n"
        "v_and_b32 v71, 0xffff0000, v59\n"
        "v_mul_f32 v64, s56, v64\n"
        "v_mul_f32 v65, s56, v65\n"
        "v_mul_f32 v66, s56, v66\n"
        "v_mul_f32 v67, s56, v67\n"
        "v_mul_f32 v68, s56, v68\n"
        "v_mul_f32 v69, s56, v69\n"
        "v_mul_f32 v70, s56, v70\n"
        "v_mul_f32 v71, s56, v71\n"
        "v_cvt_pk_bf16_f32 v56, v64, v65\n"
        "v_cvt_pk_bf16_f32 v57, v66, v67\n"
        "v_cvt_pk_bf16_f32 v58, v68, v69\n"
        "v_cvt_pk_bf16_f32 v59, v70, v71\n"
        "ds_write_b128 v156, v[56:59] offset:2048\n"
        "s_waitcnt vmcnt(0) lgkmcnt(0)\n"
        "9:\n"
        "s_barrier\n"
        "s_setprio 1\n"
        "ds_read_b128 v[112:115], v156\n"
        "ds_read_b64 v[116:117], v157\n"
        "ds_read_b128 v[124:127], v158\n"
        "ds_read_b64 v[128:129], v159\n"
        "ds_read_b128 v[118:121], v156 offset:1024\n"
        "ds_read_b64 v[122:123], v157 offset:512\n"
        "ds_read_b128 v[130:133], v158 offset:2848\n"
        "ds_read_b64 v[134:135], v159 offset:2848\n"
        "ds_read_b128 v[136:139], v160\n"
        "ds_read_b128 v[140:143], v161\n"
        "s_waitcnt lgkmcnt(0)\n"
        "s_mov_b32 exec_lo, 0\n"
        "v_mov_b32 v116, 0\n"
        "v_mov_b32 v117, 0\n"
        "v_mov_b32 v122, 0\n"
        "v_mov_b32 v123, 0\n"
        "s_mov_b32 exec_lo, -1\n"
        "v_mfma_f32_32x32x16_bf16 v[48:63], v[124:127], v[112:115], 0\n"
        "v_mfma_f32_32x32x8_bf16 v[48:63], v[128:129], v[116:117], v[48:63]\n"
        "s_nop 11\n"
        "1:\n"
        "v_mfma_f32_32x32x16_bf16 v[64:79], v[124:127], v[118:121], 0\n"
        "v_exp_f32 v48, v48\n"
        "v_exp_f32 v49, v49\n"
        "v_exp_f32 v50, v50\n"
        "v_exp_f32 v51, v51\n"
        "v_mfma_f32_32x32x8_bf16 v[64:79], v[128:129], v[122:123], v[64:79]\n"
        "v_exp_f32 v52, v52\n"
        "v_exp_f32 v53, v53\n"
        "v_exp_f32 v54, v54\n"
        "v_exp_f32 v55, v55\n"
        "v_smfmac_f32_32x32x32_bf16 v[32:47], v[148:151], v[104:111], v152\n"
        "v_exp_f32 v56, v56\n"
        "v_exp_f32 v57, v57\n"
        "v_exp_f32 v58, v58\n"
        "v_exp_f32 v59, v59\n"
        "s_waitcnt lgkmcnt(0)\n"
        "v_smfmac_f32_32x32x32_bf16 v[16:31], v[136:139], v[48:55], v152\n"
        "v_exp_f32 v60, v60\n"
        "v_exp_f32 v61, v61\n"
        "v_exp_f32 v62, v62\n"
        "v_exp_f32 v63, v63\n"
        "v_mfma_f32_32x32x16_bf16 v[80:95], v[130:133], v[112:115], 0\n"
        "ds_read_b128 v[144:147], v160 offset:2848\n"
        "v_exp_f32 v64, v64\n"
        "v_exp_f32 v65, v65\n"
        "v_exp_f32 v66, v66\n"
        "v_exp_f32 v67, v67\n"
        "v_mfma_f32_32x32x8_bf16 v[80:95], v[134:135], v[116:117], v[80:95]\n"
        "ds_read_b128 v[148:151], v161 offset:2848\n"
        "v_exp_f32 v68, v68\n"
        "v_exp_f32 v69, v69\n"
        "v_exp_f32 v70, v70\n"
        "v_exp_f32 v71, v71\n"
        "v_smfmac_f32_32x32x32_bf16 v[16:31], v[140:143], v[56:63], v152\n"
        "ds_read_b128 v[124:127], v158 offset:5696\n"
        "v_exp_f32 v72, v72\n"
        "v_exp_f32 v73, v73\n"
        "v_exp_f32 v74, v74\n"
        "v_exp_f32 v75, v75\n"
        "v_smfmac_f32_32x32x32_bf16 v[32:47], v[136:139], v[64:71], v152\n"
        "ds_read_b64 v[128:129], v159 offset:5696\n"
        "v_exp_f32 v76, v76\n"
        "v_exp_f32 v77, v77\n"
        "v_exp_f32 v78, v78\n"
        "v_exp_f32 v79, v79\n"
        "v_mfma_f32_32x32x16_bf16 v[96:111], v[130:133], v[118:121], 0\n"
        "v_exp_f32 v80, v80\n"
        "v_exp_f32 v81, v81\n"
        "v_exp_f32 v82, v82\n"
        "v_exp_f32 v83, v83\n"
        "v_mfma_f32_32x32x8_bf16 v[96:111], v[134:135], v[122:123], v[96:111]\n"
        "v_exp_f32 v84, v84\n"
        "v_exp_f32 v85, v85\n"
        "v_exp_f32 v86, v86\n"
        "v_exp_f32 v87, v87\n"
        "v_smfmac_f32_32x32x32_bf16 v[32:47], v[140:143], v[72:79], v152\n"
        "v_exp_f32 v88, v88\n"
        "v_exp_f32 v89, v89\n"
        "v_exp_f32 v90, v90\n"
        "v_exp_f32 v91, v91\n"
        "s_waitcnt lgkmcnt(0)\n"
        "v_smfmac_f32_32x32x32_bf16 v[16:31], v[144:147], v[80:87], v152\n"
        "v_exp_f32 v92, v92\n"
        "v_exp_f32 v93, v93\n"
        "v_exp_f32 v94, v94\n"
        "v_exp_f32 v95, v95\n"
        "v_mfma_f32_32x32x16_bf16 v[48:63], v[124:127], v[112:115], 0\n"
        "ds_read_b128 v[136:139], v160 offset:5696\n"
        "v_exp_f32 v96, v96\n"
        "v_exp_f32 v97, v97\n"
        "v_exp_f32 v98, v98\n"
        "v_exp_f32 v99, v99\n"
        "v_mfma_f32_32x32x8_bf16 v[48:63], v[128:129], v[116:117], v[48:63]\n"
        "ds_read_b128 v[140:143], v161 offset:5696\n"
        "v_exp_f32 v100, v100\n"
        "v_exp_f32 v101, v101\n"
        "v_exp_f32 v102, v102\n"
        "v_exp_f32 v103, v103\n"
        "v_smfmac_f32_32x32x32_bf16 v[16:31], v[148:151], v[88:95], v152\n"
        "ds_read_b128 v[130:133], v158 offset:8544\n"
        "v_exp_f32 v104, v104\n"
        "v_exp_f32 v105, v105\n"
        "v_exp_f32 v106, v106\n"
        "v_exp_f32 v107, v107\n"
        "v_smfmac_f32_32x32x32_bf16 v[32:47], v[144:147], v[96:103], v152\n"
        "ds_read_b64 v[134:135], v159 offset:8544\n"
        "v_exp_f32 v108, v108\n"
        "v_exp_f32 v109, v109\n"
        "v_exp_f32 v110, v110\n"
        "v_exp_f32 v111, v111\n"
        "v_mfma_f32_32x32x16_bf16 v[64:79], v[124:127], v[118:121], 0\n"
        "v_exp_f32 v48, v48\n"
        "v_exp_f32 v49, v49\n"
        "v_exp_f32 v50, v50\n"
        "v_exp_f32 v51, v51\n"
        "v_mfma_f32_32x32x8_bf16 v[64:79], v[128:129], v[122:123], v[64:79]\n"
        "v_exp_f32 v52, v52\n"
        "v_exp_f32 v53, v53\n"
        "v_exp_f32 v54, v54\n"
        "v_exp_f32 v55, v55\n"
        "v_smfmac_f32_32x32x32_bf16 v[32:47], v[148:151], v[104:111], v152\n"
        "v_exp_f32 v56, v56\n"
        "v_exp_f32 v57, v57\n"
        "v_exp_f32 v58, v58\n"
        "v_exp_f32 v59, v59\n"
        "s_waitcnt lgkmcnt(0)\n"
        "v_smfmac_f32_32x32x32_bf16 v[16:31], v[136:139], v[48:55], v152\n"
        "v_exp_f32 v60, v60\n"
        "v_exp_f32 v61, v61\n"
        "v_exp_f32 v62, v62\n"
        "v_exp_f32 v63, v63\n"
        "v_mfma_f32_32x32x16_bf16 v[80:95], v[130:133], v[112:115], 0\n"
        "ds_read_b128 v[144:147], v160 offset:8544\n"
        "v_exp_f32 v64, v64\n"
        "v_exp_f32 v65, v65\n"
        "v_exp_f32 v66, v66\n"
        "v_exp_f32 v67, v67\n"
        "v_mfma_f32_32x32x8_bf16 v[80:95], v[134:135], v[116:117], v[80:95]\n"
        "ds_read_b128 v[148:151], v161 offset:8544\n"
        "v_exp_f32 v68, v68\n"
        "v_exp_f32 v69, v69\n"
        "v_exp_f32 v70, v70\n"
        "v_exp_f32 v71, v71\n"
        "v_smfmac_f32_32x32x32_bf16 v[16:31], v[140:143], v[56:63], v152\n"
        "v_exp_f32 v72, v72\n"
        "v_exp_f32 v73, v73\n"
        "v_exp_f32 v74, v74\n"
        "v_exp_f32 v75, v75\n"
        "v_smfmac_f32_32x32x32_bf16 v[32:47], v[136:139], v[64:71], v152\n"
        "s_waitcnt vmcnt(0) lgkmcnt(0)\n"
        "s_barrier\n"
        "ds_read_b128 v[124:127], v158 offset:22784\n"
        "ds_read_b64 v[128:129], v159 offset:22784\n"
        "v_exp_f32 v76, v76\n"
        "v_exp_f32 v77, v77\n"
        "v_exp_f32 v78, v78\n"
        "v_exp_f32 v79, v79\n"
        "v_mfma_f32_32x32x16_bf16 v[96:111], v[130:133], v[118:121], 0\n"
        "v_exp_f32 v80, v80\n"
        "v_exp_f32 v81, v81\n"
        "v_exp_f32 v82, v82\n"
        "v_exp_f32 v83, v83\n"
        "v_mfma_f32_32x32x8_bf16 v[96:111], v[134:135], v[122:123], v[96:111]\n"
        "v_exp_f32 v84, v84\n"
        "v_exp_f32 v85, v85\n"
        "v_exp_f32 v86, v86\n"
        "v_exp_f32 v87, v87\n"
        "v_smfmac_f32_32x32x32_bf16 v[32:47], v[140:143], v[72:79], v152\n"
        "v_exp_f32 v88, v88\n"
        "v_exp_f32 v89, v89\n"
        "v_exp_f32 v90, v90\n"
        "v_exp_f32 v91, v91\n"
        "s_waitcnt lgkmcnt(0)\n"
        "v_smfmac_f32_32x32x32_bf16 v[16:31], v[144:147], v[80:87], v152\n"
        "v_exp_f32 v92, v92\n"
        "v_exp_f32 v93, v93\n"
        "v_exp_f32 v94, v94\n"
        "v_exp_f32 v95, v95\n"
        "v_mfma_f32_32x32x16_bf16 v[48:63], v[124:127], v[112:115], 0\n"
        "ds_read_b128 v[136:139], v160 offset:22784\n"
        "v_exp_f32 v96, v96\n"
        "v_exp_f32 v97, v97\n"
        "v_exp_f32 v98, v98\n"
        "v_exp_f32 v99, v99\n"
        "v_mfma_f32_32x32x8_bf16 v[48:63], v[128:129], v[116:117], v[48:63]\n"
        "ds_read_b128 v[140:143], v161 offset:22784\n"
        "v_exp_f32 v100, v100\n"
        "v_exp_f32 v101, v101\n"
        "v_exp_f32 v102, v102\n"
        "v_exp_f32 v103, v103\n"
        "v_smfmac_f32_32x32x32_bf16 v[16:31], v[148:151], v[88:95], v152\n"
        "ds_read_b128 v[130:133], v158 offset:25632\n"
        "v_exp_f32 v104, v104\n"
        "v_exp_f32 v105, v105\n"
        "v_exp_f32 v106, v106\n"
        "v_exp_f32 v107, v107\n"
        "v_smfmac_f32_32x32x32_bf16 v[32:47], v[144:147], v[96:103], v152\n"
        "ds_read_b64 v[134:135], v159 offset:25632\n"
        "v_exp_f32 v108, v108\n"
        "v_exp_f32 v109, v109\n"
        "v_exp_f32 v110, v110\n"
        "v_exp_f32 v111, v111\n"
        "v_mfma_f32_32x32x16_bf16 v[64:79], v[124:127], v[118:121], 0\n"
        "v_exp_f32 v48, v48\n"
        "v_exp_f32 v49, v49\n"
        "v_exp_f32 v50, v50\n"
        "v_exp_f32 v51, v51\n"
        "v_mfma_f32_32x32x8_bf16 v[64:79], v[128:129], v[122:123], v[64:79]\n"
        "v_exp_f32 v52, v52\n"
        "v_exp_f32 v53, v53\n"
        "v_exp_f32 v54, v54\n"
        "v_exp_f32 v55, v55\n"
        "v_smfmac_f32_32x32x32_bf16 v[32:47], v[148:151], v[104:111], v152\n"
        "v_exp_f32 v56, v56\n"
        "v_exp_f32 v57, v57\n"
        "v_exp_f32 v58, v58\n"
        "v_exp_f32 v59, v59\n"
        "s_waitcnt lgkmcnt(0)\n"
        "v_smfmac_f32_32x32x32_bf16 v[16:31], v[136:139], v[48:55], v152\n"
        "v_exp_f32 v60, v60\n"
        "v_exp_f32 v61, v61\n"
        "v_exp_f32 v62, v62\n"
        "v_exp_f32 v63, v63\n"
        "v_mfma_f32_32x32x16_bf16 v[80:95], v[130:133], v[112:115], 0\n"
        "ds_read_b128 v[144:147], v160 offset:25632\n"
        "v_exp_f32 v64, v64\n"
        "v_exp_f32 v65, v65\n"
        "v_exp_f32 v66, v66\n"
        "v_exp_f32 v67, v67\n"
        "v_mfma_f32_32x32x8_bf16 v[80:95], v[134:135], v[116:117], v[80:95]\n"
        "ds_read_b128 v[148:151], v161 offset:25632\n"
        "v_exp_f32 v68, v68\n"
        "v_exp_f32 v69, v69\n"
        "v_exp_f32 v70, v70\n"
        "v_exp_f32 v71, v71\n"
        "v_smfmac_f32_32x32x32_bf16 v[16:31], v[140:143], v[56:63], v152\n"
        "ds_read_b128 v[124:127], v158 offset:28480\n"
        "v_exp_f32 v72, v72\n"
        "v_exp_f32 v73, v73\n"
        "v_exp_f32 v74, v74\n"
        "v_exp_f32 v75, v75\n"
        "v_smfmac_f32_32x32x32_bf16 v[32:47], v[136:139], v[64:71], v152\n"
        "ds_read_b64 v[128:129], v159 offset:28480\n"
        "v_exp_f32 v76, v76\n"
        "v_exp_f32 v77, v77\n"
        "v_exp_f32 v78, v78\n"
        "v_exp_f32 v79, v79\n"
        "v_mfma_f32_32x32x16_bf16 v[96:111], v[130:133], v[118:121], 0\n"
        "v_exp_f32 v80, v80\n"
        "v_exp_f32 v81, v81\n"
        "v_exp_f32 v82, v82\n"
        "v_exp_f32 v83, v83\n"
        "v_mfma_f32_32x32x8_bf16 v[96:111], v[134:135], v[122:123], v[96:111]\n"
        "v_exp_f32 v84, v84\n"
        "v_exp_f32 v85, v85\n"
        "v_exp_f32 v86, v86\n"
        "v_exp_f32 v87, v87\n"
        "v_smfmac_f32_32x32x32_bf16 v[32:47], v[140:143], v[72:79], v152\n"
        "v_exp_f32 v88, v88\n"
        "v_exp_f32 v89, v89\n"
        "v_exp_f32 v90, v90\n"
        "v_exp_f32 v91, v91\n"
        "s_waitcnt lgkmcnt(0)\n"
        "v_smfmac_f32_32x32x32_bf16 v[16:31], v[144:147], v[80:87], v152\n"
        "v_exp_f32 v92, v92\n"
        "v_exp_f32 v93, v93\n"
        "v_exp_f32 v94, v94\n"
        "v_exp_f32 v95, v95\n"
        "v_mfma_f32_32x32x16_bf16 v[48:63], v[124:127], v[112:115], 0\n"
        "ds_read_b128 v[136:139], v160 offset:28480\n"
        "v_exp_f32 v96, v96\n"
        "v_exp_f32 v97, v97\n"
        "v_exp_f32 v98, v98\n"
        "v_exp_f32 v99, v99\n"
        "v_mfma_f32_32x32x8_bf16 v[48:63], v[128:129], v[116:117], v[48:63]\n"
        "ds_read_b128 v[140:143], v161 offset:28480\n"
        ".Lk_hop2:\n"
        "s_cbranch_execz .Lk_pad9\n"
        "v_exp_f32 v100, v100\n"
        "v_exp_f32 v101, v101\n"
        "v_exp_f32 v102, v102\n"
        "v_exp_f32 v103, v103\n"
        "v_smfmac_f32_32x32x32_bf16 v[16:31], v[148:151], v[88:95], v152\n"
        "ds_read_b128 v[130:133], v158 offset:31328\n"
        "v_exp_f32 v104, v104\n"
        "v_exp_f32 v105, v105\n"
        "v_exp_f32 v106, v106\n"
        "v_exp_f32 v107, v107\n"
        "v_smfmac_f32_32x32x32_bf16 v[32:47], v[144:147], v[96:103], v152\n"
        "ds_read_b64 v[134:135], v159 offset:31328\n"
        "v_exp_f32 v108, v108\n"
        "v_exp_f32 v109, v109\n"
        "v_exp_f32 v110, v110\n"
        "v_exp_f32 v111, v111\n"
        "v_mfma_f32_32x32x16_bf16 v[64:79], v[124:127], v[118:121], 0\n"
        "v_exp_f32 v48, v48\n"
        "v_exp_f32 v49, v49\n"
        "v_exp_f32 v50, v50\n"
        "v_exp_f32 v51, v51\n"
        "v_mfma_f32_32x32x8_bf16 v[64:79], v[128:129], v[122:123], v[64:79]\n"
        "v_exp_f32 v52, v52\n"
        "v_exp_f32 v53, v53\n"
        "v_exp_f32 v54, v54\n"
        "v_exp_f32 v55, v55\n"
        "v_smfmac_f32_32x32x32_bf16 v[32:47], v[148:151], v[104:111], v152\n"
        "v_exp_f32 v56, v56\n"
        "v_exp_f32 v57, v57\n"
        "v_exp_f32 v58, v58\n"
        "v_exp_f32 v59, v59\n"
        "s_waitcnt lgkmcnt(0)\n"
        "v_smfmac_f32_32x32x32_bf16 v[16:31], v[136:139], v[48:55], v152\n"
        "v_exp_f32 v60, v60\n"
        "v_exp_f32 v61, v61\n"
        "v_exp_f32 v62, v62\n"
        "v_exp_f32 v63, v63\n"
        "v_mfma_f32_32x32x16_bf16 v[80:95], v[130:133], v[112:115], 0\n"
        "ds_read_b128 v[144:147], v160 offset:31328\n"
        "v_exp_f32 v64, v64\n"
        "v_exp_f32 v65, v65\n"
        "v_exp_f32 v66, v66\n"
        "v_exp_f32 v67, v67\n"
        "v_mfma_f32_32x32x8_bf16 v[80:95], v[134:135], v[116:117], v[80:95]\n"
        "ds_read_b128 v[148:151], v161 offset:31328\n"
        "v_exp_f32 v68, v68\n"
        "v_exp_f32 v69, v69\n"
        "v_exp_f32 v70, v70\n"
        "v_exp_f32 v71, v71\n"
        "v_smfmac_f32_32x32x32_bf16 v[16:31], v[140:143], v[56:63], v152\n"
        "v_exp_f32 v72, v72\n"
        "v_exp_f32 v73, v73\n"
        "v_exp_f32 v74, v74\n"
        "v_exp_f32 v75, v75\n"
        "v_smfmac_f32_32x32x32_bf16 v[32:47], v[136:139], v[64:71], v152\n"
        "s_waitcnt vmcnt(0) lgkmcnt(0)\n"
        "s_barrier\n"
        "ds_read_b128 v[124:127], v158\n"
        "ds_read_b64 v[128:129], v159\n"
        "v_exp_f32 v76, v76\n"
        "v_exp_f32 v77, v77\n"
        "v_exp_f32 v78, v78\n"
        "v_exp_f32 v79, v79\n"
        "v_mfma_f32_32x32x16_bf16 v[96:111], v[130:133], v[118:121], 0\n"
        "v_exp_f32 v80, v80\n"
        "v_exp_f32 v81, v81\n"
        "v_exp_f32 v82, v82\n"
        "v_exp_f32 v83, v83\n"
        "v_mfma_f32_32x32x8_bf16 v[96:111], v[134:135], v[122:123], v[96:111]\n"
        "v_exp_f32 v84, v84\n"
        "v_exp_f32 v85, v85\n"
        "v_exp_f32 v86, v86\n"
        "v_exp_f32 v87, v87\n"
        "v_smfmac_f32_32x32x32_bf16 v[32:47], v[140:143], v[72:79], v152\n"
        "v_exp_f32 v88, v88\n"
        "v_exp_f32 v89, v89\n"
        "v_exp_f32 v90, v90\n"
        "v_exp_f32 v91, v91\n"
        "s_waitcnt lgkmcnt(0)\n"
        "v_smfmac_f32_32x32x32_bf16 v[16:31], v[144:147], v[80:87], v152\n"
        "v_exp_f32 v92, v92\n"
        "v_exp_f32 v93, v93\n"
        "v_exp_f32 v94, v94\n"
        "v_exp_f32 v95, v95\n"
        "v_mfma_f32_32x32x16_bf16 v[48:63], v[124:127], v[112:115], 0\n"
        "ds_read_b128 v[136:139], v160\n"
        "v_exp_f32 v96, v96\n"
        "v_exp_f32 v97, v97\n"
        "v_exp_f32 v98, v98\n"
        "v_exp_f32 v99, v99\n"
        "v_mfma_f32_32x32x8_bf16 v[48:63], v[128:129], v[116:117], v[48:63]\n"
        "ds_read_b128 v[140:143], v161\n"
        "v_exp_f32 v100, v100\n"
        "v_exp_f32 v101, v101\n"
        "v_exp_f32 v102, v102\n"
        "v_exp_f32 v103, v103\n"
        "v_smfmac_f32_32x32x32_bf16 v[16:31], v[148:151], v[88:95], v152\n"
        "ds_read_b128 v[130:133], v158 offset:2848\n"
        "v_exp_f32 v104, v104\n"
        "v_exp_f32 v105, v105\n"
        "v_exp_f32 v106, v106\n"
        "v_exp_f32 v107, v107\n"
        "v_smfmac_f32_32x32x32_bf16 v[32:47], v[144:147], v[96:103], v152\n"
        "ds_read_b64 v[134:135], v159 offset:2848\n"
        "v_exp_f32 v108, v108\n"
        "v_exp_f32 v109, v109\n"
        "v_exp_f32 v110, v110\n"
        "v_exp_f32 v111, v111\n"
        "s_add_u32 s40, s40, 1\n"
        "s_bitcmp1_b32 s40, 0\n"
        "s_cbranch_scc1 1b\n"
        "s_cmp_eq_u32 s40, 4\n"
        "s_cbranch_scc1 3f\n"
        "ds_read_b64_tr_b16 v[216:217], v169\n"
        "ds_read_b64_tr_b16 v[218:219], v169 offset:1280\n"
        "ds_read_b64_tr_b16 v[220:221], v169 offset:32\n"
        "ds_read_b64_tr_b16 v[222:223], v169 offset:1312\n"
        "ds_read_b64_tr_b16 v[224:225], v169 offset:64\n"
        "ds_read_b64_tr_b16 v[226:227], v169 offset:1344\n"
        "ds_read_b64_tr_b16 v[228:229], v169 offset:96\n"
        "ds_read_b64_tr_b16 v[230:231], v169 offset:1376\n"
        "ds_read_b64_tr_b16 v[232:233], v169 offset:128\n"
        "ds_read_b64_tr_b16 v[234:235], v169 offset:1408\n"
        "buffer_load_dword v212, v166, s[52:55], 0 offen sc1\n"
        "buffer_load_dword v213, v166, s[52:55], 0 offen offset:64 sc1\n"
        "buffer_load_dword v214, v166, s[52:55], 0 offen offset:128 sc1\n"
        "buffer_load_dword v215, v166, s[52:55], 0 offen offset:192 sc1\n"
        "buffer_load_dword v156, v254, s[52:55], 0 offen sc1\n"
        "buffer_load_dword v157, v254, s[52:55], 0 offen offset:128 sc1\n"
        "s_branch 1b\n"
        "3:\n"
        "v_smfmac_f32_32x32x32_bf16 v[32:47], v[148:151], v[104:111], v152\n"
        "s_waitcnt vmcnt(0)\n"
        "ds_read_b128 v[188:191], v165\n"
        "ds_read_b128 v[172:175], v163\n"
        "ds_read_b128 v[176:179], v163 offset:2048\n"
        "ds_read_b128 v[180:183], v163 offset:4096\n"
        "ds_read_b128 v[184:187], v163 offset:6144\n"
        "ds_read_b128 v[192:195], v165 offset:1024\n"
        "ds_read_b128 v[196:199], v165 offset:2048\n"
        "ds_read_b128 v[200:203], v165 offset:3072\n"
        "ds_read_b128 v[204:207], v165 offset:4096\n"
        "s_waitcnt lgkmcnt(0)\n"
        "v_mfma_f32_16x16x32_bf16 v[48:51], v[188:191], v[172:175], 0\n"
        "ds_read_b128 v[128:131], v165 offset:10240\n"
        "v_mfma_f32_16x16x32_bf16 v[68:71], v[188:191], v[176:179], 0\n"
        "ds_read_b128 v[236:239], v164\n"
        "v_mfma_f32_16x16x32_bf16 v[88:91], v[188:191], v[180:183], 0\n"
        "ds_read_b128 v[240:243], v164 offset:2048\n"
        "v_mfma_f32_16x16x32_bf16 v[108:111], v[188:191], v[184:187], 0\n"
        "ds_read_b128 v[244:247], v164 offset:4096\n"
        "v_mfma_f32_16x16x32_bf16 v[112:115], v[192:195], v[184:187], 0\n"
        "ds_read_b128 v[248:251], v164 offset:6144\n"
        "v_mfma_f32_16x16x32_bf16 v[92:95], v[192:195], v[180:183], 0\n"
        "ds_read_b128 v[132:135], v165 offset:11264\n"
        "v_mfma_f32_16x16x32_bf16 v[72:75], v[192:195], v[176:179], 0\n"
        "ds_read_b128 v[136:139], v165 offset:12288\n"
        "v_mfma_f32_16x16x32_bf16 v[52:55], v[192:195], v[172:175], 0\n"
        "ds_read_b128 v[140:143], v165 offset:13312\n"
        "v_mfma_f32_16x16x32_bf16 v[56:59], v[196:199], v[172:175], 0\n"
        "ds_read_b128 v[144:147], v165 offset:14336\n"
        "v_mfma_f32_16x16x32_bf16 v[76:79], v[196:199], v[176:179], 0\n"
        "ds_write_b64 v170, v[216:217]\n"
        "v_mfma_f32_16x16x32_bf16 v[96:99], v[196:199], v[180:183], 0\n"
        "v_mfma_f32_16x16x32_bf16 v[116:119], v[196:199], v[184:187], 0\n"
        "ds_write_b64 v170, v[218:219] offset:512\n"
        "v_mfma_f32_16x16x32_bf16 v[120:123], v[200:203], v[184:187], 0\n"
        "v_mfma_f32_16x16x32_bf16 v[100:103], v[200:203], v[180:183], 0\n"
        "ds_write_b64 v170, v[220:221] offset:1024\n"
        "v_mfma_f32_16x16x32_bf16 v[80:83], v[200:203], v[176:179], 0\n"
        "v_mfma_f32_16x16x32_bf16 v[60:63], v[200:203], v[172:175], 0\n"
        "ds_write_b64 v170, v[222:223] offset:1536\n"
        "v_mfma_f32_16x16x32_bf16 v[64:67], v[204:207], v[172:175], 0\n"
        "v_mfma_f32_16x16x32_bf16 v[84:87], v[204:207], v[176:179], 0\n"
        "ds_write_b64 v170, v[224:225] offset:2048\n"
        "v_mfma_f32_16x16x32_bf16 v[104:107], v[204:207], v[180:183], 0\n"
        "s_mov_b32 s46, 0\n"
        "s_mov_b32 s47, -1\n"
        "v_mfma_f32_16x16x32_bf16 v[124:127], v[204:207], v[184:187], 0\n"
        "s_mov_b32 s41, 1.0\n"
        "s_mov_b32 s42, 0x6c800000\n"
        "s_waitcnt lgkmcnt(5)\n"
        "v_mfma_f32_16x16x32_bf16 v[48:51], v[128:131], v[236:239], v[48:51]\n"
        "v_mfma_f32_16x16x32_bf16 v[68:71], v[128:131], v[240:243], v[68:71]\n"
        "v_mfma_f32_16x16x32_bf16 v[88:91], v[128:131], v[244:247], v[88:91]\n"
        "v_mfma_f32_16x16x32_bf16 v[108:111], v[128:131], v[248:251], v[108:111]\n"
        "s_waitcnt lgkmcnt(0)\n"
        "s_barrier\n"
        "ds_read_b128 v[148:151], v162\n"
        "v_mfma_f32_16x16x32_bf16 v[112:115], v[132:135], v[248:251], v[112:115]\n"
        "ds_read_b128 v[188:191], v165 offset:20480\n"
        "v_mfma_f32_16x16x32_bf16 v[92:95], v[132:135], v[244:247], v[92:95]\n"
        "ds_read_b128 v[172:175], v163 offset:16384\n"
        "v_mfma_f32_16x16x32_bf16 v[72:75], v[132:135], v[240:243], v[72:75]\n"
        "ds_read_b128 v[176:179], v163 offset:18432\n"
        "v_mfma_f32_16x16x32_bf16 v[52:55], v[132:135], v[236:239], v[52:55]\n"
        "ds_read_b128 v[180:183], v163 offset:20480\n"
        "v_mfma_f32_16x16x32_bf16 v[56:59], v[136:139], v[236:239], v[56:59]\n"
        "ds_read_b128 v[184:187], v163 offset:22528\n"
        "v_mfma_f32_16x16x32_bf16 v[76:79], v[136:139], v[240:243], v[76:79]\n"
        "ds_read_b128 v[192:195], v165 offset:21504\n"
        "v_mfma_f32_16x16x32_bf16 v[96:99], v[136:139], v[244:247], v[96:99]\n"
        "ds_read_b128 v[196:199], v165 offset:22528\n"
        "v_mfma_f32_16x16x32_bf16 v[116:119], v[136:139], v[248:251], v[116:119]\n"
        "ds_read_b128 v[200:203], v165 offset:23552\n"
        "v_mfma_f32_16x16x32_bf16 v[120:123], v[140:143], v[248:251], v[120:123]\n"
        "ds_read_b128 v[204:207], v165 offset:24576\n"
        "v_mfma_f32_16x16x32_bf16 v[100:103], v[140:143], v[244:247], v[100:103]\n"
        "ds_read_b128 v[152:155], v162 offset:1024\n"
        "v_mfma_f32_16x16x32_bf16 v[80:83], v[140:143], v[240:243], v[80:83]\n"
        "ds_read_b128 v[208:211], v162 offset:2048\n"
        "v_mfma_f32_16x16x32_bf16 v[60:63], v[140:143], v[236:239], v[60:63]\n"
        "ds_read_b32 v15, v171\n"
        "v_mfma_f32_16x16x32_bf16 v[64:67], v[144:147], v[236:239], v[64:67]\n"
        "v_mfma_f32_16x16x32_bf16 v[84:87], v[144:147], v[240:243], v[84:87]\n"
        "v_mfma_f32_16x16x32_bf16 v[104:107], v[144:147], v[244:247], v[104:107]\n"
        "v_mfma_f32_16x16x32_bf16 v[124:127], v[144:147], v[248:251], v[124:127]\n"
        "s_waitcnt lgkmcnt(3)\n"
        "v_mfma_f32_16x16x32_bf16 v[48:51], v[188:191], v[172:175], v[48:51]\n"
        "ds_read_b128 v[128:131], v165 offset:30720\n"
        "v_mfma_f32_16x16x32_bf16 v[68:71], v[188:191], v[176:179], v[68:71]\n"
        "ds_read_b128 v[236:239], v164 offset:16384\n"
        "v_mfma_f32_16x16x32_bf16 v[88:91], v[188:191], v[180:183], v[88:91]\n"
        "ds_read_b128 v[240:243], v164 offset:18432\n"
        "s_waitcnt lgkmcnt(3)\n"
        "v_mfma_f32_16x16x32_bf16 v[108:111], v[188:191], v[184:187], v[108:111]\n"
        "ds_read_b128 v[244:247], v164 offset:20480\n"
        "v_add_f32 v16, v16, v148\n"
        "v_mfma_f32_16x16x32_bf16 v[112:115], v[192:195], v[184:187], v[112:115]\n"
        "ds_read_b128 v[248:251], v164 offset:22528\n"
        "v_add_f32 v17, v17, v149\n"
        "v_mfma_f32_16x16x32_bf16 v[92:95], v[192:195], v[180:183], v[92:95]\n"
        "ds_read_b128 v[132:135], v165 offset:31744\n"
        "v_add_f32 v18, v18, v150\n"
        "v_mfma_f32_16x16x32_bf16 v[72:75], v[192:195], v[176:179], v[72:75]\n"
        "ds_read_b128 v[136:139], v165 offset:32768\n"
        "v_add_f32 v19, v19, v151\n"
        "v_mfma_f32_16x16x32_bf16 v[52:55], v[192:195], v[172:175], v[52:55]\n"
        "ds_read_b128 v[140:143], v165 offset:33792\n"
        "v_add_f32 v20, v20, v152\n"
        "v_mfma_f32_16x16x32_bf16 v[56:59], v[196:199], v[172:175], v[56:59]\n"
        "ds_read_b128 v[144:147], v165 offset:34816\n"
        "v_add_f32 v21, v21, v153\n"
        "v_mfma_f32_16x16x32_bf16 v[76:79], v[196:199], v[176:179], v[76:79]\n"
        "v_add_f32 v22, v22, v154\n"
        "v_add_f32 v23, v23, v155\n"
        "v_mfma_f32_16x16x32_bf16 v[96:99], v[196:199], v[180:183], v[96:99]\n"
        "v_add_f32 v24, v24, v208\n"
        "v_add_f32 v25, v25, v209\n"
        "v_mfma_f32_16x16x32_bf16 v[116:119], v[196:199], v[184:187], v[116:119]\n"
        "v_add_f32 v26, v26, v210\n"
        "v_add_f32 v27, v27, v211\n"
        "v_mfma_f32_16x16x32_bf16 v[120:123], v[200:203], v[184:187], v[120:123]\n"
        "v_add_f32 v28, v28, v15\n"
        "v_mfma_f32_16x16x32_bf16 v[100:103], v[200:203], v[180:183], v[100:103]\n"
        "ds_read_b128 v[148:151], v162 offset:3328\n"
        "v_mfma_f32_16x16x32_bf16 v[80:83], v[200:203], v[176:179], v[80:83]\n"
        "ds_read_b128 v[152:155], v162 offset:4352\n"
        "v_mfma_f32_16x16x32_bf16 v[60:63], v[200:203], v[172:175], v[60:63]\n"
        "ds_read_b128 v[208:211], v162 offset:5376\n"
        "v_mfma_f32_16x16x32_bf16 v[64:67], v[204:207], v[172:175], v[64:67]\n"
        "ds_read_b32 v15, v171 offset:3328\n"
        "v_mfma_f32_16x16x32_bf16 v[84:87], v[204:207], v[176:179], v[84:87]\n"
        "v_cndmask_b32 v158, v28, v24, s[46:47]\n"
        "v_fmac_f32 v158, 0, v16\n"
        "v_mfma_f32_16x16x32_bf16 v[104:107], v[204:207], v[180:183], v[104:107]\n"
        "v_fmac_f32 v158, 0, v17\n"
        "v_fmac_f32 v158, 0, v18\n"
        "v_mfma_f32_16x16x32_bf16 v[124:127], v[204:207], v[184:187], v[124:127]\n"
        "v_fmac_f32 v158, 0, v19\n"
        "v_fmac_f32 v158, 0, v20\n"
        "s_waitcnt lgkmcnt(4)\n"
        "v_mfma_f32_16x16x32_bf16 v[48:51], v[128:131], v[236:239], v[48:51]\n"
        "v_fmac_f32 v158, 0, v21\n"
        "v_fmac_f32 v158, 0, v22\n"
        "v_mfma_f32_16x16x32_bf16 v[68:71], v[128:131], v[240:243], v[68:71]\n"
        "v_fmac_f32 v158, 0, v23\n"
        "v_fmac_f32 v158, 0, v24\n"
        "v_mfma_f32_16x16x32_bf16 v[88:91], v[128:131], v[244:247], v[88:91]\n"
        "v_fmac_f32 v158, 0, v25\n"
        "v_fmac_f32 v158, 0, v26\n"
        "v_mfma_f32_16x16x32_bf16 v[108:111], v[128:131], v[248:251], v[108:111]\n"
        "s_waitcnt lgkmcnt(0)\n"
        "s_barrier\n"
        "v_fmac_f32 v158, 0, v27\n"
        "v_add_f32 v32, v32, v148\n"
        "v_mfma_f32_16x16x32_bf16 v[112:115], v[132:135], v[248:251], v[112:115]\n"
        "ds_read_b128 v[188:191], v165 offset:40960\n"
        "v_add_f32 v33, v33, v149\n"
        "v_mfma_f32_16x16x32_bf16 v[92:95], v[132:135], v[244:247], v[92:95]\n"
        "ds_read_b128 v[172:175], v163 offset:32768\n"
        "v_add_f32 v34, v34, v150\n"
        "v_mfma_f32_16x16x32_bf16 v[72:75], v[132:135], v[240:243], v[72:75]\n"
        "ds_read_b128 v[176:179], v163 offset:34816\n"
        "v_add_f32 v35, v35, v151\n"
        "v_mfma_f32_16x16x32_bf16 v[52:55], v[132:135], v[236:239], v[52:55]\n"
        "ds_read_b128 v[180:183], v163 offset:36864\n"
        "v_add_f32 v36, v36, v152\n"
        "v_mfma_f32_16x16x32_bf16 v[56:59], v[136:139], v[236:239], v[56:59]\n"
        "ds_read_b128 v[184:187], v163 offset:38912\n"
        "v_add_f32 v37, v37, v153\n"
        "v_mfma_f32_16x16x32_bf16 v[76:79], v[136:139], v[240:243], v[76:79]\n"
        "ds_read_b128 v[192:195], v165 offset:41984\n"
        "v_add_f32 v38, v38, v154\n"
        "v_mfma_f32_16x16x32_bf16 v[96:99], v[136:139], v[244:247], v[96:99]\n"
        "ds_read_b128 v[196:199], v165 offset:43008\n"
        "v_add_f32 v39, v39, v155\n"
        "v_mfma_f32_16x16x32_bf16 v[116:119], v[136:139], v[248:251], v[116:119]\n"
        "ds_read_b128 v[200:203], v165 offset:44032\n"
        "v_add_f32 v40, v40, v208\n"
        "v_mfma_f32_16x16x32_bf16 v[120:123], v[140:143], v[248:251], v[120:123]\n"
        "ds_read_b128 v[204:207], v165 offset:45056\n"
        "v_add_f32 v41, v41, v209\n"
        "v_mfma_f32_16x16x32_bf16 v[100:103], v[140:143], v[244:247], v[100:103]\n"
        "v_add_f32 v42, v42, v210\n"
        "v_add_f32 v43, v43, v211\n"
        "v_mfma_f32_16x16x32_bf16 v[80:83], v[140:143], v[240:243], v[80:83]\n"
        "v_add_f32 v44, v44, v15\n"
        "v_cndmask_b32 v159, v44, v40, s[46:47]\n"
        "v_mfma_f32_16x16x32_bf16 v[60:63], v[140:143], v[236:239], v[60:63]\n"
        "v_fmac_f32 v159, 0, v32\n"
        "v_fmac_f32 v159, 0, v33\n"
        "v_mfma_f32_16x16x32_bf16 v[64:67], v[144:147], v[236:239], v[64:67]\n"
        "v_fmac_f32 v159, 0, v34\n"
        "v_fmac_f32 v159, 0, v35\n"
        "v_mfma_f32_16x16x32_bf16 v[84:87], v[144:147], v[240:243], v[84:87]\n"
        "v_fmac_f32 v159, 0, v36\n"
        "v_fmac_f32 v159, 0, v37\n"
        "v_mfma_f32_16x16x32_bf16 v[104:107], v[144:147], v[244:247], v[104:107]\n"
        "v_fmac_f32 v159, 0, v38\n"
        "v_fmac_f32 v159, 0, v39\n"
        "v_mfma_f32_16x16x32_bf16 v[124:127], v[144:147], v[248:251], v[124:127]\n"
        "v_fmac_f32 v159, 0, v40\n"
        "v_fmac_f32 v159, 0, v41\n"
        "s_waitcnt lgkmcnt(0)\n"
        "v_mfma_f32_16x16x32_bf16 v[48:51], v[188:191], v[172:175], v[48:51]\n"
        "ds_read_b128 v[128:131], v165 offset:51200\n"
        "v_fmac_f32 v159, 0, v42\n"
        "v_mfma_f32_16x16x32_bf16 v[68:71], v[188:191], v[176:179], v[68:71]\n"
        "ds_read_b128 v[236:239], v164 offset:32768\n"
        "v_fmac_f32 v159, 0, v43\n"
        "v_mfma_f32_16x16x32_bf16 v[88:91], v[188:191], v[180:183], v[88:91]\n"
        "ds_read_b128 v[240:243], v164 offset:34816\n"
        "v_cmp_nle_f32 vcc, s41, v158\n"
        "v_mfma_f32_16x16x32_bf16 v[108:111], v[188:191], v[184:187], v[108:111]\n"
        "ds_read_b128 v[244:247], v164 offset:36864\n"
        "v_cmp_nge_f32 s[46:47], s42, v158\n"
        "v_mfma_f32_16x16x32_bf16 v[112:115], v[192:195], v[184:187], v[112:115]\n"
        "ds_read_b128 v[248:251], v164 offset:38912\n"
        "v_cmp_nle_f32 s[56:57], s41, v159\n"
        "v_mfma_f32_16x16x32_bf16 v[92:95], v[192:195], v[180:183], v[92:95]\n"
        "ds_read_b128 v[132:135], v165 offset:52224\n"
        "v_cmp_nge_f32 s[44:45], s42, v159\n"
        "v_mfma_f32_16x16x32_bf16 v[72:75], v[192:195], v[176:179], v[72:75]\n"
        "ds_read_b128 v[136:139], v165 offset:53248\n"
        "s_nop 0\n"
        "v_mfma_f32_16x16x32_bf16 v[52:55], v[192:195], v[172:175], v[52:55]\n"
        "ds_read_b128 v[140:143], v165 offset:54272\n"
        "s_or_b64 vcc, vcc, s[46:47]\n"
        "v_mfma_f32_16x16x32_bf16 v[56:59], v[196:199], v[172:175], v[56:59]\n"
        "ds_read_b128 v[144:147], v165 offset:55296\n"
        "s_or_b64 s[44:45], s[44:45], s[56:57]\n"
        "v_mfma_f32_16x16x32_bf16 v[76:79], v[196:199], v[176:179], v[76:79]\n"
        "s_or_b64 vcc, vcc, s[44:45]\n"
        "s_cselect_b32 s44, 0x40000000, 0\n"
        "v_mfma_f32_16x16x32_bf16 v[96:99], v[196:199], v[180:183], v[96:99]\n"
        "s_add_u32 s45, s44, 40960\n"
        "v_mov_b32 v15, s44\n"
        "v_mfma_f32_16x16x32_bf16 v[116:119], v[196:199], v[184:187], v[116:119]\n"
        "ds_write_b32 v171, v15\n"
        "v_mfma_f32_16x16x32_bf16 v[120:123], v[200:203], v[184:187], v[120:123]\n"
        "v_rcp_f32 v158, v158\n"
        "v_sub_f32 v156, 1.0, v156\n"
        "v_mfma_f32_16x16x32_bf16 v[100:103], v[200:203], v[180:183], v[100:103]\n"
        "v_mul_f32 v158, v158, v156\n"
        "v_mul_f32 v16, v158, v16\n"
        "v_mfma_f32_16x16x32_bf16 v[80:83], v[200:203], v[176:179], v[80:83]\n"
        "v_mul_f32 v17, v158, v17\n"
        "v_mul_f32 v18, v158, v18\n"
        "v_mfma_f32_16x16x32_bf16 v[60:63], v[200:203], v[172:175], v[60:63]\n"
        "v_mul_f32 v19, v158, v19\n"
        "v_mul_f32 v20, v158, v20\n"
        "v_mfma_f32_16x16x32_bf16 v[64:67], v[204:207], v[172:175], v[64:67]\n"
        "v_mul_f32 v21, v158, v21\n"
        "v_mul_f32 v22, v158, v22\n"
        "v_mfma_f32_16x16x32_bf16 v[84:87], v[204:207], v[176:179], v[84:87]\n"
        "v_mul_f32 v23, v158, v23\n"
        "v_mul_f32 v24, v158, v24\n"
        "v_mfma_f32_16x16x32_bf16 v[104:107], v[204:207], v[180:183], v[104:107]\n"
        "v_mul_f32 v25, v158, v25\n"
        "v_mul_f32 v26, v158, v26\n"
        "v_mfma_f32_16x16x32_bf16 v[124:127], v[204:207], v[184:187], v[124:127]\n"
        "v_mul_f32 v27, v158, v27\n"
        "v_cvt_pk_bf16_f32 v16, v16, v17\n"
        "s_waitcnt lgkmcnt(1)\n"
        "v_mfma_f32_16x16x32_bf16 v[48:51], v[128:131], v[236:239], v[48:51]\n"
        "v_cvt_pk_bf16_f32 v17, v18, v19\n"
        "v_cvt_pk_bf16_f32 v18, v20, v21\n"
        "v_mfma_f32_16x16x32_bf16 v[68:71], v[128:131], v[240:243], v[68:71]\n"
        "v_cvt_pk_bf16_f32 v19, v22, v23\n"
        "v_cvt_pk_bf16_f32 v20, v24, v25\n"
        "v_mfma_f32_16x16x32_bf16 v[88:91], v[128:131], v[244:247], v[88:91]\n"
        "v_cvt_pk_bf16_f32 v21, v26, v27\n"
        "v_mfma_f32_16x16x32_bf16 v[108:111], v[128:131], v[248:251], v[108:111]\n"
        "s_waitcnt lgkmcnt(0)\n"
        "s_barrier\n"
        "buffer_store_dwordx4 v[16:19], v252, s[52:55], s44 offen sc1\n"
        "v_mfma_f32_16x16x32_bf16 v[112:115], v[132:135], v[248:251], v[112:115]\n"
        "ds_read_b128 v[188:191], v165\n"
        "v_mfma_f32_16x16x32_bf16 v[92:95], v[132:135], v[244:247], v[92:95]\n"
        "ds_read_b128 v[172:175], v163\n"
        "v_mfma_f32_16x16x32_bf16 v[72:75], v[132:135], v[240:243], v[72:75]\n"
        "ds_read_b128 v[176:179], v163 offset:2048\n"
        "v_mfma_f32_16x16x32_bf16 v[52:55], v[132:135], v[236:239], v[52:55]\n"
        "ds_read_b128 v[180:183], v163 offset:4096\n"
        "v_mfma_f32_16x16x32_bf16 v[56:59], v[136:139], v[236:239], v[56:59]\n"
        "ds_read_b128 v[184:187], v163 offset:6144\n"
        "v_mfma_f32_16x16x32_bf16 v[76:79], v[136:139], v[240:243], v[76:79]\n"
        "ds_read_b128 v[192:195], v165 offset:1024\n"
        "v_mfma_f32_16x16x32_bf16 v[96:99], v[136:139], v[244:247], v[96:99]\n"
        "ds_read_b128 v[196:199], v165 offset:2048\n"
        "v_mfma_f32_16x16x32_bf16 v[116:119], v[136:139], v[248:251], v[116:119]\n"
        "ds_read_b128 v[200:203], v165 offset:3072\n"
        "v_mfma_f32_16x16x32_bf16 v[120:123], v[140:143], v[248:251], v[120:123]\n"
        "ds_read_b128 v[204:207], v165 offset:4096\n"
        "v_mfma_f32_16x16x32_bf16 v[100:103], v[140:143], v[244:247], v[100:103]\n"
        "buffer_store_dwordx2 v[20:21], v253, s[52:55], s44 offen sc1\n"
        "v_mfma_f32_16x16x32_bf16 v[80:83], v[140:143], v[240:243], v[80:83]\n"
        "v_rcp_f32 v159, v159\n"
        "v_sub_f32 v157, 1.0, v157\n"
        "v_mfma_f32_16x16x32_bf16 v[60:63], v[140:143], v[236:239], v[60:63]\n"
        "v_mul_f32 v159, v159, v157\n"
        "v_mul_f32 v32, v159, v32\n"
        "v_mfma_f32_16x16x32_bf16 v[64:67], v[144:147], v[236:239], v[64:67]\n"
        "v_mul_f32 v33, v159, v33\n"
        "v_mul_f32 v34, v159, v34\n"
        "v_mfma_f32_16x16x32_bf16 v[84:87], v[144:147], v[240:243], v[84:87]\n"
        "v_mul_f32 v35, v159, v35\n"
        "v_mul_f32 v36, v159, v36\n"
        "v_mfma_f32_16x16x32_bf16 v[104:107], v[144:147], v[244:247], v[104:107]\n"
        "v_mul_f32 v37, v159, v37\n"
        "v_mul_f32 v38, v159, v38\n"
        "v_mfma_f32_16x16x32_bf16 v[124:127], v[144:147], v[248:251], v[124:127]\n"
        "v_mul_f32 v39, v159, v39\n"
        "v_mul_f32 v40, v159, v40\n"
        "s_waitcnt lgkmcnt(0)\n"
        "v_mfma_f32_16x16x32_bf16 v[48:51], v[188:191], v[172:175], v[48:51]\n"
        "ds_read_b128 v[128:131], v165 offset:10240\n"
        "v_mul_f32 v41, v159, v41\n"
        "v_mfma_f32_16x16x32_bf16 v[68:71], v[188:191], v[176:179], v[68:71]\n"
        "ds_read_b128 v[236:239], v164\n"
        "v_mul_f32 v42, v159, v42\n"
        "v_mfma_f32_16x16x32_bf16 v[88:91], v[188:191], v[180:183], v[88:91]\n"
        "ds_read_b128 v[240:243], v164 offset:2048\n"
        "v_mul_f32 v43, v159, v43\n"
        "v_mfma_f32_16x16x32_bf16 v[108:111], v[188:191], v[184:187], v[108:111]\n"
        "ds_read_b128 v[244:247], v164 offset:4096\n"
        "v_cvt_pk_bf16_f32 v32, v32, v33\n"
        "v_mfma_f32_16x16x32_bf16 v[112:115], v[192:195], v[184:187], v[112:115]\n"
        "ds_read_b128 v[248:251], v164 offset:6144\n"
        "v_cvt_pk_bf16_f32 v33, v34, v35\n"
        "v_mfma_f32_16x16x32_bf16 v[92:95], v[192:195], v[180:183], v[92:95]\n"
        "ds_read_b128 v[132:135], v165 offset:11264\n"
        "v_cvt_pk_bf16_f32 v34, v36, v37\n"
        "v_mfma_f32_16x16x32_bf16 v[72:75], v[192:195], v[176:179], v[72:75]\n"
        "ds_read_b128 v[136:139], v165 offset:12288\n"
        "v_cvt_pk_bf16_f32 v35, v38, v39\n"
        "v_mfma_f32_16x16x32_bf16 v[52:55], v[192:195], v[172:175], v[52:55]\n"
        "ds_read_b128 v[140:143], v165 offset:13312\n"
        "v_cvt_pk_bf16_f32 v36, v40, v41\n"
        "v_mfma_f32_16x16x32_bf16 v[56:59], v[196:199], v[172:175], v[56:59]\n"
        "ds_read_b128 v[144:147], v165 offset:14336\n"
        "v_cvt_pk_bf16_f32 v37, v42, v43\n"
        "v_mfma_f32_16x16x32_bf16 v[76:79], v[196:199], v[176:179], v[76:79]\n"
        "buffer_store_dwordx4 v[32:35], v252, s[52:55], s45 offen sc1\n"
        "v_mfma_f32_16x16x32_bf16 v[96:99], v[196:199], v[180:183], v[96:99]\n"
        "v_mfma_f32_16x16x32_bf16 v[116:119], v[196:199], v[184:187], v[116:119]\n"
        "buffer_store_dwordx2 v[36:37], v253, s[52:55], s45 offen sc1\n"
        "v_mfma_f32_16x16x32_bf16 v[120:123], v[200:203], v[184:187], v[120:123]\n"
        "v_mfma_f32_16x16x32_bf16 v[100:103], v[200:203], v[180:183], v[100:103]\n"
        "ds_write_b64 v170, v[226:227] offset:2560\n"
        "v_mfma_f32_16x16x32_bf16 v[80:83], v[200:203], v[176:179], v[80:83]\n"
        "v_mfma_f32_16x16x32_bf16 v[60:63], v[200:203], v[172:175], v[60:63]\n"
        "ds_write_b64 v170, v[228:229] offset:3072\n"
        "v_mfma_f32_16x16x32_bf16 v[64:67], v[204:207], v[172:175], v[64:67]\n"
        "v_mfma_f32_16x16x32_bf16 v[84:87], v[204:207], v[176:179], v[84:87]\n"
        "ds_write_b64 v170, v[230:231] offset:3584\n"
        "v_mfma_f32_16x16x32_bf16 v[104:107], v[204:207], v[180:183], v[104:107]\n"
        "v_mfma_f32_16x16x32_bf16 v[124:127], v[204:207], v[184:187], v[124:127]\n"
        "ds_write_b64 v170, v[232:233] offset:4096\n"
        "s_waitcnt lgkmcnt(4)\n"
        "v_mfma_f32_16x16x32_bf16 v[48:51], v[128:131], v[236:239], v[48:51]\n"
        "v_mfma_f32_16x16x32_bf16 v[68:71], v[128:131], v[240:243], v[68:71]\n"
        "ds_write_b64 v170, v[234:235] offset:4608\n"
        "v_mfma_f32_16x16x32_bf16 v[88:91], v[128:131], v[244:247], v[88:91]\n"
        "s_lshr_b32 s60, %[b], 3\n"
        "v_mfma_f32_16x16x32_bf16 v[108:111], v[128:131], v[248:251], v[108:111]\n"
        "s_waitcnt lgkmcnt(0)\n"
        "s_barrier\n"
        "s_and_b32 s60, s60, 7\n"
        "v_mfma_f32_16x16x32_bf16 v[112:115], v[132:135], v[248:251], v[112:115]\n"
        "ds_read_b128 v[188:191], v165 offset:20480\n"
        "v_mfma_f32_16x16x32_bf16 v[92:95], v[132:135], v[244:247], v[92:95]\n"
        "ds_read_b128 v[172:175], v163 offset:16384\n"
        "v_mfma_f32_16x16x32_bf16 v[72:75], v[132:135], v[240:243], v[72:75]\n"
        "ds_read_b128 v[176:179], v163 offset:18432\n"
        "v_mfma_f32_16x16x32_bf16 v[52:55], v[132:135], v[236:239], v[52:55]\n"
        "ds_read_b128 v[180:183], v163 offset:20480\n"
        "v_mfma_f32_16x16x32_bf16 v[56:59], v[136:139], v[236:239], v[56:59]\n"
        "ds_read_b128 v[184:187], v163 offset:22528\n"
        "v_mfma_f32_16x16x32_bf16 v[76:79], v[136:139], v[240:243], v[76:79]\n"
        "ds_read_b128 v[192:195], v165 offset:21504\n"
        "v_mfma_f32_16x16x32_bf16 v[96:99], v[136:139], v[244:247], v[96:99]\n"
        "ds_read_b128 v[196:199], v165 offset:22528\n"
        "v_mfma_f32_16x16x32_bf16 v[116:119], v[136:139], v[248:251], v[116:119]\n"
        "ds_read_b128 v[200:203], v165 offset:23552\n"
        "v_mfma_f32_16x16x32_bf16 v[120:123], v[140:143], v[248:251], v[120:123]\n"
        "ds_read_b128 v[204:207], v165 offset:24576\n"
        "v_mfma_f32_16x16x32_bf16 v[100:103], v[140:143], v[244:247], v[100:103]\n"
        "s_mul_i32 s60, s60, 2\n"
        "v_mfma_f32_16x16x32_bf16 v[80:83], v[140:143], v[240:243], v[80:83]\n"
        "s_and_b32 s61, %[b], 7\n"
        "v_mfma_f32_16x16x32_bf16 v[60:63], v[140:143], v[236:239], v[60:63]\n"
        "s_and_b32 s62, s61, 1\n"
        "v_mfma_f32_16x16x32_bf16 v[64:67], v[144:147], v[236:239], v[64:67]\n"
        "s_add_u32 s62, s60, s62\n"
        "v_mfma_f32_16x16x32_bf16 v[84:87], v[144:147], v[240:243], v[84:87]\n"
        "s_mul_i32 s60, s62, 4\n"
        "v_mfma_f32_16x16x32_bf16 v[104:107], v[144:147], v[244:247], v[104:107]\n"
        "s_lshr_b32 s63, %[b], 6\n"
        "v_mfma_f32_16x16x32_bf16 v[124:127], v[144:147], v[248:251], v[124:127]\n"
        "s_add_u32 s60, s60, s63\n"
        "s_waitcnt lgkmcnt(0)\n"
        "v_mfma_f32_16x16x32_bf16 v[48:51], v[188:191], v[172:175], v[48:51]\n"
        "ds_read_b128 v[128:131], v165 offset:30720\n"
        "v_mfma_f32_16x16x32_bf16 v[68:71], v[188:191], v[176:179], v[68:71]\n"
        "ds_read_b128 v[236:239], v164 offset:16384\n"
        "v_mfma_f32_16x16x32_bf16 v[88:91], v[188:191], v[180:183], v[88:91]\n"
        "ds_read_b128 v[240:243], v164 offset:18432\n"
        "v_mfma_f32_16x16x32_bf16 v[108:111], v[188:191], v[184:187], v[108:111]\n"
        "ds_read_b128 v[244:247], v164 offset:20480\n"
        "v_mfma_f32_16x16x32_bf16 v[112:115], v[192:195], v[184:187], v[112:115]\n"
        "ds_read_b128 v[248:251], v164 offset:22528\n"
        "v_mfma_f32_16x16x32_bf16 v[92:95], v[192:195], v[180:183], v[92:95]\n"
        "ds_read_b128 v[132:135], v165 offset:31744\n"
        "v_mfma_f32_16x16x32_bf16 v[72:75], v[192:195], v[176:179], v[72:75]\n"
        "ds_read_b128 v[136:139], v165 offset:32768\n"
        "v_mfma_f32_16x16x32_bf16 v[52:55], v[192:195], v[172:175], v[52:55]\n"
        "ds_read_b128 v[140:143], v165 offset:33792\n"
        "v_mfma_f32_16x16x32_bf16 v[56:59], v[196:199], v[172:175], v[56:59]\n"
        "ds_read_b128 v[144:147], v165 offset:34816\n"
        "v_mfma_f32_16x16x32_bf16 v[76:79], v[196:199], v[176:179], v[76:79]\n"
        "s_mul_i32 s60, s60, 4\n"
        "v_mfma_f32_16x16x32_bf16 v[96:99], v[196:199], v[180:183], v[96:99]\n"
        "s_lshr_b32 s61, s61, 1\n"
        "v_mfma_f32_16x16x32_bf16 v[116:119], v[196:199], v[184:187], v[116:119]\n"
        "s_add_u32 s64, s60, s61\n"
        "v_mfma_f32_16x16x32_bf16 v[120:123], v[200:203], v[184:187], v[120:123]\n"
        "s_mul_i32 s64, s64, 0xa000\n"
        "v_mfma_f32_16x16x32_bf16 v[100:103], v[200:203], v[180:183], v[100:103]\n"
        "s_add_u32 s64, s64, 0xf1a080\n"
        "v_mfma_f32_16x16x32_bf16 v[80:83], v[200:203], v[176:179], v[80:83]\n"
        "s_add_u32 s65, s64, 0x27fc\n"
        "v_mfma_f32_16x16x32_bf16 v[60:63], v[200:203], v[172:175], v[60:63]\n"
        "v_add_u32 v22, 1, %[ln]\n"
        "v_mfma_f32_16x16x32_bf16 v[64:67], v[204:207], v[172:175], v[64:67]\n"
        "v_xor_b32 v22, s61, v22\n"
        "v_mfma_f32_16x16x32_bf16 v[84:87], v[204:207], v[176:179], v[84:87]\n"
        "v_mul_u32_u24 v23, 0x2800, v22\n"
        "v_mfma_f32_16x16x32_bf16 v[104:107], v[204:207], v[180:183], v[104:107]\n"
        "v_add_u32 v23, s65, v23\n"
        "v_mfma_f32_16x16x32_bf16 v[124:127], v[204:207], v[184:187], v[124:127]\n"
        "v_mov_b32 v22, 0x7f000000\n"
        "s_waitcnt lgkmcnt(0)\n"
        "v_mfma_f32_16x16x32_bf16 v[48:51], v[128:131], v[236:239], v[48:51]\n"
        "v_cmp_gt_u32 vcc, 3, %[ln]\n"
        "v_mfma_f32_16x16x32_bf16 v[68:71], v[128:131], v[240:243], v[68:71]\n"
        "s_nop 1\n"
        "v_mfma_f32_16x16x32_bf16 v[88:91], v[128:131], v[244:247], v[88:91]\n"
        "v_cndmask_b32 v8, v22, v23, vcc\n"
        "v_mfma_f32_16x16x32_bf16 v[108:111], v[128:131], v[248:251], v[108:111]\n"
        "s_waitcnt lgkmcnt(0)\n"
        "s_barrier\n"
        "s_mul_i32 s65, %[w], 0x400\n"
        "v_mfma_f32_16x16x32_bf16 v[112:115], v[132:135], v[248:251], v[112:115]\n"
        "ds_read_b128 v[188:191], v165 offset:40960\n"
        "v_mfma_f32_16x16x32_bf16 v[92:95], v[132:135], v[244:247], v[92:95]\n"
        "ds_read_b128 v[172:175], v163 offset:32768\n"
        "v_mfma_f32_16x16x32_bf16 v[72:75], v[132:135], v[240:243], v[72:75]\n"
        "ds_read_b128 v[176:179], v163 offset:34816\n"
        "v_mfma_f32_16x16x32_bf16 v[52:55], v[132:135], v[236:239], v[52:55]\n"
        "ds_read_b128 v[180:183], v163 offset:36864\n"
        "v_mfma_f32_16x16x32_bf16 v[56:59], v[136:139], v[236:239], v[56:59]\n"
        "ds_read_b128 v[184:187], v163 offset:38912\n"
        "v_mfma_f32_16x16x32_bf16 v[76:79], v[136:139], v[240:243], v[76:79]\n"
        "ds_read_b128 v[192:195], v165 offset:41984\n"
        "v_mfma_f32_16x16x32_bf16 v[96:99], v[136:139], v[244:247], v[96:99]\n"
        "ds_read_b128 v[196:199], v165 offset:43008\n"
        "v_mfma_f32_16x16x32_bf16 v[116:119], v[136:139], v[248:251], v[116:119]\n"
        "ds_read_b128 v[200:203], v165 offset:44032\n"
        "v_mfma_f32_16x16x32_bf16 v[120:123], v[140:143], v[248:251], v[120:123]\n"
        "ds_read_b128 v[204:207], v165 offset:45056\n"
        "v_mfma_f32_16x16x32_bf16 v[100:103], v[140:143], v[244:247], v[100:103]\n"
        "s_add_u32 s65, s64, s65\n"
        "v_mfma_f32_16x16x32_bf16 v[80:83], v[140:143], v[240:243], v[80:83]\n"
        "v_lshl_add_u32 v9, %[ln], 4, s65\n"
        "v_mfma_f32_16x16x32_bf16 v[60:63], v[140:143], v[236:239], v[60:63]\n"
        "s_mul_i32 s65, %[w], 0x1000\n"
        "v_mfma_f32_16x16x32_bf16 v[64:67], v[144:147], v[236:239], v[64:67]\n"
        "v_lshl_add_u32 v10, %[ln], 4, s65\n"
        "v_mfma_f32_16x16x32_bf16 v[84:87], v[144:147], v[240:243], v[84:87]\n"
        "s_add_u32 s65, %[w], 9\n"
        "v_mfma_f32_16x16x32_bf16 v[104:107], v[144:147], v[244:247], v[104:107]\n"
        "s_cmp_eq_u32 %[w], 0\n"
        "v_mfma_f32_16x16x32_bf16 v[124:127], v[144:147], v[248:251], v[124:127]\n"
        "s_cselect_b32 s64, 4, s65\n"
        "s_waitcnt lgkmcnt(0)\n"
        "v_mfma_f32_16x16x32_bf16 v[48:51], v[188:191], v[172:175], v[48:51]\n"
        "ds_read_b128 v[128:131], v165 offset:51200\n"
        "v_mfma_f32_16x16x32_bf16 v[68:71], v[188:191], v[176:179], v[68:71]\n"
        "ds_read_b128 v[236:239], v164 offset:32768\n"
        "v_mfma_f32_16x16x32_bf16 v[88:91], v[188:191], v[180:183], v[88:91]\n"
        "ds_read_b128 v[240:243], v164 offset:34816\n"
        ".Lk_pad9:\n"
        "s_cbranch_execz .Lk_ret\n"
        "v_mfma_f32_16x16x32_bf16 v[108:111], v[188:191], v[184:187], v[108:111]\n"
        "ds_read_b128 v[244:247], v164 offset:36864\n"
        "v_mfma_f32_16x16x32_bf16 v[112:115], v[192:195], v[184:187], v[112:115]\n"
        "ds_read_b128 v[248:251], v164 offset:38912\n"
        "v_mfma_f32_16x16x32_bf16 v[92:95], v[192:195], v[180:183], v[92:95]\n"
        "ds_read_b128 v[132:135], v165 offset:52224\n"
        "v_mfma_f32_16x16x32_bf16 v[72:75], v[192:195], v[176:179], v[72:75]\n"
        "ds_read_b128 v[136:139], v165 offset:53248\n"
        "v_mfma_f32_16x16x32_bf16 v[52:55], v[192:195], v[172:175], v[52:55]\n"
        "ds_read_b128 v[140:143], v165 offset:54272\n"
        "v_mfma_f32_16x16x32_bf16 v[56:59], v[196:199], v[172:175], v[56:59]\n"
        "ds_read_b128 v[144:147], v165 offset:55296\n"
        "v_mfma_f32_16x16x32_bf16 v[76:79], v[196:199], v[176:179], v[76:79]\n"
        "s_mul_i32 s64, s64, 0x1000\n"
        "v_mfma_f32_16x16x32_bf16 v[96:99], v[196:199], v[180:183], v[96:99]\n"
        "v_lshl_add_u32 v11, %[ln], 4, s64\n"
        "v_mfma_f32_16x16x32_bf16 v[116:119], v[196:199], v[184:187], v[116:119]\n"
        "s_mul_i32 s62, s62, 2\n"
        "v_mfma_f32_16x16x32_bf16 v[120:123], v[200:203], v[184:187], v[120:123]\n"
        "s_lshr_b32 s64, s61, 1\n"
        "v_mfma_f32_16x16x32_bf16 v[100:103], v[200:203], v[180:183], v[100:103]\n"
        "s_add_u32 s64, s62, s64\n"
        "v_mfma_f32_16x16x32_bf16 v[80:83], v[200:203], v[176:179], v[80:83]\n"
        "s_mul_i32 s64, s64, 64\n"
        "v_mfma_f32_16x16x32_bf16 v[60:63], v[200:203], v[172:175], v[60:63]\n"
        "s_mul_i32 s64, s64, 0x500\n"
        "v_mfma_f32_16x16x32_bf16 v[64:67], v[204:207], v[172:175], v[64:67]\n"
        "s_mul_i32 s63, s63, 2\n"
        "v_mfma_f32_16x16x32_bf16 v[84:87], v[204:207], v[176:179], v[84:87]\n"
        "s_and_b32 s62, s61, 1\n"
        "v_mfma_f32_16x16x32_bf16 v[104:107], v[204:207], v[180:183], v[104:107]\n"
        "s_add_u32 s62, s63, s62\n"
        "v_mfma_f32_16x16x32_bf16 v[124:127], v[204:207], v[184:187], v[124:127]\n"
        "s_mul_i32 s62, s62, 0xa0\n"
        "s_waitcnt lgkmcnt(0)\n"
        "v_mfma_f32_16x16x32_bf16 v[48:51], v[128:131], v[236:239], v[48:51]\n"
        "s_add_u32 s62, s64, s62\n"
        "v_mfma_f32_16x16x32_bf16 v[68:71], v[128:131], v[240:243], v[68:71]\n"
        "s_mul_i32 s64, %[w], 32\n"
        "v_mfma_f32_16x16x32_bf16 v[88:91], v[128:131], v[244:247], v[88:91]\n"
        "s_add_u32 s63, s62, s64\n"
        "v_mfma_f32_16x16x32_bf16 v[108:111], v[128:131], v[248:251], v[108:111]\n"
        "s_waitcnt vmcnt(0) lgkmcnt(0)\n"
        "s_barrier\n"
        "v_lshrrev_b32 v23, 4, %[ln]\n"
        "v_mfma_f32_16x16x32_bf16 v[112:115], v[132:135], v[248:251], v[112:115]\n"
        "ds_read_b128 v[188:191], v165\n"
        "v_mfma_f32_16x16x32_bf16 v[92:95], v[132:135], v[244:247], v[92:95]\n"
        "ds_read_b128 v[172:175], v163\n"
        "v_mfma_f32_16x16x32_bf16 v[72:75], v[132:135], v[240:243], v[72:75]\n"
        "ds_read_b128 v[176:179], v163 offset:2048\n"
        "v_mfma_f32_16x16x32_bf16 v[52:55], v[132:135], v[236:239], v[52:55]\n"
        "ds_read_b128 v[180:183], v163 offset:4096\n"
        "v_mfma_f32_16x16x32_bf16 v[56:59], v[136:139], v[236:239], v[56:59]\n"
        "ds_read_b128 v[184:187], v163 offset:6144\n"
        "v_mfma_f32_16x16x32_bf16 v[76:79], v[136:139], v[240:243], v[76:79]\n"
        "ds_read_b128 v[192:195], v165 offset:1024\n"
        "v_mfma_f32_16x16x32_bf16 v[96:99], v[136:139], v[244:247], v[96:99]\n"
        "ds_read_b128 v[196:199], v165 offset:2048\n"
        "v_mfma_f32_16x16x32_bf16 v[116:119], v[136:139], v[248:251], v[116:119]\n"
        "ds_read_b128 v[200:203], v165 offset:3072\n"
        "v_mfma_f32_16x16x32_bf16 v[120:123], v[140:143], v[248:251], v[120:123]\n"
        "ds_read_b128 v[204:207], v165 offset:4096\n"
        "v_mfma_f32_16x16x32_bf16 v[100:103], v[140:143], v[244:247], v[100:103]\n"
        "v_and_b32 v23, 1, v23\n"
        "v_mfma_f32_16x16x32_bf16 v[80:83], v[140:143], v[240:243], v[80:83]\n"
        "v_and_b32 v22, 15, %[ln]\n"
        "v_mfma_f32_16x16x32_bf16 v[60:63], v[140:143], v[236:239], v[60:63]\n"
        "v_lshrrev_b32 v24, 5, %[ln]\n"
        "v_mfma_f32_16x16x32_bf16 v[64:67], v[144:147], v[236:239], v[64:67]\n"
        "v_mul_u32_u24 v12, 0x5000, v23\n"
        "v_mfma_f32_16x16x32_bf16 v[84:87], v[144:147], v[240:243], v[84:87]\n"
        "v_mul_u32_u24 v25, 0x500, v22\n"
        "v_mfma_f32_16x16x32_bf16 v[104:107], v[144:147], v[244:247], v[104:107]\n"
        "v_add_u32 v12, v12, v25\n"
        "v_mfma_f32_16x16x32_bf16 v[124:127], v[144:147], v[248:251], v[124:127]\n"
        "v_lshl_add_u32 v12, v24, 4, v12\n"
        "s_waitcnt lgkmcnt(0)\n"
        "v_mfma_f32_16x16x32_bf16 v[48:51], v[188:191], v[172:175], v[48:51]\n"
        "ds_read_b128 v[128:131], v165 offset:10240\n"
        "v_mfma_f32_16x16x32_bf16 v[68:71], v[188:191], v[176:179], v[68:71]\n"
        "ds_read_b128 v[236:239], v164\n"
        "v_mfma_f32_16x16x32_bf16 v[88:91], v[188:191], v[180:183], v[88:91]\n"
        "ds_read_b128 v[240:243], v164 offset:2048\n"
        "v_mfma_f32_16x16x32_bf16 v[108:111], v[188:191], v[184:187], v[108:111]\n"
        "ds_read_b128 v[244:247], v164 offset:4096\n"
        "v_mfma_f32_16x16x32_bf16 v[112:115], v[192:195], v[184:187], v[112:115]\n"
        "ds_read_b128 v[248:251], v164 offset:6144\n"
        "v_mfma_f32_16x16x32_bf16 v[92:95], v[192:195], v[180:183], v[92:95]\n"
        "ds_read_b128 v[132:135], v165 offset:11264\n"
        "v_mfma_f32_16x16x32_bf16 v[72:75], v[192:195], v[176:179], v[72:75]\n"
        "ds_read_b128 v[136:139], v165 offset:12288\n"
        "v_mfma_f32_16x16x32_bf16 v[52:55], v[192:195], v[172:175], v[52:55]\n"
        "ds_read_b128 v[140:143], v165 offset:13312\n"
        "v_mfma_f32_16x16x32_bf16 v[56:59], v[196:199], v[172:175], v[56:59]\n"
        "ds_read_b128 v[144:147], v165 offset:14336\n"
        "v_mfma_f32_16x16x32_bf16 v[76:79], v[196:199], v[176:179], v[76:79]\n"
        "v_add_u32 v12, s63, v12\n"
        "v_mfma_f32_16x16x32_bf16 v[96:99], v[196:199], v[180:183], v[96:99]\n"
        "s_add_u32 s64, s64, 0x9fe0\n"
        "v_mfma_f32_16x16x32_bf16 v[116:119], v[196:199], v[184:187], v[116:119]\n"
        "s_cmp_eq_u32 %[w], 0\n"
        "v_mfma_f32_16x16x32_bf16 v[120:123], v[200:203], v[184:187], v[120:123]\n"
        "s_cselect_b32 s63, 0x80, s64\n"
        "v_mfma_f32_16x16x32_bf16 v[100:103], v[200:203], v[180:183], v[100:103]\n"
        "s_add_u32 s63, s62, s63\n"
        "v_mfma_f32_16x16x32_bf16 v[80:83], v[200:203], v[176:179], v[80:83]\n"
        "v_mul_u32_u24 v13, 0x5000, v23\n"
        "v_mfma_f32_16x16x32_bf16 v[60:63], v[200:203], v[172:175], v[60:63]\n"
        "v_mul_u32_u24 v25, 0x500, v22\n"
        "v_mfma_f32_16x16x32_bf16 v[64:67], v[204:207], v[172:175], v[64:67]\n"
        "v_add_u32 v13, v13, v25\n"
        "v_mfma_f32_16x16x32_bf16 v[84:87], v[204:207], v[176:179], v[84:87]\n"
        "v_lshl_add_u32 v13, v24, 4, v13\n"
        "v_mfma_f32_16x16x32_bf16 v[104:107], v[204:207], v[180:183], v[104:107]\n"
        "v_add_u32 v13, s63, v13\n"
        "v_mfma_f32_16x16x32_bf16 v[124:127], v[204:207], v[184:187], v[124:127]\n"
        "s_add_u32 s62, s62, 0xa000\n"
        "s_waitcnt lgkmcnt(0)\n"
        "v_mfma_f32_16x16x32_bf16 v[48:51], v[128:131], v[236:239], v[48:51]\n"
        "s_add_u32 s63, %[w], 3\n"
        "v_mfma_f32_16x16x32_bf16 v[68:71], v[128:131], v[240:243], v[68:71]\n"
        "s_mul_i32 s63, s63, 32\n"
        "v_mfma_f32_16x16x32_bf16 v[88:91], v[128:131], v[244:247], v[88:91]\n"
        "s_add_u32 s63, s62, s63\n"
        "v_mfma_f32_16x16x32_bf16 v[108:111], v[128:131], v[248:251], v[108:111]\n"
        "s_waitcnt lgkmcnt(0)\n"
        "s_barrier\n"
        "s_lshr_b32 s62, %[w], 1\n"
        "v_mfma_f32_16x16x32_bf16 v[112:115], v[132:135], v[248:251], v[112:115]\n"
        "ds_read_b128 v[188:191], v165 offset:20480\n"
        "v_mfma_f32_16x16x32_bf16 v[92:95], v[132:135], v[244:247], v[92:95]\n"
        "ds_read_b128 v[172:175], v163 offset:16384\n"
        "v_mfma_f32_16x16x32_bf16 v[72:75], v[132:135], v[240:243], v[72:75]\n"
        "ds_read_b128 v[176:179], v163 offset:18432\n"
        "v_mfma_f32_16x16x32_bf16 v[52:55], v[132:135], v[236:239], v[52:55]\n"
        "ds_read_b128 v[180:183], v163 offset:20480\n"
        "v_mfma_f32_16x16x32_bf16 v[56:59], v[136:139], v[236:239], v[56:59]\n"
        "ds_read_b128 v[184:187], v163 offset:22528\n"
        "v_mfma_f32_16x16x32_bf16 v[76:79], v[136:139], v[240:243], v[76:79]\n"
        "ds_read_b128 v[192:195], v165 offset:21504\n"
        "v_mfma_f32_16x16x32_bf16 v[96:99], v[136:139], v[244:247], v[96:99]\n"
        "ds_read_b128 v[196:199], v165 offset:22528\n"
        "v_mfma_f32_16x16x32_bf16 v[116:119], v[136:139], v[248:251], v[116:119]\n"
        "ds_read_b128 v[200:203], v165 offset:23552\n"
        "v_mfma_f32_16x16x32_bf16 v[120:123], v[140:143], v[248:251], v[120:123]\n"
        "ds_read_b128 v[204:207], v165 offset:24576\n"
        "v_mfma_f32_16x16x32_bf16 v[100:103], v[140:143], v[244:247], v[100:103]\n"
        "s_mul_i32 s62, s62, 0x40000000\n"
        "v_mfma_f32_16x16x32_bf16 v[80:83], v[140:143], v[240:243], v[80:83]\n"
        "s_add_u32 s62, s63, s62\n"
        "v_mfma_f32_16x16x32_bf16 v[60:63], v[140:143], v[236:239], v[60:63]\n"
        "v_mul_u32_u24 v14, 0x5000, v23\n"
        "v_mfma_f32_16x16x32_bf16 v[64:67], v[144:147], v[236:239], v[64:67]\n"
        "v_mul_u32_u24 v25, 0x500, v22\n"
        "v_mfma_f32_16x16x32_bf16 v[84:87], v[144:147], v[240:243], v[84:87]\n"
        "v_add_u32 v14, v14, v25\n"
        "v_mfma_f32_16x16x32_bf16 v[104:107], v[144:147], v[244:247], v[104:107]\n"
        "v_lshl_add_u32 v14, v24, 4, v14\n"
        "v_mfma_f32_16x16x32_bf16 v[124:127], v[144:147], v[248:251], v[124:127]\n"
        "v_add_u32 v14, s62, v14\n"
        "s_waitcnt lgkmcnt(0)\n"
        "v_mfma_f32_16x16x32_bf16 v[48:51], v[188:191], v[172:175], v[48:51]\n"
        "ds_read_b128 v[128:131], v165 offset:30720\n"
        "v_mfma_f32_16x16x32_bf16 v[68:71], v[188:191], v[176:179], v[68:71]\n"
        "ds_read_b128 v[236:239], v164 offset:16384\n"
        "v_mfma_f32_16x16x32_bf16 v[88:91], v[188:191], v[180:183], v[88:91]\n"
        "ds_read_b128 v[240:243], v164 offset:18432\n"
        "v_mfma_f32_16x16x32_bf16 v[108:111], v[188:191], v[184:187], v[108:111]\n"
        "ds_read_b128 v[244:247], v164 offset:20480\n"
        "v_mfma_f32_16x16x32_bf16 v[112:115], v[192:195], v[184:187], v[112:115]\n"
        "ds_read_b128 v[248:251], v164 offset:22528\n"
        "v_mfma_f32_16x16x32_bf16 v[92:95], v[192:195], v[180:183], v[92:95]\n"
        "ds_read_b128 v[132:135], v165 offset:31744\n"
        "v_mfma_f32_16x16x32_bf16 v[72:75], v[192:195], v[176:179], v[72:75]\n"
        "ds_read_b128 v[136:139], v165 offset:32768\n"
        "v_mfma_f32_16x16x32_bf16 v[52:55], v[192:195], v[172:175], v[52:55]\n"
        "ds_read_b128 v[140:143], v165 offset:33792\n"
        "v_mfma_f32_16x16x32_bf16 v[56:59], v[196:199], v[172:175], v[56:59]\n"
        "ds_read_b128 v[144:147], v165 offset:34816\n"
        "v_mfma_f32_16x16x32_bf16 v[76:79], v[196:199], v[176:179], v[76:79]\n"
        "s_add_u32 s60, s60, %[w]\n"
        "v_mfma_f32_16x16x32_bf16 v[96:99], v[196:199], v[180:183], v[96:99]\n"
        "s_mul_i32 s60, s60, 4\n"
        "v_mfma_f32_16x16x32_bf16 v[116:119], v[196:199], v[184:187], v[116:119]\n"
        "s_add_u32 s61, s60, s61\n"
        "v_mfma_f32_16x16x32_bf16 v[120:123], v[200:203], v[184:187], v[120:123]\n"
        "s_mul_i32 s61, s61, 0x2800\n"
        "v_mfma_f32_16x16x32_bf16 v[100:103], v[200:203], v[180:183], v[100:103]\n"
        "s_add_u32 s61, s61, 0xf1a080\n"
        "v_mfma_f32_16x16x32_bf16 v[80:83], v[200:203], v[176:179], v[80:83]\n"
        "v_lshl_add_u32 v167, %[ln], 4, s61\n"
        "v_mfma_f32_16x16x32_bf16 v[60:63], v[200:203], v[172:175], v[60:63]\n"
        "v_lshlrev_b32 v168, 4, %[ln]\n"
        "v_mfma_f32_16x16x32_bf16 v[64:67], v[204:207], v[172:175], v[64:67]\n"
        "v_add_u32 v168, 0x1b000, v168\n"
        "v_mfma_f32_16x16x32_bf16 v[84:87], v[204:207], v[176:179], v[84:87]\n"
        "s_mov_b64 s[44:45], s[26:27]\n"
        "v_mfma_f32_16x16x32_bf16 v[104:107], v[204:207], v[180:183], v[104:107]\n"
        "s_and_b32 s45, s45, 0xffff\n"
        "v_mfma_f32_16x16x32_bf16 v[124:127], v[204:207], v[184:187], v[124:127]\n"
        "s_mov_b32 s46, 2621440\n"
        "s_waitcnt lgkmcnt(0)\n"
        "v_mfma_f32_16x16x32_bf16 v[48:51], v[128:131], v[236:239], v[48:51]\n"
        "s_mov_b32 s47, 0x20000\n"
        "v_mfma_f32_16x16x32_bf16 v[68:71], v[128:131], v[240:243], v[68:71]\n"
        "s_xor_b32 s56, s74, 1\n"
        "v_mfma_f32_16x16x32_bf16 v[88:91], v[128:131], v[244:247], v[88:91]\n"
        "s_mul_i32 s56, s56, 10240\n"
        "v_mfma_f32_16x16x32_bf16 v[108:111], v[128:131], v[248:251], v[108:111]\n"
        "s_waitcnt lgkmcnt(0)\n"
        "s_barrier\n"
        "s_xor_b32 s57, s74, 2\n"
        "v_mfma_f32_16x16x32_bf16 v[112:115], v[132:135], v[248:251], v[112:115]\n"
        "ds_read_b128 v[188:191], v165 offset:40960\n"
        "v_mfma_f32_16x16x32_bf16 v[92:95], v[132:135], v[244:247], v[92:95]\n"
        "ds_read_b128 v[172:175], v163 offset:32768\n"
        "v_mfma_f32_16x16x32_bf16 v[72:75], v[132:135], v[240:243], v[72:75]\n"
        "ds_read_b128 v[176:179], v163 offset:34816\n"
        "v_mfma_f32_16x16x32_bf16 v[52:55], v[132:135], v[236:239], v[52:55]\n"
        "ds_read_b128 v[180:183], v163 offset:36864\n"
        "v_mfma_f32_16x16x32_bf16 v[56:59], v[136:139], v[236:239], v[56:59]\n"
        "ds_read_b128 v[184:187], v163 offset:38912\n"
        "v_mfma_f32_16x16x32_bf16 v[76:79], v[136:139], v[240:243], v[76:79]\n"
        "ds_read_b128 v[192:195], v165 offset:41984\n"
        "v_mfma_f32_16x16x32_bf16 v[96:99], v[136:139], v[244:247], v[96:99]\n"
        "ds_read_b128 v[196:199], v165 offset:43008\n"
        "v_mfma_f32_16x16x32_bf16 v[116:119], v[136:139], v[248:251], v[116:119]\n"
        "ds_read_b128 v[200:203], v165 offset:44032\n"
        "v_mfma_f32_16x16x32_bf16 v[120:123], v[140:143], v[248:251], v[120:123]\n"
        "ds_read_b128 v[204:207], v165 offset:45056\n"
        "v_mfma_f32_16x16x32_bf16 v[100:103], v[140:143], v[244:247], v[100:103]\n"
        "s_mul_i32 s57, s57, 10240\n"
        "v_mfma_f32_16x16x32_bf16 v[80:83], v[140:143], v[240:243], v[80:83]\n"
        "s_xor_b32 s58, s74, 3\n"
        "v_mfma_f32_16x16x32_bf16 v[60:63], v[140:143], v[236:239], v[60:63]\n"
        "s_mul_i32 s58, s58, 10240\n"
        "v_mfma_f32_16x16x32_bf16 v[64:67], v[144:147], v[236:239], v[64:67]\n"
        "s_lshr_b32 s41, %[w], 1\n"
        "v_mfma_f32_16x16x32_bf16 v[84:87], v[144:147], v[240:243], v[84:87]\n"
        "s_lshl_b32 s41, s41, 30\n"
        "v_mfma_f32_16x16x32_bf16 v[104:107], v[144:147], v[244:247], v[104:107]\n"
        "s_add_u32 s41, s41, 0x2000\n"
        "v_mfma_f32_16x16x32_bf16 v[124:127], v[144:147], v[248:251], v[124:127]\n"
        "v_add_u32 v154, 0x1000, v9\n"
        "s_waitcnt lgkmcnt(0)\n"
        "v_mfma_f32_16x16x32_bf16 v[48:51], v[188:191], v[172:175], v[48:51]\n"
        "ds_read_b128 v[128:131], v165 offset:51200\n"
        "v_mfma_f32_16x16x32_bf16 v[68:71], v[188:191], v[176:179], v[68:71]\n"
        "ds_read_b128 v[236:239], v164 offset:32768\n"
        "v_mfma_f32_16x16x32_bf16 v[88:91], v[188:191], v[180:183], v[88:91]\n"
        "ds_read_b128 v[240:243], v164 offset:34816\n"
        "v_mfma_f32_16x16x32_bf16 v[108:111], v[188:191], v[184:187], v[108:111]\n"
        "ds_read_b128 v[244:247], v164 offset:36864\n"
        "v_mfma_f32_16x16x32_bf16 v[112:115], v[192:195], v[184:187], v[112:115]\n"
        "ds_read_b128 v[248:251], v164 offset:38912\n"
        "v_mfma_f32_16x16x32_bf16 v[92:95], v[192:195], v[180:183], v[92:95]\n"
        "ds_read_b128 v[132:135], v165 offset:52224\n"
        "v_mfma_f32_16x16x32_bf16 v[72:75], v[192:195], v[176:179], v[72:75]\n"
        "ds_read_b128 v[136:139], v165 offset:53248\n"
        "v_mfma_f32_16x16x32_bf16 v[52:55], v[192:195], v[172:175], v[52:55]\n"
        "ds_read_b128 v[140:143], v165 offset:54272\n"
        "v_mfma_f32_16x16x32_bf16 v[56:59], v[196:199], v[172:175], v[56:59]\n"
        "ds_read_b128 v[144:147], v165 offset:55296\n"
        "v_mfma_f32_16x16x32_bf16 v[76:79], v[196:199], v[176:179], v[76:79]\n"
        "v_add_u32 v155, s41, v9\n"
        "v_mfma_f32_16x16x32_bf16 v[96:99], v[196:199], v[180:183], v[96:99]\n"
        "v_add_u32 v158, 53248, v10\n"
        "v_mfma_f32_16x16x32_bf16 v[116:119], v[196:199], v[184:187], v[116:119]\n"
        "s_lshl_b32 s41, %[w], 10\n"
        "v_mfma_f32_16x16x32_bf16 v[120:123], v[200:203], v[184:187], v[120:123]\n"
        "v_add_u32 v159, s41, v168\n"
        "v_mfma_f32_16x16x32_bf16 v[100:103], v[200:203], v[180:183], v[100:103]\n"
        "s_cmp_lt_u32 s74, 2\n"
        "v_mfma_f32_16x16x32_bf16 v[80:83], v[200:203], v[176:179], v[80:83]\n"
        "s_cselect_b32 s41, 1.0, -1.0\n"
        "v_mfma_f32_16x16x32_bf16 v[60:63], v[200:203], v[172:175], v[60:63]\n"
        "s_cmp_eq_u32 %[w], 0\n"
        "v_mfma_f32_16x16x32_bf16 v[64:67], v[204:207], v[172:175], v[64:67]\n"
        "s_cselect_b64 vcc, -1, 0\n"
        "v_mfma_f32_16x16x32_bf16 v[84:87], v[204:207], v[176:179], v[84:87]\n"
        "v_mul_f32 v148, s41, v212\n"
        "v_mfma_f32_16x16x32_bf16 v[104:107], v[204:207], v[180:183], v[104:107]\n"
        "v_mul_f32 v149, s41, v213\n"
        "v_mfma_f32_16x16x32_bf16 v[124:127], v[204:207], v[184:187], v[124:127]\n"
        "v_mul_f32 v152, s41, v214\n"
        "s_waitcnt lgkmcnt(0)\n"
        "v_mfma_f32_16x16x32_bf16 v[48:51], v[128:131], v[236:239], v[48:51]\n"
        "v_mul_f32 v153, s41, v215\n"
        "v_mfma_f32_16x16x32_bf16 v[68:71], v[128:131], v[240:243], v[68:71]\n"
        "v_cndmask_b32 v150, v152, v148, vcc\n"
        "v_mfma_f32_16x16x32_bf16 v[88:91], v[128:131], v[244:247], v[88:91]\n"
        "v_cndmask_b32 v151, v153, v149, vcc\n"
        "v_mfma_f32_16x16x32_bf16 v[108:111], v[128:131], v[248:251], v[108:111]\n"
        "s_waitcnt lgkmcnt(0)\n"
        "s_barrier\n"
        "s_movk_i32 s41, 0x1000\n"
        "v_mfma_f32_16x16x32_bf16 v[112:115], v[132:135], v[248:251], v[112:115]\n"
        "ds_read_b128 v[188:191], v165\n"
        "v_mfma_f32_16x16x32_bf16 v[92:95], v[132:135], v[244:247], v[92:95]\n"
        "ds_read_b128 v[172:175], v163\n"
        "v_mfma_f32_16x16x32_bf16 v[72:75], v[132:135], v[240:243], v[72:75]\n"
        "ds_read_b128 v[176:179], v163 offset:2048\n"
        "v_mfma_f32_16x16x32_bf16 v[52:55], v[132:135], v[236:239], v[52:55]\n"
        "ds_read_b128 v[180:183], v163 offset:4096\n"
        "v_mfma_f32_16x16x32_bf16 v[56:59], v[136:139], v[236:239], v[56:59]\n"
        "ds_read_b128 v[184:187], v163 offset:6144\n"
        "v_mfma_f32_16x16x32_bf16 v[76:79], v[136:139], v[240:243], v[76:79]\n"
        "ds_read_b128 v[192:195], v165 offset:1024\n"
        "v_mfma_f32_16x16x32_bf16 v[96:99], v[136:139], v[244:247], v[96:99]\n"
        "ds_read_b128 v[196:199], v165 offset:2048\n"
        "v_mfma_f32_16x16x32_bf16 v[116:119], v[136:139], v[248:251], v[116:119]\n"
        "ds_read_b128 v[200:203], v165 offset:3072\n"
        "v_mfma_f32_16x16x32_bf16 v[120:123], v[140:143], v[248:251], v[120:123]\n"
        "ds_read_b128 v[204:207], v165 offset:4096\n"
        "v_mfma_f32_16x16x32_bf16 v[100:103], v[140:143], v[244:247], v[100:103]\n"
        "s_movk_i32 s42, 0x2000\n"
        "v_mfma_f32_16x16x32_bf16 v[80:83], v[140:143], v[240:243], v[80:83]\n"
        "s_mov_b32 s40, 0\n"
        "v_mfma_f32_16x16x32_bf16 v[60:63], v[140:143], v[236:239], v[60:63]\n"
        "v_mfma_f32_16x16x32_bf16 v[64:67], v[144:147], v[236:239], v[64:67]\n"
        "v_mfma_f32_16x16x32_bf16 v[84:87], v[144:147], v[240:243], v[84:87]\n"
        "v_mfma_f32_16x16x32_bf16 v[104:107], v[144:147], v[244:247], v[104:107]\n"
        "v_mfma_f32_16x16x32_bf16 v[124:127], v[144:147], v[248:251], v[124:127]\n"
        "s_waitcnt lgkmcnt(0)\n"
        "v_mfma_f32_16x16x32_bf16 v[48:51], v[188:191], v[172:175], v[48:51]\n"
        "ds_read_b128 v[128:131], v165 offset:10240\n"
        "v_mfma_f32_16x16x32_bf16 v[68:71], v[188:191], v[176:179], v[68:71]\n"
        "ds_read_b128 v[236:239], v164\n"
        "v_mfma_f32_16x16x32_bf16 v[88:91], v[188:191], v[180:183], v[88:91]\n"
        "ds_read_b128 v[240:243], v164 offset:2048\n"
        "v_mfma_f32_16x16x32_bf16 v[108:111], v[188:191], v[184:187], v[108:111]\n"
        "ds_read_b128 v[244:247], v164 offset:4096\n"
        "v_mfma_f32_16x16x32_bf16 v[112:115], v[192:195], v[184:187], v[112:115]\n"
        "ds_read_b128 v[248:251], v164 offset:6144\n"
        "v_mfma_f32_16x16x32_bf16 v[92:95], v[192:195], v[180:183], v[92:95]\n"
        "ds_read_b128 v[132:135], v165 offset:11264\n"
        "v_mfma_f32_16x16x32_bf16 v[72:75], v[192:195], v[176:179], v[72:75]\n"
        "ds_read_b128 v[136:139], v165 offset:12288\n"
        "v_mfma_f32_16x16x32_bf16 v[52:55], v[192:195], v[172:175], v[52:55]\n"
        "ds_read_b128 v[140:143], v165 offset:13312\n"
        "v_mfma_f32_16x16x32_bf16 v[56:59], v[196:199], v[172:175], v[56:59]\n"
        "ds_read_b128 v[144:147], v165 offset:14336\n"
        "v_mfma_f32_16x16x32_bf16 v[76:79], v[196:199], v[176:179], v[76:79]\n"
        "v_mfma_f32_16x16x32_bf16 v[96:99], v[196:199], v[180:183], v[96:99]\n"
        "v_mfma_f32_16x16x32_bf16 v[116:119], v[196:199], v[184:187], v[116:119]\n"
        "v_mfma_f32_16x16x32_bf16 v[120:123], v[200:203], v[184:187], v[120:123]\n"
        "v_mfma_f32_16x16x32_bf16 v[100:103], v[200:203], v[180:183], v[100:103]\n"
        "v_mfma_f32_16x16x32_bf16 v[80:83], v[200:203], v[176:179], v[80:83]\n"
        "v_mfma_f32_16x16x32_bf16 v[60:63], v[200:203], v[172:175], v[60:63]\n"
        "v_mfma_f32_16x16x32_bf16 v[64:67], v[204:207], v[172:175], v[64:67]\n"
        "v_mfma_f32_16x16x32_bf16 v[84:87], v[204:207], v[176:179], v[84:87]\n"
        "v_mfma_f32_16x16x32_bf16 v[104:107], v[204:207], v[180:183], v[104:107]\n"
        "v_mfma_f32_16x16x32_bf16 v[124:127], v[204:207], v[184:187], v[124:127]\n"
        "s_waitcnt lgkmcnt(0)\n"
        "v_mfma_f32_16x16x32_bf16 v[48:51], v[128:131], v[236:239], v[48:51]\n"
        "v_mfma_f32_16x16x32_bf16 v[68:71], v[128:131], v[240:243], v[68:71]\n"
        "v_mfma_f32_16x16x32_bf16 v[88:91], v[128:131], v[244:247], v[88:91]\n"
        "v_mfma_f32_16x16x32_bf16 v[108:111], v[128:131], v[248:251], v[108:111]\n"
        "s_waitcnt lgkmcnt(0)\n"
        "s_barrier\n"
        "v_mfma_f32_16x16x32_bf16 v[112:115], v[132:135], v[248:251], v[112:115]\n"
        "ds_read_b128 v[188:191], v165 offset:20480\n"
        "v_mfma_f32_16x16x32_bf16 v[92:95], v[132:135], v[244:247], v[92:95]\n"
        "ds_read_b128 v[172:175], v163 offset:16384\n"
        "v_mfma_f32_16x16x32_bf16 v[72:75], v[132:135], v[240:243], v[72:75]\n"
        "ds_read_b128 v[176:179], v163 offset:18432\n"
        "v_mfma_f32_16x16x32_bf16 v[52:55], v[132:135], v[236:239], v[52:55]\n"
        "ds_read_b128 v[180:183], v163 offset:20480\n"
        "v_mfma_f32_16x16x32_bf16 v[56:59], v[136:139], v[236:239], v[56:59]\n"
        "ds_read_b128 v[184:187], v163 offset:22528\n"
        "v_mfma_f32_16x16x32_bf16 v[76:79], v[136:139], v[240:243], v[76:79]\n"
        "ds_read_b128 v[192:195], v165 offset:21504\n"
        "v_mfma_f32_16x16x32_bf16 v[96:99], v[136:139], v[244:247], v[96:99]\n"
        "ds_read_b128 v[196:199], v165 offset:22528\n"
        "v_mfma_f32_16x16x32_bf16 v[116:119], v[136:139], v[248:251], v[116:119]\n"
        "ds_read_b128 v[200:203], v165 offset:23552\n"
        "v_mfma_f32_16x16x32_bf16 v[120:123], v[140:143], v[248:251], v[120:123]\n"
        "ds_read_b128 v[204:207], v165 offset:24576\n"
        "v_mfma_f32_16x16x32_bf16 v[100:103], v[140:143], v[244:247], v[100:103]\n"
        "v_mfma_f32_16x16x32_bf16 v[80:83], v[140:143], v[240:243], v[80:83]\n"
        "v_mfma_f32_16x16x32_bf16 v[60:63], v[140:143], v[236:239], v[60:63]\n"
        "v_mfma_f32_16x16x32_bf16 v[64:67], v[144:147], v[236:239], v[64:67]\n"
        "v_mfma_f32_16x16x32_bf16 v[84:87], v[144:147], v[240:243], v[84:87]\n"
        "v_mfma_f32_16x16x32_bf16 v[104:107], v[144:147], v[244:247], v[104:107]\n"
        "v_mfma_f32_16x16x32_bf16 v[124:127], v[144:147], v[248:251], v[124:127]\n"
        "s_waitcnt lgkmcnt(0)\n"
        "v_mfma_f32_16x16x32_bf16 v[48:51], v[188:191], v[172:175], v[48:51]\n"
        "ds_read_b128 v[128:131], v165 offset:30720\n"
        "v_mfma_f32_16x16x32_bf16 v[68:71], v[188:191], v[176:179], v[68:71]\n"
        "ds_read_b128 v[236:239], v164 offset:16384\n"
        "v_mfma_f32_16x16x32_bf16 v[88:91], v[188:191], v[180:183], v[88:91]\n"
        "ds_read_b128 v[240:243], v164 offset:18432\n"
        "v_mfma_f32_16x16x32_bf16 v[108:111], v[188:191], v[184:187], v[108:111]\n"
        "ds_read_b128 v[244:247], v164 offset:20480\n"
        "v_mfma_f32_16x16x32_bf16 v[112:115], v[192:195], v[184:187], v[112:115]\n"
        "ds_read_b128 v[248:251], v164 offset:22528\n"
        "v_mfma_f32_16x16x32_bf16 v[92:95], v[192:195], v[180:183], v[92:95]\n"
        "ds_read_b128 v[132:135], v165 offset:31744\n"
        "v_mfma_f32_16x16x32_bf16 v[72:75], v[192:195], v[176:179], v[72:75]\n"
        "ds_read_b128 v[136:139], v165 offset:32768\n"
        "v_mfma_f32_16x16x32_bf16 v[52:55], v[192:195], v[172:175], v[52:55]\n"
        "ds_read_b128 v[140:143], v165 offset:33792\n"
        "v_mfma_f32_16x16x32_bf16 v[56:59], v[196:199], v[172:175], v[56:59]\n"
        "ds_read_b128 v[144:147], v165 offset:34816\n"
        "v_mfma_f32_16x16x32_bf16 v[76:79], v[196:199], v[176:179], v[76:79]\n"
        "v_mfma_f32_16x16x32_bf16 v[96:99], v[196:199], v[180:183], v[96:99]\n"
        "v_mfma_f32_16x16x32_bf16 v[116:119], v[196:199], v[184:187], v[116:119]\n"
        "v_mfma_f32_16x16x32_bf16 v[120:123], v[200:203], v[184:187], v[120:123]\n"
        "v_mfma_f32_16x16x32_bf16 v[100:103], v[200:203], v[180:183], v[100:103]\n"
        "v_mfma_f32_16x16x32_bf16 v[80:83], v[200:203], v[176:179], v[80:83]\n"
        "v_mfma_f32_16x16x32_bf16 v[60:63], v[200:203], v[172:175], v[60:63]\n"
        "v_mfma_f32_16x16x32_bf16 v[64:67], v[204:207], v[172:175], v[64:67]\n"
        "v_mfma_f32_16x16x32_bf16 v[84:87], v[204:207], v[176:179], v[84:87]\n"
        "v_mfma_f32_16x16x32_bf16 v[104:107], v[204:207], v[180:183], v[104:107]\n"
        "v_mfma_f32_16x16x32_bf16 v[124:127], v[204:207], v[184:187], v[124:127]\n"
        "s_waitcnt lgkmcnt(0)\n"
        "v_mfma_f32_16x16x32_bf16 v[48:51], v[128:131], v[236:239], v[48:51]\n"
        "v_mfma_f32_16x16x32_bf16 v[68:71], v[128:131], v[240:243], v[68:71]\n"
        "v_mfma_f32_16x16x32_bf16 v[88:91], v[128:131], v[244:247], v[88:91]\n"
        "v_mfma_f32_16x16x32_bf16 v[108:111], v[128:131], v[248:251], v[108:111]\n"
        "s_waitcnt lgkmcnt(0)\n"
        "s_barrier\n"
        "v_mfma_f32_16x16x32_bf16 v[112:115], v[132:135], v[248:251], v[112:115]\n"
        "ds_read_b128 v[188:191], v165 offset:40960\n"
        "v_mfma_f32_16x16x32_bf16 v[92:95], v[132:135], v[244:247], v[92:95]\n"
        "ds_read_b128 v[172:175], v163 offset:32768\n"
        "v_mfma_f32_16x16x32_bf16 v[72:75], v[132:135], v[240:243], v[72:75]\n"
        "ds_read_b128 v[176:179], v163 offset:34816\n"
        "v_mfma_f32_16x16x32_bf16 v[52:55], v[132:135], v[236:239], v[52:55]\n"
        "ds_read_b128 v[180:183], v163 offset:36864\n"
        "v_mfma_f32_16x16x32_bf16 v[56:59], v[136:139], v[236:239], v[56:59]\n"
        "ds_read_b128 v[184:187], v163 offset:38912\n"
        "v_mfma_f32_16x16x32_bf16 v[76:79], v[136:139], v[240:243], v[76:79]\n"
        "ds_read_b128 v[192:195], v165 offset:41984\n"
        "v_mfma_f32_16x16x32_bf16 v[96:99], v[136:139], v[244:247], v[96:99]\n"
        "ds_read_b128 v[196:199], v165 offset:43008\n"
        "v_mfma_f32_16x16x32_bf16 v[116:119], v[136:139], v[248:251], v[116:119]\n"
        "ds_read_b128 v[200:203], v165 offset:44032\n"
        "v_mfma_f32_16x16x32_bf16 v[120:123], v[140:143], v[248:251], v[120:123]\n"
        "ds_read_b128 v[204:207], v165 offset:45056\n"
        "v_mfma_f32_16x16x32_bf16 v[100:103], v[140:143], v[244:247], v[100:103]\n"
        "v_mfma_f32_16x16x32_bf16 v[80:83], v[140:143], v[240:243], v[80:83]\n"
        "v_mfma_f32_16x16x32_bf16 v[60:63], v[140:143], v[236:239], v[60:63]\n"
        "v_mfma_f32_16x16x32_bf16 v[64:67], v[144:147], v[236:239], v[64:67]\n"
        "v_mfma_f32_16x16x32_bf16 v[84:87], v[144:147], v[240:243], v[84:87]\n"
        "v_mfma_f32_16x16x32_bf16 v[104:107], v[144:147], v[244:247], v[104:107]\n"
        "v_mfma_f32_16x16x32_bf16 v[124:127], v[144:147], v[248:251], v[124:127]\n"
        "s_waitcnt lgkmcnt(0)\n"
        "v_mfma_f32_16x16x32_bf16 v[48:51], v[188:191], v[172:175], v[48:51]\n"
        "ds_read_b128 v[128:131], v165 offset:51200\n"
        "v_mfma_f32_16x16x32_bf16 v[68:71], v[188:191], v[176:179], v[68:71]\n"
        "ds_read_b128 v[236:239], v164 offset:32768\n"
        "v_mfma_f32_16x16x32_bf16 v[88:91], v[188:191], v[180:183], v[88:91]\n"
        "ds_read_b128 v[240:243], v164 offset:34816\n"
        "v_mfma_f32_16x16x32_bf16 v[108:111], v[188:191], v[184:187], v[108:111]\n"
        "ds_read_b128 v[244:247], v164 offset:36864\n"
        "v_mfma_f32_16x16x32_bf16 v[112:115], v[192:195], v[184:187], v[112:115]\n"
        "ds_read_b128 v[248:251], v164 offset:38912\n"
        "v_mfma_f32_16x16x32_bf16 v[92:95], v[192:195], v[180:183], v[92:95]\n"
        "ds_read_b128 v[132:135], v165 offset:52224\n"
        "v_mfma_f32_16x16x32_bf16 v[72:75], v[192:195], v[176:179], v[72:75]\n"
        "ds_read_b128 v[136:139], v165 offset:53248\n"
        "v_mfma_f32_16x16x32_bf16 v[52:55], v[192:195], v[172:175], v[52:55]\n"
        "ds_read_b128 v[140:143], v165 offset:54272\n"
        "v_mfma_f32_16x16x32_bf16 v[56:59], v[196:199], v[172:175], v[56:59]\n"
        "ds_read_b128 v[144:147], v165 offset:55296\n"
        "v_mfma_f32_16x16x32_bf16 v[76:79], v[196:199], v[176:179], v[76:79]\n"
        "v_mfma_f32_16x16x32_bf16 v[96:99], v[196:199], v[180:183], v[96:99]\n"
        "v_mfma_f32_16x16x32_bf16 v[116:119], v[196:199], v[184:187], v[116:119]\n"
        "v_mfma_f32_16x16x32_bf16 v[120:123], v[200:203], v[184:187], v[120:123]\n"
        "v_mfma_f32_16x16x32_bf16 v[100:103], v[200:203], v[180:183], v[100:103]\n"
        "v_mfma_f32_16x16x32_bf16 v[80:83], v[200:203], v[176:179], v[80:83]\n"
        "v_mfma_f32_16x16x32_bf16 v[60:63], v[200:203], v[172:175], v[60:63]\n"
        "v_mfma_f32_16x16x32_bf16 v[64:67], v[204:207], v[172:175], v[64:67]\n"
        "v_mfma_f32_16x16x32_bf16 v[84:87], v[204:207], v[176:179], v[84:87]\n"
        "v_mfma_f32_16x16x32_bf16 v[104:107], v[204:207], v[180:183], v[104:107]\n"
        "v_mfma_f32_16x16x32_bf16 v[124:127], v[204:207], v[184:187], v[124:127]\n"
        "s_waitcnt lgkmcnt(0)\n"
        "v_mfma_f32_16x16x32_bf16 v[48:51], v[128:131], v[236:239], v[48:51]\n"
        "v_mfma_f32_16x16x32_bf16 v[68:71], v[128:131], v[240:243], v[68:71]\n"
        "v_mfma_f32_16x16x32_bf16 v[88:91], v[128:131], v[244:247], v[88:91]\n"
        "v_mfma_f32_16x16x32_bf16 v[108:111], v[128:131], v[248:251], v[108:111]\n"
        "s_waitcnt lgkmcnt(0)\n"
        "s_barrier\n"
        "v_mfma_f32_16x16x32_bf16 v[112:115], v[132:135], v[248:251], v[112:115]\n"
        "ds_read_b128 v[188:191], v165\n"
        "v_mfma_f32_16x16x32_bf16 v[92:95], v[132:135], v[244:247], v[92:95]\n"
        "ds_read_b128 v[172:175], v163\n"
        "v_mfma_f32_16x16x32_bf16 v[72:75], v[132:135], v[240:243], v[72:75]\n"
        "ds_read_b128 v[176:179], v163 offset:2048\n"
        "v_mfma_f32_16x16x32_bf16 v[52:55], v[132:135], v[236:239], v[52:55]\n"
        "ds_read_b128 v[180:183], v163 offset:4096\n"
        "v_mfma_f32_16x16x32_bf16 v[56:59], v[136:139], v[236:239], v[56:59]\n"
        "ds_read_b128 v[184:187], v163 offset:6144\n"
        "v_mfma_f32_16x16x32_bf16 v[76:79], v[136:139], v[240:243], v[76:79]\n"
        "ds_read_b128 v[192:195], v165 offset:1024\n"
        "v_mfma_f32_16x16x32_bf16 v[96:99], v[136:139], v[244:247], v[96:99]\n"
        "ds_read_b128 v[196:199], v165 offset:2048\n"
        "v_mfma_f32_16x16x32_bf16 v[116:119], v[136:139], v[248:251], v[116:119]\n"
        "ds_read_b128 v[200:203], v165 offset:3072\n"
        "v_mfma_f32_16x16x32_bf16 v[120:123], v[140:143], v[248:251], v[120:123]\n"
        "ds_read_b128 v[204:207], v165 offset:4096\n"
        "v_mfma_f32_16x16x32_bf16 v[100:103], v[140:143], v[244:247], v[100:103]\n"
        "v_mfma_f32_16x16x32_bf16 v[80:83], v[140:143], v[240:243], v[80:83]\n"
        "v_mfma_f32_16x16x32_bf16 v[60:63], v[140:143], v[236:239], v[60:63]\n"
        "v_mfma_f32_16x16x32_bf16 v[64:67], v[144:147], v[236:239], v[64:67]\n"
        "v_mfma_f32_16x16x32_bf16 v[84:87], v[144:147], v[240:243], v[84:87]\n"
        "v_mfma_f32_16x16x32_bf16 v[104:107], v[144:147], v[244:247], v[104:107]\n"
        "v_mfma_f32_16x16x32_bf16 v[124:127], v[144:147], v[248:251], v[124:127]\n"
        "s_waitcnt lgkmcnt(0)\n"
        "v_mfma_f32_16x16x32_bf16 v[48:51], v[188:191], v[172:175], v[48:51]\n"
        "ds_read_b128 v[128:131], v165 offset:10240\n"
        "v_mfma_f32_16x16x32_bf16 v[68:71], v[188:191], v[176:179], v[68:71]\n"
        "ds_read_b128 v[236:239], v164\n"
        "v_mfma_f32_16x16x32_bf16 v[88:91], v[188:191], v[180:183], v[88:91]\n"
        "ds_read_b128 v[240:243], v164 offset:2048\n"
        "v_mfma_f32_16x16x32_bf16 v[108:111], v[188:191], v[184:187], v[108:111]\n"
        "ds_read_b128 v[244:247], v164 offset:4096\n"
        "v_mfma_f32_16x16x32_bf16 v[112:115], v[192:195], v[184:187], v[112:115]\n"
        "ds_read_b128 v[248:251], v164 offset:6144\n"
        "v_mfma_f32_16x16x32_bf16 v[92:95], v[192:195], v[180:183], v[92:95]\n"
        "ds_read_b128 v[132:135], v165 offset:11264\n"
        "v_mfma_f32_16x16x32_bf16 v[72:75], v[192:195], v[176:179], v[72:75]\n"
        "ds_read_b128 v[136:139], v165 offset:12288\n"
        "v_mfma_f32_16x16x32_bf16 v[52:55], v[192:195], v[172:175], v[52:55]\n"
        "ds_read_b128 v[140:143], v165 offset:13312\n"
        "v_mfma_f32_16x16x32_bf16 v[56:59], v[196:199], v[172:175], v[56:59]\n"
        "ds_read_b128 v[144:147], v165 offset:14336\n"
        "v_mfma_f32_16x16x32_bf16 v[76:79], v[196:199], v[176:179], v[76:79]\n"
        "v_mfma_f32_16x16x32_bf16 v[96:99], v[196:199], v[180:183], v[96:99]\n"
        "v_mfma_f32_16x16x32_bf16 v[116:119], v[196:199], v[184:187], v[116:119]\n"
        "v_mfma_f32_16x16x32_bf16 v[120:123], v[200:203], v[184:187], v[120:123]\n"
        "v_mfma_f32_16x16x32_bf16 v[100:103], v[200:203], v[180:183], v[100:103]\n"
        "v_mfma_f32_16x16x32_bf16 v[80:83], v[200:203], v[176:179], v[80:83]\n"
        "v_mfma_f32_16x16x32_bf16 v[60:63], v[200:203], v[172:175], v[60:63]\n"
        "v_mfma_f32_16x16x32_bf16 v[64:67], v[204:207], v[172:175], v[64:67]\n"
        "v_mfma_f32_16x16x32_bf16 v[84:87], v[204:207], v[176:179], v[84:87]\n"
        "v_mfma_f32_16x16x32_bf16 v[104:107], v[204:207], v[180:183], v[104:107]\n"
        "v_mfma_f32_16x16x32_bf16 v[124:127], v[204:207], v[184:187], v[124:127]\n"
        "s_waitcnt lgkmcnt(0)\n"
        "v_mfma_f32_16x16x32_bf16 v[48:51], v[128:131], v[236:239], v[48:51]\n"
        "v_mfma_f32_16x16x32_bf16 v[68:71], v[128:131], v[240:243], v[68:71]\n"
        "v_mfma_f32_16x16x32_bf16 v[88:91], v[128:131], v[244:247], v[88:91]\n"
        "v_mfma_f32_16x16x32_bf16 v[108:111], v[128:131], v[248:251], v[108:111]\n"
        "s_waitcnt lgkmcnt(0)\n"
        "s_barrier\n"
        "v_mfma_f32_16x16x32_bf16 v[112:115], v[132:135], v[248:251], v[112:115]\n"
        "ds_read_b128 v[188:191], v165 offset:20480\n"
        "v_mfma_f32_16x16x32_bf16 v[92:95], v[132:135], v[244:247], v[92:95]\n"
        "ds_read_b128 v[172:175], v163 offset:16384\n"
        "v_mfma_f32_16x16x32_bf16 v[72:75], v[132:135], v[240:243], v[72:75]\n"
        "ds_read_b128 v[176:179], v163 offset:18432\n"
        "v_mfma_f32_16x16x32_bf16 v[52:55], v[132:135], v[236:239], v[52:55]\n"
        ".Lk_hop3:\n"
        "s_cbranch_execz .Lk_pad11\n"
        "ds_read_b128 v[180:183], v163 offset:20480\n"
        "v_mfma_f32_16x16x32_bf16 v[56:59], v[136:139], v[236:239], v[56:59]\n"
        "ds_read_b128 v[184:187], v163 offset:22528\n"
        "v_mfma_f32_16x16x32_bf16 v[76:79], v[136:139], v[240:243], v[76:79]\n"
        "ds_read_b128 v[192:195], v165 offset:21504\n"
        "v_mfma_f32_16x16x32_bf16 v[96:99], v[136:139], v[244:247], v[96:99]\n"
        "ds_read_b128 v[196:199], v165 offset:22528\n"
        "v_mfma_f32_16x16x32_bf16 v[116:119], v[136:139], v[248:251], v[116:119]\n"
        "ds_read_b128 v[200:203], v165 offset:23552\n"
        "v_mfma_f32_16x16x32_bf16 v[120:123], v[140:143], v[248:251], v[120:123]\n"
        "ds_read_b128 v[204:207], v165 offset:24576\n"
        "v_mfma_f32_16x16x32_bf16 v[100:103], v[140:143], v[244:247], v[100:103]\n"
        "v_mfma_f32_16x16x32_bf16 v[80:83], v[140:143], v[240:243], v[80:83]\n"
        "v_mfma_f32_16x16x32_bf16 v[60:63], v[140:143], v[236:239], v[60:63]\n"
        "v_mfma_f32_16x16x32_bf16 v[64:67], v[144:147], v[236:239], v[64:67]\n"
        "v_mfma_f32_16x16x32_bf16 v[84:87], v[144:147], v[240:243], v[84:87]\n"
        "v_mfma_f32_16x16x32_bf16 v[104:107], v[144:147], v[244:247], v[104:107]\n"
        "v_mfma_f32_16x16x32_bf16 v[124:127], v[144:147], v[248:251], v[124:127]\n"
        "s_waitcnt lgkmcnt(0)\n"
        "v_mfma_f32_16x16x32_bf16 v[48:51], v[188:191], v[172:175], v[48:51]\n"
        "ds_read_b128 v[128:131], v165 offset:30720\n"
        "v_mfma_f32_16x16x32_bf16 v[68:71], v[188:191], v[176:179], v[68:71]\n"
        "ds_read_b128 v[236:239], v164 offset:16384\n"
        "v_mfma_f32_16x16x32_bf16 v[88:91], v[188:191], v[180:183], v[88:91]\n"
        "ds_read_b128 v[240:243], v164 offset:18432\n"
        "v_mfma_f32_16x16x32_bf16 v[108:111], v[188:191], v[184:187], v[108:111]\n"
        "ds_read_b128 v[244:247], v164 offset:20480\n"
        "v_mfma_f32_16x16x32_bf16 v[112:115], v[192:195], v[184:187], v[112:115]\n"
        "ds_read_b128 v[248:251], v164 offset:22528\n"
        "v_mfma_f32_16x16x32_bf16 v[92:95], v[192:195], v[180:183], v[92:95]\n"
        "ds_read_b128 v[132:135], v165 offset:31744\n"
        "v_mfma_f32_16x16x32_bf16 v[72:75], v[192:195], v[176:179], v[72:75]\n"
        "ds_read_b128 v[136:139], v165 offset:32768\n"
        "v_mfma_f32_16x16x32_bf16 v[52:55], v[192:195], v[172:175], v[52:55]\n"
        "ds_read_b128 v[140:143], v165 offset:33792\n"
        "v_mfma_f32_16x16x32_bf16 v[56:59], v[196:199], v[172:175], v[56:59]\n"
        "ds_read_b128 v[144:147], v165 offset:34816\n"
        "v_mfma_f32_16x16x32_bf16 v[76:79], v[196:199], v[176:179], v[76:79]\n"
        "v_mfma_f32_16x16x32_bf16 v[96:99], v[196:199], v[180:183], v[96:99]\n"
        "v_mfma_f32_16x16x32_bf16 v[116:119], v[196:199], v[184:187], v[116:119]\n"
        "v_mfma_f32_16x16x32_bf16 v[120:123], v[200:203], v[184:187], v[120:123]\n"
        "v_mfma_f32_16x16x32_bf16 v[100:103], v[200:203], v[180:183], v[100:103]\n"
        "v_mfma_f32_16x16x32_bf16 v[80:83], v[200:203], v[176:179], v[80:83]\n"
        "v_mfma_f32_16x16x32_bf16 v[60:63], v[200:203], v[172:175], v[60:63]\n"
        "v_mfma_f32_16x16x32_bf16 v[64:67], v[204:207], v[172:175], v[64:67]\n"
        "v_mfma_f32_16x16x32_bf16 v[84:87], v[204:207], v[176:179], v[84:87]\n"
        "v_mfma_f32_16x16x32_bf16 v[104:107], v[204:207], v[180:183], v[104:107]\n"
        "v_mfma_f32_16x16x32_bf16 v[124:127], v[204:207], v[184:187], v[124:127]\n"
        "s_waitcnt lgkmcnt(0)\n"
        "v_mfma_f32_16x16x32_bf16 v[48:51], v[128:131], v[236:239], v[48:51]\n"
        "v_mfma_f32_16x16x32_bf16 v[68:71], v[128:131], v[240:243], v[68:71]\n"
        "v_mfma_f32_16x16x32_bf16 v[88:91], v[128:131], v[244:247], v[88:91]\n"
        "v_mfma_f32_16x16x32_bf16 v[108:111], v[128:131], v[248:251], v[108:111]\n"
        "s_waitcnt lgkmcnt(0)\n"
        "s_barrier\n"
        "v_mfma_f32_16x16x32_bf16 v[112:115], v[132:135], v[248:251], v[112:115]\n"
        "ds_read_b128 v[188:191], v165 offset:40960\n"
        "v_mfma_f32_16x16x32_bf16 v[92:95], v[132:135], v[244:247], v[92:95]\n"
        "ds_read_b128 v[172:175], v163 offset:32768\n"
        "v_mfma_f32_16x16x32_bf16 v[72:75], v[132:135], v[240:243], v[72:75]\n"
        "ds_read_b128 v[176:179], v163 offset:34816\n"
        "v_mfma_f32_16x16x32_bf16 v[52:55], v[132:135], v[236:239], v[52:55]\n"
        "ds_read_b128 v[180:183], v163 offset:36864\n"
        "v_mfma_f32_16x16x32_bf16 v[56:59], v[136:139], v[236:239], v[56:59]\n"
        "ds_read_b128 v[184:187], v163 offset:38912\n"
        "v_mfma_f32_16x16x32_bf16 v[76:79], v[136:139], v[240:243], v[76:79]\n"
        "ds_read_b128 v[192:195], v165 offset:41984\n"
        "v_mfma_f32_16x16x32_bf16 v[96:99], v[136:139], v[244:247], v[96:99]\n"
        "ds_read_b128 v[196:199], v165 offset:43008\n"
        "v_mfma_f32_16x16x32_bf16 v[116:119], v[136:139], v[248:251], v[116:119]\n"
        "ds_read_b128 v[200:203], v165 offset:44032\n"
        "v_mfma_f32_16x16x32_bf16 v[120:123], v[140:143], v[248:251], v[120:123]\n"
        "ds_read_b128 v[204:207], v165 offset:45056\n"
        "v_mfma_f32_16x16x32_bf16 v[100:103], v[140:143], v[244:247], v[100:103]\n"
        "v_mfma_f32_16x16x32_bf16 v[80:83], v[140:143], v[240:243], v[80:83]\n"
        "v_mfma_f32_16x16x32_bf16 v[60:63], v[140:143], v[236:239], v[60:63]\n"
        "v_mfma_f32_16x16x32_bf16 v[64:67], v[144:147], v[236:239], v[64:67]\n"
        "v_mfma_f32_16x16x32_bf16 v[84:87], v[144:147], v[240:243], v[84:87]\n"
        "v_mfma_f32_16x16x32_bf16 v[104:107], v[144:147], v[244:247], v[104:107]\n"
        "v_mfma_f32_16x16x32_bf16 v[124:127], v[144:147], v[248:251], v[124:127]\n"
        "s_waitcnt lgkmcnt(0)\n"
        "v_mfma_f32_16x16x32_bf16 v[48:51], v[188:191], v[172:175], v[48:51]\n"
        "ds_read_b128 v[128:131], v165 offset:51200\n"
        "v_mfma_f32_16x16x32_bf16 v[68:71], v[188:191], v[176:179], v[68:71]\n"
        "ds_read_b128 v[236:239], v164 offset:32768\n"
        "v_mfma_f32_16x16x32_bf16 v[88:91], v[188:191], v[180:183], v[88:91]\n"
        "ds_read_b128 v[240:243], v164 offset:34816\n"
        "v_mfma_f32_16x16x32_bf16 v[108:111], v[188:191], v[184:187], v[108:111]\n"
        "ds_read_b128 v[244:247], v164 offset:36864\n"
        "v_mfma_f32_16x16x32_bf16 v[112:115], v[192:195], v[184:187], v[112:115]\n"
        "ds_read_b128 v[248:251], v164 offset:38912\n"
        "v_mfma_f32_16x16x32_bf16 v[92:95], v[192:195], v[180:183], v[92:95]\n"
        "ds_read_b128 v[132:135], v165 offset:52224\n"
        "v_mfma_f32_16x16x32_bf16 v[72:75], v[192:195], v[176:179], v[72:75]\n"
        "ds_read_b128 v[136:139], v165 offset:53248\n"
        "v_mfma_f32_16x16x32_bf16 v[52:55], v[192:195], v[172:175], v[52:55]\n"
        "ds_read_b128 v[140:143], v165 offset:54272\n"
        "v_mfma_f32_16x16x32_bf16 v[56:59], v[196:199], v[172:175], v[56:59]\n"
        "ds_read_b128 v[144:147], v165 offset:55296\n"
        "v_mfma_f32_16x16x32_bf16 v[76:79], v[196:199], v[176:179], v[76:79]\n"
        "v_mfma_f32_16x16x32_bf16 v[96:99], v[196:199], v[180:183], v[96:99]\n"
        "v_mfma_f32_16x16x32_bf16 v[116:119], v[196:199], v[184:187], v[116:119]\n"
        "v_mfma_f32_16x16x32_bf16 v[120:123], v[200:203], v[184:187], v[120:123]\n"
        "v_mfma_f32_16x16x32_bf16 v[100:103], v[200:203], v[180:183], v[100:103]\n"
        "v_mfma_f32_16x16x32_bf16 v[80:83], v[200:203], v[176:179], v[80:83]\n"
        "v_mfma_f32_16x16x32_bf16 v[60:63], v[200:203], v[172:175], v[60:63]\n"
        "v_mfma_f32_16x16x32_bf16 v[64:67], v[204:207], v[172:175], v[64:67]\n"
        "v_mfma_f32_16x16x32_bf16 v[84:87], v[204:207], v[176:179], v[84:87]\n"
        "v_mfma_f32_16x16x32_bf16 v[104:107], v[204:207], v[180:183], v[104:107]\n"
        "v_mfma_f32_16x16x32_bf16 v[124:127], v[204:207], v[184:187], v[124:127]\n"
        "s_waitcnt lgkmcnt(0)\n"
        "v_mfma_f32_16x16x32_bf16 v[48:51], v[128:131], v[236:239], v[48:51]\n"
        "v_mfma_f32_16x16x32_bf16 v[68:71], v[128:131], v[240:243], v[68:71]\n"
        "v_mfma_f32_16x16x32_bf16 v[88:91], v[128:131], v[244:247], v[88:91]\n"
        "v_mfma_f32_16x16x32_bf16 v[108:111], v[128:131], v[248:251], v[108:111]\n"
        "s_waitcnt lgkmcnt(0)\n"
        "s_barrier\n"
        "v_mfma_f32_16x16x32_bf16 v[112:115], v[132:135], v[248:251], v[112:115]\n"
        "ds_read_b128 v[188:191], v165\n"
        "v_mfma_f32_16x16x32_bf16 v[92:95], v[132:135], v[244:247], v[92:95]\n"
        "ds_read_b128 v[172:175], v163\n"
        "v_mfma_f32_16x16x32_bf16 v[72:75], v[132:135], v[240:243], v[72:75]\n"
        "ds_read_b128 v[176:179], v163 offset:2048\n"
        "v_mfma_f32_16x16x32_bf16 v[52:55], v[132:135], v[236:239], v[52:55]\n"
        "ds_read_b128 v[180:183], v163 offset:4096\n"
        "v_mfma_f32_16x16x32_bf16 v[56:59], v[136:139], v[236:239], v[56:59]\n"
        "ds_read_b128 v[184:187], v163 offset:6144\n"
        "v_mfma_f32_16x16x32_bf16 v[76:79], v[136:139], v[240:243], v[76:79]\n"
        "ds_read_b128 v[192:195], v165 offset:1024\n"
        "v_mfma_f32_16x16x32_bf16 v[96:99], v[136:139], v[244:247], v[96:99]\n"
        "ds_read_b128 v[196:199], v165 offset:2048\n"
        "v_mfma_f32_16x16x32_bf16 v[116:119], v[136:139], v[248:251], v[116:119]\n"
        "ds_read_b128 v[200:203], v165 offset:3072\n"
        "v_mfma_f32_16x16x32_bf16 v[120:123], v[140:143], v[248:251], v[120:123]\n"
        "ds_read_b128 v[204:207], v165 offset:4096\n"
        "v_mfma_f32_16x16x32_bf16 v[100:103], v[140:143], v[244:247], v[100:103]\n"
        "v_mfma_f32_16x16x32_bf16 v[80:83], v[140:143], v[240:243], v[80:83]\n"
        "v_mfma_f32_16x16x32_bf16 v[60:63], v[140:143], v[236:239], v[60:63]\n"
        "v_mfma_f32_16x16x32_bf16 v[64:67], v[144:147], v[236:239], v[64:67]\n"
        "v_mfma_f32_16x16x32_bf16 v[84:87], v[144:147], v[240:243], v[84:87]\n"
        "v_mfma_f32_16x16x32_bf16 v[104:107], v[144:147], v[244:247], v[104:107]\n"
        "v_mfma_f32_16x16x32_bf16 v[124:127], v[144:147], v[248:251], v[124:127]\n"
        "s_waitcnt lgkmcnt(0)\n"
        "v_mfma_f32_16x16x32_bf16 v[48:51], v[188:191], v[172:175], v[48:51]\n"
        "ds_read_b128 v[128:131], v165 offset:10240\n"
        "v_mfma_f32_16x16x32_bf16 v[68:71], v[188:191], v[176:179], v[68:71]\n"
        "ds_read_b128 v[236:239], v164\n"
        "v_mfma_f32_16x16x32_bf16 v[88:91], v[188:191], v[180:183], v[88:91]\n"
        "ds_read_b128 v[240:243], v164 offset:2048\n"
        "v_mfma_f32_16x16x32_bf16 v[108:111], v[188:191], v[184:187], v[108:111]\n"
        "ds_read_b128 v[244:247], v164 offset:4096\n"
        "v_mfma_f32_16x16x32_bf16 v[112:115], v[192:195], v[184:187], v[112:115]\n"
        "ds_read_b128 v[248:251], v164 offset:6144\n"
        "v_mfma_f32_16x16x32_bf16 v[92:95], v[192:195], v[180:183], v[92:95]\n"
        "ds_read_b128 v[132:135], v165 offset:11264\n"
        "v_mfma_f32_16x16x32_bf16 v[72:75], v[192:195], v[176:179], v[72:75]\n"
        "ds_read_b128 v[136:139], v165 offset:12288\n"
        "v_mfma_f32_16x16x32_bf16 v[52:55], v[192:195], v[172:175], v[52:55]\n"
        "ds_read_b128 v[140:143], v165 offset:13312\n"
        "v_mfma_f32_16x16x32_bf16 v[56:59], v[196:199], v[172:175], v[56:59]\n"
        "ds_read_b128 v[144:147], v165 offset:14336\n"
        "v_mfma_f32_16x16x32_bf16 v[76:79], v[196:199], v[176:179], v[76:79]\n"
        "v_mfma_f32_16x16x32_bf16 v[96:99], v[196:199], v[180:183], v[96:99]\n"
        "v_mfma_f32_16x16x32_bf16 v[116:119], v[196:199], v[184:187], v[116:119]\n"
        "v_mfma_f32_16x16x32_bf16 v[120:123], v[200:203], v[184:187], v[120:123]\n"
        "v_mfma_f32_16x16x32_bf16 v[100:103], v[200:203], v[180:183], v[100:103]\n"
        "v_mfma_f32_16x16x32_bf16 v[80:83], v[200:203], v[176:179], v[80:83]\n"
        "v_mfma_f32_16x16x32_bf16 v[60:63], v[200:203], v[172:175], v[60:63]\n"
        "v_mfma_f32_16x16x32_bf16 v[64:67], v[204:207], v[172:175], v[64:67]\n"
        "v_mfma_f32_16x16x32_bf16 v[84:87], v[204:207], v[176:179], v[84:87]\n"
        "v_mfma_f32_16x16x32_bf16 v[104:107], v[204:207], v[180:183], v[104:107]\n"
        "v_mfma_f32_16x16x32_bf16 v[124:127], v[204:207], v[184:187], v[124:127]\n"
        "s_waitcnt lgkmcnt(0)\n"
        "v_mfma_f32_16x16x32_bf16 v[48:51], v[128:131], v[236:239], v[48:51]\n"
        "v_mfma_f32_16x16x32_bf16 v[68:71], v[128:131], v[240:243], v[68:71]\n"
        "v_mfma_f32_16x16x32_bf16 v[88:91], v[128:131], v[244:247], v[88:91]\n"
        "v_mfma_f32_16x16x32_bf16 v[108:111], v[128:131], v[248:251], v[108:111]\n"
        "v_mfma_f32_16x16x32_bf16 v[112:115], v[132:135], v[248:251], v[112:115]\n"
        "v_mfma_f32_16x16x32_bf16 v[92:95], v[132:135], v[244:247], v[92:95]\n"
        "v_mfma_f32_16x16x32_bf16 v[72:75], v[132:135], v[240:243], v[72:75]\n"
        "v_mfma_f32_16x16x32_bf16 v[52:55], v[132:135], v[236:239], v[52:55]\n"
        "v_mfma_f32_16x16x32_bf16 v[56:59], v[136:139], v[236:239], v[56:59]\n"
        "v_mfma_f32_16x16x32_bf16 v[76:79], v[136:139], v[240:243], v[76:79]\n"
        "v_mfma_f32_16x16x32_bf16 v[96:99], v[136:139], v[244:247], v[96:99]\n"
        "v_mfma_f32_16x16x32_bf16 v[116:119], v[136:139], v[248:251], v[116:119]\n"
        "v_mfma_f32_16x16x32_bf16 v[120:123], v[140:143], v[248:251], v[120:123]\n"
        "v_mfma_f32_16x16x32_bf16 v[100:103], v[140:143], v[244:247], v[100:103]\n"
        "v_mfma_f32_16x16x32_bf16 v[80:83], v[140:143], v[240:243], v[80:83]\n"
        "v_mfma_f32_16x16x32_bf16 v[60:63], v[140:143], v[236:239], v[60:63]\n"
        "v_mfma_f32_16x16x32_bf16 v[64:67], v[144:147], v[236:239], v[64:67]\n"
        "v_mfma_f32_16x16x32_bf16 v[84:87], v[144:147], v[240:243], v[84:87]\n"
        "v_mfma_f32_16x16x32_bf16 v[104:107], v[144:147], v[244:247], v[104:107]\n"
        "v_mfma_f32_16x16x32_bf16 v[124:127], v[144:147], v[248:251], v[124:127]\n"
        "s_barrier\n"
        "s_nop 5\n"
        "s_cmp_eq_u32 %[w], s74\n"
        "s_cbranch_scc1 5f\n"
        "v_cvt_pk_bf16_f32 v172, v48, v49\n"
        "v_cvt_pk_bf16_f32 v173, v50, v51\n"
        "v_cvt_pk_bf16_f32 v174, v68, v69\n"
        "v_cvt_pk_bf16_f32 v175, v70, v71\n"
        "v_cvt_pk_bf16_f32 v176, v52, v53\n"
        "v_cvt_pk_bf16_f32 v177, v54, v55\n"
        "v_cvt_pk_bf16_f32 v178, v72, v73\n"
        "v_cvt_pk_bf16_f32 v179, v74, v75\n"
        "buffer_store_dwordx4 v[172:175], v167, s[52:55], 0 offen sc1\n"
        "v_cvt_pk_bf16_f32 v180, v56, v57\n"
        "v_cvt_pk_bf16_f32 v181, v58, v59\n"
        "v_cvt_pk_bf16_f32 v182, v76, v77\n"
        "v_cvt_pk_bf16_f32 v183, v78, v79\n"
        "buffer_store_dwordx4 v[176:179], v167, s[52:55], 0 offen offset:1024 sc1\n"
        "v_cvt_pk_bf16_f32 v184, v60, v61\n"
        "v_cvt_pk_bf16_f32 v185, v62, v63\n"
        "v_cvt_pk_bf16_f32 v186, v80, v81\n"
        "v_cvt_pk_bf16_f32 v187, v82, v83\n"
        "buffer_store_dwordx4 v[180:183], v167, s[52:55], 0 offen offset:2048 sc1\n"
        "v_cvt_pk_bf16_f32 v188, v64, v65\n"
        "v_cvt_pk_bf16_f32 v189, v66, v67\n"
        "v_cvt_pk_bf16_f32 v190, v84, v85\n"
        "v_cvt_pk_bf16_f32 v191, v86, v87\n"
        "buffer_store_dwordx4 v[184:187], v167, s[52:55], 0 offen offset:3072 sc1\n"
        "v_cvt_pk_bf16_f32 v192, v88, v89\n"
        "v_cvt_pk_bf16_f32 v193, v90, v91\n"
        "v_cvt_pk_bf16_f32 v194, v108, v109\n"
        "v_cvt_pk_bf16_f32 v195, v110, v111\n"
        "buffer_store_dwordx4 v[188:191], v167, s[52:55], s41 offen sc1\n"
        "v_cvt_pk_bf16_f32 v196, v92, v93\n"
        "v_cvt_pk_bf16_f32 v197, v94, v95\n"
        "v_cvt_pk_bf16_f32 v198, v112, v113\n"
        "v_cvt_pk_bf16_f32 v199, v114, v115\n"
        "buffer_store_dwordx4 v[192:195], v167, s[52:55], s41 offen offset:1024 sc1\n"
        "v_cvt_pk_bf16_f32 v200, v96, v97\n"
        "v_cvt_pk_bf16_f32 v201, v98, v99\n"
        "v_cvt_pk_bf16_f32 v202, v116, v117\n"
        "v_cvt_pk_bf16_f32 v203, v118, v119\n"
        "buffer_store_dwordx4 v[196:199], v167, s[52:55], s41 offen offset:2048 sc1\n"
        "v_cvt_pk_bf16_f32 v204, v100, v101\n"
        "v_cvt_pk_bf16_f32 v205, v102, v103\n"
        "v_cvt_pk_bf16_f32 v206, v120, v121\n"
        "v_cvt_pk_bf16_f32 v207, v122, v123\n"
        "buffer_store_dwordx4 v[200:203], v167, s[52:55], s41 offen offset:3072 sc1\n"
        "v_cvt_pk_bf16_f32 v208, v104, v105\n"
        "v_cvt_pk_bf16_f32 v209, v106, v107\n"
        "v_cvt_pk_bf16_f32 v210, v124, v125\n"
        "v_cvt_pk_bf16_f32 v211, v126, v127\n"
        "buffer_store_dwordx4 v[204:207], v167, s[52:55], s42 offen sc1\n"
        "s_nop 1\n"
        "buffer_store_dwordx4 v[208:211], v167, s[52:55], s42 offen offset:1024 sc1\n"
        "s_branch 6f\n"
        "5:\n"
        "v_cvt_pk_bf16_f32 v172, v48, v49\n"
        "v_cvt_pk_bf16_f32 v173, v50, v51\n"
        "v_cvt_pk_bf16_f32 v174, v68, v69\n"
        "v_cvt_pk_bf16_f32 v175, v70, v71\n"
        "v_cvt_pk_bf16_f32 v176, v52, v53\n"
        "v_cvt_pk_bf16_f32 v177, v54, v55\n"
        "v_cvt_pk_bf16_f32 v178, v72, v73\n"
        "v_cvt_pk_bf16_f32 v179, v74, v75\n"
        "ds_write_b128 v168, v[172:175]\n"
        "v_cvt_pk_bf16_f32 v180, v56, v57\n"
        "v_cvt_pk_bf16_f32 v181, v58, v59\n"
        "v_cvt_pk_bf16_f32 v182, v76, v77\n"
        "v_cvt_pk_bf16_f32 v183, v78, v79\n"
        "ds_write_b128 v168, v[176:179] offset:1024\n"
        "v_cvt_pk_bf16_f32 v184, v60, v61\n"
        "v_cvt_pk_bf16_f32 v185, v62, v63\n"
        "v_cvt_pk_bf16_f32 v186, v80, v81\n"
        "v_cvt_pk_bf16_f32 v187, v82, v83\n"
        "ds_write_b128 v168, v[180:183] offset:2048\n"
        "v_cvt_pk_bf16_f32 v188, v64, v65\n"
        "v_cvt_pk_bf16_f32 v189, v66, v67\n"
        "v_cvt_pk_bf16_f32 v190, v84, v85\n"
        "v_cvt_pk_bf16_f32 v191, v86, v87\n"
        "ds_write_b128 v168, v[184:187] offset:3072\n"
        "v_cvt_pk_bf16_f32 v192, v88, v89\n"
        "v_cvt_pk_bf16_f32 v193, v90, v91\n"
        "v_cvt_pk_bf16_f32 v194, v108, v109\n"
        "v_cvt_pk_bf16_f32 v195, v110, v111\n"
        "ds_write_b128 v168, v[188:191] offset:4096\n"
        "v_cvt_pk_bf16_f32 v196, v92, v93\n"
        "v_cvt_pk_bf16_f32 v197, v94, v95\n"
        "v_cvt_pk_bf16_f32 v198, v112, v113\n"
        "v_cvt_pk_bf16_f32 v199, v114, v115\n"
        "ds_write_b128 v168, v[192:195] offset:5120\n"
        "v_cvt_pk_bf16_f32 v200, v96, v97\n"
        "v_cvt_pk_bf16_f32 v201, v98, v99\n"
        "v_cvt_pk_bf16_f32 v202, v116, v117\n"
        "v_cvt_pk_bf16_f32 v203, v118, v119\n"
        "ds_write_b128 v168, v[196:199] offset:6144\n"
        "v_cvt_pk_bf16_f32 v204, v100, v101\n"
        "v_cvt_pk_bf16_f32 v205, v102, v103\n"
        "v_cvt_pk_bf16_f32 v206, v120, v121\n"
        "v_cvt_pk_bf16_f32 v207, v122, v123\n"
        "ds_write_b128 v168, v[200:203] offset:7168\n"
        "v_cvt_pk_bf16_f32 v208, v104, v105\n"
        "v_cvt_pk_bf16_f32 v209, v106, v107\n"
        "v_cvt_pk_bf16_f32 v210, v124, v125\n"
        "v_cvt_pk_bf16_f32 v211, v126, v127\n"
        "ds_write_b128 v168, v[204:207] offset:8192\n"
        "s_nop 1\n"
        "ds_write_b128 v168, v[208:211] offset:9216\n"
        "6:\n"
        "buffer_load_dword v15, v8, s[52:55], 0 offen sc1\n"
        "7:\n"
        "s_waitcnt vmcnt(0)\n"
        "v_cmp_eq_u32 vcc, -1, v15\n"
        "s_add_u32 s40, s40, 1\n"
        "s_cmp_lt_u32 s40, 262144\n"
        "s_cbranch_vccz 8f\n"
        "s_sleep 2\n"
        "buffer_load_dword v15, v8, s[52:55], 0 offen sc1\n"
        "s_cbranch_scc1 7b\n"
        "8:\n"
        "buffer_load_dwordx4 v[16:19], v9, s[52:55], s56 offen sc1\n"
        "buffer_load_dwordx4 v[20:23], v9, s[52:55], s57 offen sc1\n"
        "buffer_load_dwordx4 v[24:27], v9, s[52:55], s58 offen sc1\n"
        "buffer_load_dwordx4 v[28:31], v154, s[52:55], s56 offen sc1\n"
        "buffer_load_dwordx4 v[32:35], v154, s[52:55], s57 offen sc1\n"
        "buffer_load_dwordx4 v[36:39], v154, s[52:55], s58 offen sc1\n"
        "buffer_load_dwordx4 v[40:43], v155, s[52:55], s56 offen sc1\n"
        "buffer_load_dwordx4 v[44:47], v155, s[52:55], s57 offen sc1\n"
        "buffer_load_dwordx4 v[48:51], v155, s[52:55], s58 offen sc1\n"
        "s_waitcnt lgkmcnt(0)\n"
        "s_barrier\n"
        "ds_read_b128 v[88:91], v10\n"
        "ds_read_b128 v[92:95], v10 offset:20480\n"
        "ds_read_b128 v[52:55], v10 offset:1024\n"
        "ds_read_b128 v[56:59], v10 offset:21504\n"
        "ds_read_b128 v[60:63], v10 offset:2048\n"
        "ds_read_b128 v[64:67], v10 offset:22528\n"
        "ds_read_b128 v[68:71], v10 offset:3072\n"
        "ds_read_b128 v[72:75], v10 offset:23552\n"
        "ds_read_b128 v[76:79], v159\n"
        "ds_read_b128 v[96:99], v11\n"
        "ds_read_b128 v[100:103], v11 offset:20480\n"
        "ds_read_b128 v[220:223], v11 offset:1024\n"
        "ds_read_b128 v[224:227], v11 offset:21504\n"
        "ds_read_b128 v[228:231], v11 offset:2048\n"
        "ds_read_b128 v[232:235], v11 offset:22528\n"
        "ds_read_b128 v[236:239], v11 offset:3072\n"
        "ds_read_b128 v[240:243], v11 offset:23552\n"
        "ds_read_b128 v[244:247], v159 offset:4096\n"
        "s_waitcnt lgkmcnt(9)\n"
        "v_pk_add_f32 v[88:89], v[88:89], v[52:53]\n"
        "v_pk_add_f32 v[90:91], v[90:91], v[54:55]\n"
        "v_pk_add_f32 v[92:93], v[92:93], v[56:57]\n"
        "v_pk_add_f32 v[94:95], v[94:95], v[58:59]\n"
        "v_pk_add_f32 v[88:89], v[88:89], v[60:61]\n"
        "v_pk_add_f32 v[90:91], v[90:91], v[62:63]\n"
        "v_pk_add_f32 v[92:93], v[92:93], v[64:65]\n"
        "v_pk_add_f32 v[94:95], v[94:95], v[66:67]\n"
        "v_pk_add_f32 v[88:89], v[88:89], v[68:69]\n"
        "v_pk_add_f32 v[90:91], v[90:91], v[70:71]\n"
        "v_pk_add_f32 v[92:93], v[92:93], v[72:73]\n"
        "v_pk_add_f32 v[94:95], v[94:95], v[74:75]\n"
        "v_lshlrev_b32 v120, 16, v76\n"
        "v_and_b32 v121, 0xffff0000, v76\n"
        "v_lshlrev_b32 v122, 16, v77\n"
        "v_and_b32 v123, 0xffff0000, v77\n"
        "v_lshlrev_b32 v124, 16, v78\n"
        "v_and_b32 v125, 0xffff0000, v78\n"
        "v_lshlrev_b32 v126, 16, v79\n"
        "v_and_b32 v127, 0xffff0000, v79\n"
        "v_pk_fma_f32 v[88:89], v[148:149], v[120:121], v[88:89] op_sel:[0,0,0] op_sel_hi:[0,1,1]\n"
        "v_pk_fma_f32 v[90:91], v[148:149], v[122:123], v[90:91] op_sel:[0,0,0] op_sel_hi:[0,1,1]\n"
        "v_pk_fma_f32 v[92:93], v[148:149], v[124:125], v[92:93] op_sel:[1,0,0] op_sel_hi:[1,1,1]\n"
        "v_pk_fma_f32 v[94:95], v[148:149], v[126:127], v[94:95] op_sel:[1,0,0] op_sel_hi:[1,1,1]\n"
        "ds_read_b128 v[104:107], v158\n"
        "ds_read_b128 v[108:111], v158 offset:20480\n"
        "ds_read_b128 v[52:55], v158 offset:1024\n"
        "ds_read_b128 v[56:59], v158 offset:21504\n"
        "ds_read_b128 v[60:63], v158 offset:2048\n"
        "ds_read_b128 v[64:67], v158 offset:22528\n"
        "ds_read_b128 v[68:71], v158 offset:3072\n"
        "ds_read_b128 v[72:75], v158 offset:23552\n"
        "ds_read_b128 v[76:79], v159 offset:8192\n"
        "s_waitcnt lgkmcnt(9)\n"
        "v_pk_add_f32 v[96:97], v[96:97], v[220:221]\n"
        "v_pk_add_f32 v[98:99], v[98:99], v[222:223]\n"
        "v_pk_add_f32 v[100:101], v[100:101], v[224:225]\n"
        "v_pk_add_f32 v[102:103], v[102:103], v[226:227]\n"
        "v_pk_add_f32 v[96:97], v[96:97], v[228:229]\n"
        "v_pk_add_f32 v[98:99], v[98:99], v[230:231]\n"
        "v_pk_add_f32 v[100:101], v[100:101], v[232:233]\n"
        "v_pk_add_f32 v[102:103], v[102:103], v[234:235]\n"
        "v_pk_add_f32 v[96:97], v[96:97], v[236:237]\n"
        "v_pk_add_f32 v[98:99], v[98:99], v[238:239]\n"
        "v_pk_add_f32 v[100:101], v[100:101], v[240:241]\n"
        "v_pk_add_f32 v[102:103], v[102:103], v[242:243]\n"
        "v_lshlrev_b32 v120, 16, v244\n"
        "v_and_b32 v121, 0xffff0000, v244\n"
        "v_lshlrev_b32 v122, 16, v245\n"
        "v_and_b32 v123, 0xffff0000, v245\n"
        "v_lshlrev_b32 v124, 16, v246\n"
        "v_and_b32 v125, 0xffff0000, v246\n"
        "v_lshlrev_b32 v126, 16, v247\n"
        "v_and_b32 v127, 0xffff0000, v247\n"
        "v_pk_fma_f32 v[96:97], v[150:151], v[120:121], v[96:97] op_sel:[0,0,0] op_sel_hi:[0,1,1]\n"
        "v_pk_fma_f32 v[98:99], v[150:151], v[122:123], v[98:99] op_sel:[0,0,0] op_sel_hi:[0,1,1]\n"
        "v_pk_fma_f32 v[100:101], v[150:151], v[124:125], v[100:101] op_sel:[1,0,0] op_sel_hi:[1,1,1]\n"
        "v_pk_fma_f32 v[102:103], v[150:151], v[126:127], v[102:103] op_sel:[1,0,0] op_sel_hi:[1,1,1]\n"
        "s_waitcnt lgkmcnt(0)\n"
        "v_pk_add_f32 v[104:105], v[104:105], v[52:53]\n"
        "v_pk_add_f32 v[106:107], v[106:107], v[54:55]\n"
        "v_pk_add_f32 v[108:109], v[108:109], v[56:57]\n"
        "v_pk_add_f32 v[110:111], v[110:111], v[58:59]\n"
        "v_pk_add_f32 v[104:105], v[104:105], v[60:61]\n"
        "v_pk_add_f32 v[106:107], v[106:107], v[62:63]\n"
        "v_pk_add_f32 v[108:109], v[108:109], v[64:65]\n"
        "v_pk_add_f32 v[110:111], v[110:111], v[66:67]\n"
        "v_pk_add_f32 v[104:105], v[104:105], v[68:69]\n"
        "v_pk_add_f32 v[106:107], v[106:107], v[70:71]\n"
        "v_pk_add_f32 v[108:109], v[108:109], v[72:73]\n"
        "v_pk_add_f32 v[110:111], v[110:111], v[74:75]\n"
        "v_lshlrev_b32 v120, 16, v76\n"
        "v_and_b32 v121, 0xffff0000, v76\n"
        "v_lshlrev_b32 v122, 16, v77\n"
        "v_and_b32 v123, 0xffff0000, v77\n"
        "v_lshlrev_b32 v124, 16, v78\n"
        "v_and_b32 v125, 0xffff0000, v78\n"
        "v_lshlrev_b32 v126, 16, v79\n"
        "v_and_b32 v127, 0xffff0000, v79\n"
        "v_pk_fma_f32 v[104:105], v[152:153], v[120:121], v[104:105] op_sel:[0,0,0] op_sel_hi:[0,1,1]\n"
        "v_pk_fma_f32 v[106:107], v[152:153], v[122:123], v[106:107] op_sel:[0,0,0] op_sel_hi:[0,1,1]\n"
        "v_pk_fma_f32 v[108:109], v[152:153], v[124:125], v[108:109] op_sel:[1,0,0] op_sel_hi:[1,1,1]\n"
        "v_pk_fma_f32 v[110:111], v[152:153], v[126:127], v[110:111] op_sel:[1,0,0] op_sel_hi:[1,1,1]\n"
        "s_waitcnt vmcnt(6)\n"
        "10:\n"
        "v_max3_u32 v120, v16, v17, v18\n"
        "v_max3_u32 v120, v120, v19, v20\n"
        "v_max3_u32 v120, v120, v21, v22\n"
        "v_max3_u32 v120, v120, v23, v24\n"
        "v_max3_u32 v120, v120, v25, v26\n"
        "v_max_u32 v120, v120, v27\n"
        "v_cmp_eq_u32 vcc, -1, v120\n"
        "v_lshlrev_b32 v120, 16, v16\n"
        "v_and_b32 v121, 0xffff0000, v16\n"
        "v_lshlrev_b32 v122, 16, v17\n"
        "v_and_b32 v123, 0xffff0000, v17\n"
        "v_lshlrev_b32 v124, 16, v18\n"
        "v_and_b32 v125, 0xffff0000, v18\n"
        "v_lshlrev_b32 v126, 16, v19\n"
        "v_and_b32 v127, 0xffff0000, v19\n"
        "s_cbranch_vccnz 20f\n"
        "15:\n"
        "v_pk_fma_f32 v[88:89], v[148:149], v[120:121], v[88:89] op_sel:[0,0,0] op_sel_hi:[0,1,1]\n"
        "v_pk_fma_f32 v[90:91], v[148:149], v[122:123], v[90:91] op_sel:[0,0,0] op_sel_hi:[0,1,1]\n"
        "v_pk_fma_f32 v[92:93], v[148:149], v[124:125], v[92:93] op_sel:[1,0,0] op_sel_hi:[1,1,1]\n"
        "v_pk_fma_f32 v[94:95], v[148:149], v[126:127], v[94:95] op_sel:[1,0,0] op_sel_hi:[1,1,1]\n"
        "v_lshlrev_b32 v120, 16, v20\n"
        "v_and_b32 v121, 0xffff0000, v20\n"
        "v_lshlrev_b32 v122, 16, v21\n"
        "v_and_b32 v123, 0xffff0000, v21\n"
        "v_lshlrev_b32 v124, 16, v22\n"
        "v_and_b32 v125, 0xffff0000, v22\n"
        "v_lshlrev_b32 v126, 16, v23\n"
        "v_and_b32 v127, 0xffff0000, v23\n"
        "v_pk_fma_f32 v[88:89], v[148:149], v[120:121], v[88:89] op_sel:[0,0,0] op_sel_hi:[0,1,1] neg_lo:[1,0,0] neg_hi:[1,0,0]\n"
        "v_pk_fma_f32 v[90:91], v[148:149], v[122:123], v[90:91] op_sel:[0,0,0] op_sel_hi:[0,1,1] neg_lo:[1,0,0] neg_hi:[1,0,0]\n"
        "v_pk_fma_f32 v[92:93], v[148:149], v[124:125], v[92:93] op_sel:[1,0,0] op_sel_hi:[1,1,1] neg_lo:[1,0,0] neg_hi:[1,0,0]\n"
        "v_pk_fma_f32 v[94:95], v[148:149], v[126:127], v[94:95] op_sel:[1,0,0] op_sel_hi:[1,1,1] neg_lo:[1,0,0] neg_hi:[1,0,0]\n"
        "v_lshlrev_b32 v120, 16, v24\n"
        "v_and_b32 v121, 0xffff0000, v24\n"
        "v_lshlrev_b32 v122, 16, v25\n"
        "v_and_b32 v123, 0xffff0000, v25\n"
        "v_lshlrev_b32 v124, 16, v26\n"
        "v_and_b32 v125, 0xffff0000, v26\n"
        "v_lshlrev_b32 v126, 16, v27\n"
        "v_and_b32 v127, 0xffff0000, v27\n"
        "v_pk_fma_f32 v[88:89], v[148:149], v[120:121], v[88:89] op_sel:[0,0,0] op_sel_hi:[0,1,1] neg_lo:[1,0,0] neg_hi:[1,0,0]\n"
        "v_pk_fma_f32 v[90:91], v[148:149], v[122:123], v[90:91] op_sel:[0,0,0] op_sel_hi:[0,1,1] neg_lo:[1,0,0] neg_hi:[1,0,0]\n"
        "v_pk_fma_f32 v[92:93], v[148:149], v[124:125], v[92:93] op_sel:[1,0,0] op_sel_hi:[1,1,1] neg_lo:[1,0,0] neg_hi:[1,0,0]\n"
        "v_pk_fma_f32 v[94:95], v[148:149], v[126:127], v[94:95] op_sel:[1,0,0] op_sel_hi:[1,1,1] neg_lo:[1,0,0] neg_hi:[1,0,0]\n"
        "v_cvt_pk_bf16_f32 v52, v88, v89\n"
        "v_cvt_pk_bf16_f32 v53, v90, v91\n"
        "v_cvt_pk_bf16_f32 v54, v92, v93\n"
        "v_cvt_pk_bf16_f32 v55, v94, v95\n"
        "s_nop 1\n"
        "v_permlane16_swap_b32 v52, v54\n"
        "v_permlane16_swap_b32 v53, v55\n"
        "buffer_store_dwordx4 v[52:55], v12, s[44:47], 0 offen\n"
        "s_waitcnt vmcnt(4)\n"
        "11:\n"
        "v_max3_u32 v120, v28, v29, v30\n"
        "v_max3_u32 v120, v120, v31, v32\n"
        "v_max3_u32 v120, v120, v33, v34\n"
        "v_max3_u32 v120, v120, v35, v36\n"
        "v_max3_u32 v120, v120, v37, v38\n"
        "v_max_u32 v120, v120, v39\n"
        "v_cmp_eq_u32 vcc, -1, v120\n"
        "v_lshlrev_b32 v120, 16, v28\n"
        "v_and_b32 v121, 0xffff0000, v28\n"
        "v_lshlrev_b32 v122, 16, v29\n"
        "v_and_b32 v123, 0xffff0000, v29\n"
        "v_lshlrev_b32 v124, 16, v30\n"
        "v_and_b32 v125, 0xffff0000, v30\n"
        "v_lshlrev_b32 v126, 16, v31\n"
        "v_and_b32 v127, 0xffff0000, v31\n"
        "s_cbranch_vccnz 21f\n"
        "16:\n"
        "v_pk_fma_f32 v[96:97], v[150:151], v[120:121], v[96:97] op_sel:[0,0,0] op_sel_hi:[0,1,1]\n"
        "v_pk_fma_f32 v[98:99], v[150:151], v[122:123], v[98:99] op_sel:[0,0,0] op_sel_hi:[0,1,1]\n"
        "v_pk_fma_f32 v[100:101], v[150:151], v[124:125], v[100:101] op_sel:[1,0,0] op_sel_hi:[1,1,1]\n"
        "v_pk_fma_f32 v[102:103], v[150:151], v[126:127], v[102:103] op_sel:[1,0,0] op_sel_hi:[1,1,1]\n"
        "v_lshlrev_b32 v120, 16, v32\n"
        "v_and_b32 v121, 0xffff0000, v32\n"
        "v_lshlrev_b32 v122, 16, v33\n"
        "v_and_b32 v123, 0xffff0000, v33\n"
        "v_lshlrev_b32 v124, 16, v34\n"
        "v_and_b32 v125, 0xffff0000, v34\n"
        "v_lshlrev_b32 v126, 16, v35\n"
        "v_and_b32 v127, 0xffff0000, v35\n"
        "v_pk_fma_f32 v[96:97], v[150:151], v[120:121], v[96:97] op_sel:[0,0,0] op_sel_hi:[0,1,1] neg_lo:[1,0,0] neg_hi:[1,0,0]\n"
        "v_pk_fma_f32 v[98:99], v[150:151], v[122:123], v[98:99] op_sel:[0,0,0] op_sel_hi:[0,1,1] neg_lo:[1,0,0] neg_hi:[1,0,0]\n"
        "v_pk_fma_f32 v[100:101], v[150:151], v[124:125], v[100:101] op_sel:[1,0,0] op_sel_hi:[1,1,1] neg_lo:[1,0,0] neg_hi:[1,0,0]\n"
        "v_pk_fma_f32 v[102:103], v[150:151], v[126:127], v[102:103] op_sel:[1,0,0] op_sel_hi:[1,1,1] neg_lo:[1,0,0] neg_hi:[1,0,0]\n"
        "v_lshlrev_b32 v120, 16, v36\n"
        "v_and_b32 v121, 0xffff0000, v36\n"
        "v_lshlrev_b32 v122, 16, v37\n"
        "v_and_b32 v123, 0xffff0000, v37\n"
        "v_lshlrev_b32 v124, 16, v38\n"
        "v_and_b32 v125, 0xffff0000, v38\n"
        "v_lshlrev_b32 v126, 16, v39\n"
        "v_and_b32 v127, 0xffff0000, v39\n"
        "v_pk_fma_f32 v[96:97], v[150:151], v[120:121], v[96:97] op_sel:[0,0,0] op_sel_hi:[0,1,1] neg_lo:[1,0,0] neg_hi:[1,0,0]\n"
        "v_pk_fma_f32 v[98:99], v[150:151], v[122:123], v[98:99] op_sel:[0,0,0] op_sel_hi:[0,1,1] neg_lo:[1,0,0] neg_hi:[1,0,0]\n"
        "v_pk_fma_f32 v[100:101], v[150:151], v[124:125], v[100:101] op_sel:[1,0,0] op_sel_hi:[1,1,1] neg_lo:[1,0,0] neg_hi:[1,0,0]\n"
        "v_pk_fma_f32 v[102:103], v[150:151], v[126:127], v[102:103] op_sel:[1,0,0] op_sel_hi:[1,1,1] neg_lo:[1,0,0] neg_hi:[1,0,0]\n"
        "v_cvt_pk_bf16_f32 v56, v96, v97\n"
        "v_cvt_pk_bf16_f32 v57, v98, v99\n"
        "v_cvt_pk_bf16_f32 v58, v100, v101\n"
        "v_cvt_pk_bf16_f32 v59, v102, v103\n"
        "s_nop 1\n"
        "v_permlane16_swap_b32 v56, v58\n"
        "v_permlane16_swap_b32 v57, v59\n"
        "buffer_store_dwordx4 v[56:59], v13, s[44:47], 0 offen\n"
        "s_cmp_ge_u32 %[w], 2\n"
        "s_cbranch_scc1 30f\n"
        "s_waitcnt vmcnt(2)\n"
        "12:\n"
        "v_max3_u32 v120, v40, v41, v42\n"
        "v_max3_u32 v120, v120, v43, v44\n"
        "v_max3_u32 v120, v120, v45, v46\n"
        "v_max3_u32 v120, v120, v47, v48\n"
        "v_max3_u32 v120, v120, v49, v50\n"
        "v_max_u32 v120, v120, v51\n"
        "v_cmp_eq_u32 vcc, -1, v120\n"
        "v_lshlrev_b32 v120, 16, v40\n"
        "v_and_b32 v121, 0xffff0000, v40\n"
        "v_lshlrev_b32 v122, 16, v41\n"
        "v_and_b32 v123, 0xffff0000, v41\n"
        "v_lshlrev_b32 v124, 16, v42\n"
        "v_and_b32 v125, 0xffff0000, v42\n"
        "v_lshlrev_b32 v126, 16, v43\n"
        "v_and_b32 v127, 0xffff0000, v43\n"
        "s_cbranch_vccnz 22f\n"
        "17:\n"
        "v_pk_fma_f32 v[104:105], v[152:153], v[120:121], v[104:105] op_sel:[0,0,0] op_sel_hi:[0,1,1]\n"
        "v_pk_fma_f32 v[106:107], v[152:153], v[122:123], v[106:107] op_sel:[0,0,0] op_sel_hi:[0,1,1]\n"
        "v_pk_fma_f32 v[108:109], v[152:153], v[124:125], v[108:109] op_sel:[1,0,0] op_sel_hi:[1,1,1]\n"
        "v_pk_fma_f32 v[110:111], v[152:153], v[126:127], v[110:111] op_sel:[1,0,0] op_sel_hi:[1,1,1]\n"
        "v_lshlrev_b32 v120, 16, v44\n"
        "v_and_b32 v121, 0xffff0000, v44\n"
        "v_lshlrev_b32 v122, 16, v45\n"
        "v_and_b32 v123, 0xffff0000, v45\n"
        "v_lshlrev_b32 v124, 16, v46\n"
        "v_and_b32 v125, 0xffff0000, v46\n"
        ".Lk_pad11:\n"
        "s_cbranch_execz .Lk_ret\n"
        "v_lshlrev_b32 v126, 16, v47\n"
        "v_and_b32 v127, 0xffff0000, v47\n"
        "v_pk_fma_f32 v[104:105], v[152:153], v[120:121], v[104:105] op_sel:[0,0,0] op_sel_hi:[0,1,1] neg_lo:[1,0,0] neg_hi:[1,0,0]\n"
        "v_pk_fma_f32 v[106:107], v[152:153], v[122:123], v[106:107] op_sel:[0,0,0] op_sel_hi:[0,1,1] neg_lo:[1,0,0] neg_hi:[1,0,0]\n"
        "v_pk_fma_f32 v[108:109], v[152:153], v[124:125], v[108:109] op_sel:[1,0,0] op_sel_hi:[1,1,1] neg_lo:[1,0,0] neg_hi:[1,0,0]\n"
        "v_pk_fma_f32 v[110:111], v[152:153], v[126:127], v[110:111] op_sel:[1,0,0] op_sel_hi:[1,1,1] neg_lo:[1,0,0] neg_hi:[1,0,0]\n"
        "v_lshlrev_b32 v120, 16, v48\n"
        "v_and_b32 v121, 0xffff0000, v48\n"
        "v_lshlrev_b32 v122, 16, v49\n"
        "v_and_b32 v123, 0xffff0000, v49\n"
        "v_lshlrev_b32 v124, 16, v50\n"
        "v_and_b32 v125, 0xffff0000, v50\n"
        "v_lshlrev_b32 v126, 16, v51\n"
        "v_and_b32 v127, 0xffff0000, v51\n"
        "v_pk_fma_f32 v[104:105], v[152:153], v[120:121], v[104:105] op_sel:[0,0,0] op_sel_hi:[0,1,1] neg_lo:[1,0,0] neg_hi:[1,0,0]\n"
        "v_pk_fma_f32 v[106:107], v[152:153], v[122:123], v[106:107] op_sel:[0,0,0] op_sel_hi:[0,1,1] neg_lo:[1,0,0] neg_hi:[1,0,0]\n"
        "v_pk_fma_f32 v[108:109], v[152:153], v[124:125], v[108:109] op_sel:[1,0,0] op_sel_hi:[1,1,1] neg_lo:[1,0,0] neg_hi:[1,0,0]\n"
        "v_pk_fma_f32 v[110:111], v[152:153], v[126:127], v[110:111] op_sel:[1,0,0] op_sel_hi:[1,1,1] neg_lo:[1,0,0] neg_hi:[1,0,0]\n"
        "v_cvt_pk_bf16_f32 v60, v104, v105\n"
        "v_cvt_pk_bf16_f32 v61, v106, v107\n"
        "v_cvt_pk_bf16_f32 v62, v108, v109\n"
        "v_cvt_pk_bf16_f32 v63, v110, v111\n"
        "s_nop 1\n"
        "v_permlane16_swap_b32 v60, v62\n"
        "v_permlane16_swap_b32 v61, v63\n"
        "buffer_store_dwordx4 v[60:63], v14, s[44:47], 0 offen\n"
        "s_branch 30f\n"
        "20:\n"
        "s_add_u32 s40, s40, 1\n"
        "s_cmp_lt_u32 s40, 262144\n"
        "s_cbranch_scc0 15b\n"
        "buffer_load_dwordx4 v[16:19], v9, s[52:55], s56 offen sc1\n"
        "buffer_load_dwordx4 v[20:23], v9, s[52:55], s57 offen sc1\n"
        "buffer_load_dwordx4 v[24:27], v9, s[52:55], s58 offen sc1\n"
        "s_waitcnt vmcnt(0)\n"
        "s_branch 10b\n"
        "21:\n"
        "s_add_u32 s40, s40, 1\n"
        "s_cmp_lt_u32 s40, 262144\n"
        "s_cbranch_scc0 16b\n"
        "buffer_load_dwordx4 v[28:31], v154, s[52:55], s56 offen sc1\n"
        "buffer_load_dwordx4 v[32:35], v154, s[52:55], s57 offen sc1\n"
        "buffer_load_dwordx4 v[36:39], v154, s[52:55], s58 offen sc1\n"
        "s_waitcnt vmcnt(0)\n"
        "s_branch 11b\n"
        "22:\n"
        "s_add_u32 s40, s40, 1\n"
        "s_cmp_lt_u32 s40, 262144\n"
        "s_cbranch_scc0 17b\n"
        "buffer_load_dwordx4 v[40:43], v155, s[52:55], s56 offen sc1\n"
        "buffer_load_dwordx4 v[44:47], v155, s[52:55], s57 offen sc1\n"
        "buffer_load_dwordx4 v[48:51], v155, s[52:55], s58 offen sc1\n"
        "s_waitcnt vmcnt(0)\n"
        "s_branch 12b\n"
        "30:\n"
        ".Lk_end:\n"
        : [ln] "+v"(lane)
        : [b] "s"(b), [w] "s"(w), [ka] "s"(__builtin_amdgcn_kernarg_segment_ptr()), [wsn] "n"(WS_END), "s"(lb)
        : "v8", "v9", "v10", "v11", "v12", "v13", "v14", "v15", "v16", "v17", "v18", "v19", "v20", "v21", "v22", "v23", "v24", "v25", "v26", "v27", "v28", "v29", "v30", "v31", "v32", "v33", "v34", "v35", "v36", "v37", "v38", "v39", "v40", "v41", "v42", "v43", "v44", "v45", "v46", "v47", "v48", "v49", "v50", "v51", "v52", "v53", "v54", "v55", "v56", "v57", "v58", "v59", "v60", "v61", "v62", "v63", "v64", "v65", "v66", "v67", "v68", "v69", "v70", "v71", "v72", "v73", "v74", "v75", "v76", "v77", "v78", "v79", "v80", "v81", "v82", "v83", "v84", "v85", "v86", "v87", "v88", "v89", "v90", "v91", "v92", "v93", "v94", "v95", "v96", "v97", "v98", "v99", "v100", "v101", "v102", "v103", "v104", "v105", "v106", "v107", "v108", "v109", "v110", "v111", "v112", "v113", "v114", "v115", "v116", "v117", "v118", "v119", "v120", "v121", "v122", "v123", "v124", "v125", "v126", "v127", "v128", "v129", "v130", "v131", "v132", "v133", "v134", "v135", "v136", "v137", "v138", "v139", "v140", "v141", "v142", "v143", "v144", "v145", "v146", "v147", "v148", "v149", "v150", "v151", "v152", "v153", "v154", "v155", "v156", "v157", "v158", "v159", "v160", "v161", "v162", "v163", "v164", "v165", "v166", "v167", "v168", "v169", "v170", "v171", "v172", "v173", "v174", "v175", "v176", "v177", "v178", "v179", "v180", "v181", "v182", "v183", "v184", "v185", "v186", "v187", "v188", "v189", "v190", "v191", "v192", "v193", "v194", "v195", "v196", "v197", "v198", "v199", "v200", "v201", "v202", "v203", "v204", "v205", "v206", "v207", "v208", "v209", "v210", "v211", "v212", "v213", "v214", "v215", "v216", "v217", "v218", "v219", "v220", "v221", "v222", "v223", "v224", "v225", "v226", "v227", "v228", "v229", "v230", "v231", "v232", "v233", "v234", "v235", "v236", "v237", "v238", "v239", "v240", "v241", "v242", "v243", "v244", "v245", "v246", "v247", "v248", "v249", "v250", "v251", "v252", "v253", "v254", "v255", "s20", "s21", "s22", "s23", "s24", "s25", "s26", "s27", "s40", "s41", "s42", "s43", "s44", "s45", "s46", "s47", "s48", "s49", "s50", "s51", "s52", "s53", "s54", "s55", "s56", "s57", "s58", "s59", "s60", "s61", "s62", "s63", "s64", "s65", "s66", "s67", "s68", "s69", "s70", "s71", "s72", "s73", "s74", "s75", "s80", "s81", "s82", "s83", "scc", "vcc", "memory");
    } else {
        u32x8 v3; u32x16 vat, sat, wf2; u32x32 wf0, wf1; u32x4 rd, rw; unsigned bad; u64 epoch;
        asm volatile(
        "s_load_dwordx16 s[16:31], %[ka], 0x0\n"
        "s_setprio 1\n"
        "s_mov_b32 s80, m0\n"
        "s_lshr_b32 s94, %[b], 3\n"
        "s_mul_i32 s94, s94, 4\n"
        "s_add_u32 s94, s94, %[w]\n"
        "s_mul_i32 s94, s94, 3\n"
        "v_cmp_gt_u32 vcc, 3, %[ln]\n"
        "s_nop 1\n"
        "v_cndmask_b32 v216, 0, %[ln], vcc\n"
        "v_add_u32 v217, s94, v216\n"
        "v_cmp_gt_u32 vcc, (.Lk_end-.Lk_beg)/128, v217\n"
        "s_nop 1\n"
        "v_cndmask_b32 v216, 0, v217, vcc\n"
        "v_lshlrev_b32 v15, 7, v216\n"
        "global_load_dword v194, v15, %[pc]\n"
        "s_lshr_b32 s52, %[b], 3\n"
        "s_and_b32 s52, s52, 7\n"
        "s_mul_i32 s53, s52, 2\n"
        "s_and_b32 s54, %[b], 7\n"
        "s_and_b32 s55, s54, 1\n"
        "s_add_u32 s55, s53, s55\n"
        "s_mul_i32 s55, s55, 0x80\n"
        "s_mul_i32 s53, %[w], 32\n"
        "s_add_u32 s53, s55, s53\n"
        "s_mul_i32 s84, s53, 0x500\n"
        "s_mul_i32 s53, %[w], 0xf000\n"
        "s_lshr_b32 s55, %[b], 6\n"
        "s_mul_i32 s56, s55, 4\n"
        "s_lshr_b32 s54, s54, 1\n"
        "s_add_u32 s56, s56, s54\n"
        "s_mul_i32 s57, s56, 0x100\n"
        "s_add_u32 s57, s53, s57\n"
        "s_add_u32 s56, s56, 1\n"
        "s_lshr_b32 s56, s56, 4\n"
        "s_mul_i32 s53, s56, 0x40000000\n"
        "s_add_u32 s85, s57, s53\n"
        "s_mul_i32 s86, %[w], 0x1000\n"
        "s_add_u32 s87, s86, 0x10000\n"
        "s_xor_b32 s56, s56, 1\n"
        "s_add_u32 s55, s52, s55\n"
        "s_mul_i32 s52, s55, 11\n"
        "s_lshr_b32 s52, s52, 5\n"
        "s_mul_i32 s52, s52, 3\n"
        "s_sub_u32 s52, s55, s52\n"
        "s_mul_i32 s52, s52, 3\n"
        "s_add_u32 s54, s52, s54\n"
        "s_and_b32 s52, %[b], 1\n"
        "s_mul_i32 s52, s52, 2\n"
        "s_add_u32 s52, s54, s52\n"
        "s_mul_i32 s52, s56, s52\n"
        "s_mul_i32 s56, s52, 26\n"
        "s_lshr_b32 s56, s56, 8\n"
        "s_mul_i32 s56, s56, 10\n"
        "s_sub_u32 s56, s52, s56\n"
        "s_mul_i32 s81, s56, 0x80\n"
        "v_lshrrev_b32 v216, 3, %[ln]\n"
        "v_and_b32 v217, 7, %[ln]\n"
        "v_xor_b32 v217, v216, v217\n"
        "v_mul_u32_u24 v16, 0x500, v216\n"
        "v_lshl_add_u32 v16, v217, 4, v16\n"
        "v_lshrrev_b32 v217, 4, %[ln]\n"
        "v_and_b32 v216, 15, %[ln]\n"
        "v_lshlrev_b32 v218, 1, v217\n"
        "v_xor_b32 v218, v218, v216\n"
        "v_mul_u32_u24 v20, 0xf00, v217\n"
        "v_lshl_add_u32 v20, v218, 4, v20\n"
        "v_xor_b32 v23, 0x80, v20\n"
        "v_add_u32 v17, 9216, v16\n"
        "v_add_u32 v18, 18432, v16\n"
        "v_add_u32 v19, 27648, v16\n"
        "v_add_u32 v21, 14336, v20\n"
        "v_add_u32 v22, 28672, v23\n"
        "v_add_u32 v23, 43008, v23\n"
        "s_mov_b32 s42, 2621440\n"
        "s_mov_b32 s43, 0x20000\n"
        "s_add_u32 s62, s81, s84\n"
        "s_mov_b32 s82, s81\n"
        "s_waitcnt lgkmcnt(0)\n"
        "s_mov_b64 s[40:41], s[16:17]\n"
        "s_and_b32 s41, s41, 0xffff\n"
        "s_add_u32 m0, s86, 0\n"
        "s_mul_i32 s63, s81, 1920\n"
        "buffer_load_dwordx4 v16, s[40:43], s62 offen lds\n"
        "s_mov_b64 s[48:49], s[18:19]\n"
        "s_and_b32 s49, s49, 0xffff\n"
        "buffer_load_dwordx4 v17, s[40:43], s62 offen offset:1024 lds\n"
        "s_mov_b32 s50, 2457600\n"
        "s_mov_b32 s51, 0x20000\n"
        "buffer_load_dwordx4 v18, s[40:43], s62 offen offset:2048 lds\n"
        "s_add_u32 s63, s63, s85\n"
        "buffer_load_dwordx4 v19, s[40:43], s62 offen offset:3072 lds\n"
        "s_add_u32 m0, s87, 32768\n"
        "s_nop 0\n"
        "buffer_load_dwordx4 v20, s[48:51], s63 offen lds\n"
        "buffer_load_dwordx4 v21, s[48:51], s63 offen offset:1024 lds\n"
        "buffer_load_dwordx4 v22, s[48:51], s63 offen offset:2048 lds\n"
        "buffer_load_dwordx4 v23, s[48:51], s63 offen offset:3072 lds\n"
        "v_mov_b32 v192, 0\n"
        "global_load_dwordx2 v[192:193], v192, s[20:21] sc1\n"
        "s_cmp_lt_u32 %[b], 128\n"
        "s_cselect_b64 s[22:23], s[22:23], s[24:25]\n"
        "s_bitcmp0_b32 %[b], 2\n"
        "s_cselect_b64 s[26:27], s[26:27], s[28:29]\n"
        "s_mov_b64 s[44:45], s[22:23]\n"
        "s_and_b32 s45, s45, 0xffff\n"
        "s_mov_b32 s46, 819200\n"
        "s_mov_b32 s47, 0x20000\n"
        "s_mul_i32 s94, %[w], 0x1000\n"
        "v_and_b32 v216, 15, %[ln]\n"
        "v_lshrrev_b32 v217, 4, %[ln]\n"
        "v_and_b32 v218, 7, %[ln]\n"
        "v_xor_b32 v218, v218, v217\n"
        "v_lshl_add_u32 v11, v216, 7, s94\n"
        "v_lshl_add_u32 v11, v218, 4, v11\n"
        "v_xor_b32 v12, 64, v11\n"
        "v_lshlrev_b32 v13, 7, v216\n"
        "v_lshl_add_u32 v13, v218, 4, v13\n"
        "v_xor_b32 v14, 64, v13\n"
        "v_lshrrev_b32 v216, 3, %[ln]\n"
        "s_and_b32 s52, %[w], 1\n"
        "s_mul_i32 s53, s52, 4\n"
        "v_add_u32 v217, s53, v216\n"
        "v_xor_b32 v217, v217, %[ln]\n"
        "v_and_b32 v217, 7, v217\n"
        "v_mul_u32_u24 v8, 0x500, v216\n"
        "v_lshl_add_u32 v8, v217, 4, v8\n"
        "v_mul_u32_u24 v9, 0x500, v216\n"
        "v_lshl_add_u32 v9, v217, 4, v9\n"
        "v_add_u32 v9, 0x2400, v9\n"
        "v_mul_u32_u24 v10, 0x500, v216\n"
        "v_lshl_add_u32 v10, v217, 4, v10\n"
        "v_add_u32 v10, 0x4800, v10\n"
        "s_lshr_b32 s53, %[b], 6\n"
        "s_mul_i32 s54, s53, 4\n"
        "s_and_b32 s55, %[b], 7\n"
        "s_lshr_b32 s56, s55, 1\n"
        "s_add_u32 s54, s54, s56\n"
        "s_and_b32 s57, s54, 7\n"
        "s_mul_i32 s57, s57, 0x50\n"
        "s_mul_i32 s58, %[w], 20\n"
        "s_add_u32 s58, s57, s58\n"
        "s_mul_i32 s89, s58, 0x500\n"
        "s_mul_i32 s90, %[w], 0xa00\n"
        "s_lshr_b32 s58, %[b], 3\n"
        "s_and_b32 s58, s58, 7\n"
        "s_mul_i32 s57, s58, 2\n"
        "s_and_b32 s59, s55, 1\n"
        "s_add_u32 s59, s57, s59\n"
        "s_mul_i32 s57, s59, 0x80\n"
        "s_mul_i32 s94, %[w], 32\n"
        "s_add_u32 s94, s57, s94\n"
        "s_mul_i32 s94, s94, 0x1000\n"
        "s_and_b32 s95, s56, 1\n"
        "s_mul_i32 s96, s95, 0x800\n"
        "s_add_u32 s64, s94, s96\n"
        "s_lshr_b32 s56, s56, 1\n"
        "s_mul_i32 s94, s56, 8\n"
        "s_mul_i32 s97, s53, 2\n"
        "s_add_u32 s94, s94, s97\n"
        "s_add_u32 s52, s94, s52\n"
        "s_mul_i32 s52, s52, 64\n"
        "s_mul_i32 s94, s95, 32\n"
        "s_add_u32 s94, s52, s94\n"
        "s_mul_i32 s94, s94, 0x1400\n"
        "s_add_u32 s94, s94, 0x79a080\n"
        "s_lshr_b32 s52, %[w], 1\n"
        "s_mul_i32 s52, s52, 0x1400\n"
        "s_add_u32 s65, s94, s52\n"
        "s_mul_i32 s66, %[w], 0x1000\n"
        "s_mul_i32 s52, %[w], 0x1400\n"
        "s_add_u32 s67, s52, 0xc000\n"
        "s_mul_i32 s52, %[w], 0x32000\n"
        "s_add_u32 s95, s97, s95\n"
        "s_mul_i32 s95, s95, 0xa0\n"
        "s_add_u32 s68, s52, s95\n"
        "s_mul_i32 s69, %[w], 0x6400\n"
        "s_mul_i32 s95, %[w], 0x28000\n"
        "s_add_u32 s70, s95, 0x299880\n"
        "s_mul_i32 s95, %[w], 0x1640\n"
        "s_add_u32 s71, s95, 0x1ce00\n"
        "s_mul_i32 s54, s54, 16\n"
        "s_add_u32 s54, s54, s59\n"
        "s_mul_i32 s54, s54, 0x80\n"
        "s_add_u32 s54, s54, 0x8080\n"
        "s_mul_i32 s95, %[w], 0x10000000\n"
        "s_add_u32 s72, s54, s95\n"
        "s_mul_i32 s58, s58, 32\n"
        "s_mul_i32 s55, s55, 4\n"
        "s_add_u32 s55, s55, s53\n"
        "s_add_u32 s55, s58, s55\n"
        "s_mul_i32 s55, s55, 0x80\n"
        "s_add_u32 s55, s55, 0x10080\n"
        "s_add_u32 s73, s55, s95\n"
        "s_mul_i32 s59, s59, 2\n"
        "s_add_u32 s56, s59, s56\n"
        "s_mul_i32 s56, s56, 0x80\n"
        "s_mul_i32 s59, %[w], 8\n"
        "s_add_u32 s56, s56, s59\n"
        "s_mul_i32 s56, s56, 0x280\n"
        "s_add_u32 s74, s56, 0xc9a080\n"
        "s_mul_i32 s56, s53, 32\n"
        "s_add_u32 s56, s57, s56\n"
        "s_add_u32 s59, s56, s59\n"
        "s_mul_i32 s59, s59, 0x1000\n"
        "s_add_u32 s75, s59, s96\n"
        "s_mul_i32 s96, %[w], 0x1a00\n"
        "s_add_u32 s76, s96, 0x1b000\n"
        "s_mov_b32 s77, s53\n"
        "s_mul_i32 s53, %[b], 4\n"
        "s_add_u32 s78, s53, %[w]\n"
        "s_mul_i32 s79, %[w], 0x400\n"
        "s_and_b32 s52, %[b], 7\n"
        "s_mul_i32 s53, s52, 4\n"
        "s_lshr_b32 s54, %[b], 6\n"
        "s_add_u32 s53, s53, s54\n"
        "s_lshr_b32 s55, s53, 1\n"
        "s_mul_i32 s55, s55, 0xa00\n"
        "s_add_u32 s56, s55, 0xa800\n"
        "s_add_u32 s81, s81, 128\n"
        "s_cmp_eq_u32 s81, 1280\n"
        "s_cselect_b32 s81, 0, s81\n"
        "s_add_u32 s62, s81, s84\n"
        "s_add_u32 m0, s86, 16384\n"
        "s_mul_i32 s63, s81, 1920\n"
        "buffer_load_dwordx4 v16, s[40:43], s62 offen lds\n"
        "s_and_b32 s57, s54, 1\n"
        "s_mul_i32 s58, s57, 0x600\n"
        "s_add_u32 s83, s56, s58\n"
        "s_lshr_b32 s58, %[b], 3\n"
        "s_and_b32 s56, s58, 7\n"
        "s_mul_i32 s56, s56, 2\n"
        "buffer_load_dwordx4 v17, s[40:43], s62 offen offset:1024 lds\n"
        "s_and_b32 s59, s52, 1\n"
        "s_add_u32 s59, s56, s59\n"
        "s_mul_i32 s56, s59, 4\n"
        "s_add_u32 s56, s56, s54\n"
        "s_mul_i32 s56, s56, 4\n"
        "s_add_u32 s56, s56, %[w]\n"
        "buffer_load_dwordx4 v18, s[40:43], s62 offen offset:2048 lds\n"
        "s_mul_i32 s56, s56, 4\n"
        "s_lshr_b32 s52, s52, 1\n"
        "s_add_u32 s56, s56, s52\n"
        "s_mul_i32 s56, s56, 0x2800\n"
        "s_add_u32 s56, s56, 0xf1a080\n"
        "s_xor_b32 s94, %[w], s52\n"
        "s_add_u32 s63, s63, s85\n"
        "buffer_load_dwordx4 v19, s[40:43], s62 offen offset:3072 lds\n"
        "s_add_u32 m0, s87, 0\n"
        "s_nop 0\n"
        "buffer_load_dwordx4 v20, s[48:51], s63 offen lds\n"
        "s_add_u32 s94, s94, 0xffffffff\n"
        "s_and_b32 s94, s94, 0x40000000\n"
        "s_add_u32 s92, s56, s94\n"
        "s_mul_i32 s58, s58, 4\n"
        "s_add_u32 s58, s58, %[w]\n"
        "s_mul_i32 s93, s58, 0x1900\n"
        "buffer_load_dwordx4 v21, s[48:51], s63 offen offset:1024 lds\n"
        "s_mul_i32 s58, s54, 4\n"
        "s_add_u32 s58, s58, s52\n"
        "s_mul_i32 s94, s58, 16\n"
        "s_add_u32 s94, s94, s59\n"
        "s_mul_i32 s94, s94, 0x80\n"
        "s_add_u32 s94, s94, 0x80\n"
        "buffer_load_dwordx4 v22, s[48:51], s63 offen offset:2048 lds\n"
        "s_mul_i32 s56, %[w], 0x10000000\n"
        "s_add_u32 s56, s94, s56\n"
        "s_add_u32 s58, s58, 0xfffffffb\n"
        "s_cmp_lt_u32 s58, 10\n"
        "s_cselect_b32 s94, 0, 0x40000000\n"
        "s_cmp_lt_u32 s59, 8\n"
        "s_cselect_b32 s58, 0x40000000, s94\n"
        "s_add_u32 s91, s56, s58\n"
        "buffer_load_dwordx4 v23, s[48:51], s63 offen offset:3072 lds\n"
        "s_mul_i32 s58, %[w], 0xc00\n"
        "s_add_u32 s58, s58, 0x5000\n"
        "v_lshl_add_u32 v156, %[ln], 4, s58\n"
        "s_add_u32 s58, s58, 0x800\n"
        "s_mul_i32 s57, s57, 8\n"
        "s_add_u32 s58, s58, s57\n"
        "s_waitcnt vmcnt(9) lgkmcnt(0)\n"
        "s_barrier\n"
        "s_add_u32 s81, s81, 128\n"
        "s_cmp_eq_u32 s81, 1280\n"
        "s_cselect_b32 s81, 0, s81\n"
        "s_add_u32 s62, s81, s84\n"
        "s_add_u32 m0, s86, 32768\n"
        "s_mul_i32 s63, s81, 1920\n"
        "buffer_load_dwordx4 v16, s[40:43], s62 offen lds\n"
        "v_and_b32 v216, 31, %[ln]\n"
        "v_lshl_add_u32 v157, v216, 4, s58\n"
        "v_lshlrev_b32 v158, 4, %[ln]\n"
        "v_add_u32 v158, 0x1fa80, v158\n"
        "s_add_u32 s57, s57, 0x20380\n"
        "v_lshl_add_u32 v159, v216, 4, s57\n"
        "ds_read_b128 v[24:27], v11\n"
        "ds_read_b128 v[28:31], v11 offset:2048\n"
        "buffer_load_dwordx4 v17, s[40:43], s62 offen offset:1024 lds\n"
        "v_lshrrev_b32 v217, 5, %[ln]\n"
        "v_mul_u32_u24 v218, 0x140, v217\n"
        "v_lshl_add_u32 v218, v216, 4, v218\n"
        "v_add_u32 v218, 0x1fe80, v218\n"
        "v_add_u32 v219, 4, v216\n"
        "v_and_b32 v219, 27, v219\n"
        "ds_read_b128 v[32:35], v12\n"
        "ds_read_b128 v[36:39], v12 offset:2048\n"
        "buffer_load_dwordx4 v18, s[40:43], s62 offen offset:2048 lds\n"
        "v_mov_b32 v221, 0x20580\n"
        "v_mov_b32 v222, 0x20590\n"
        "v_cmp_eq_u32 vcc, 24, v219\n"
        "s_nop 1\n"
        "v_cndmask_b32 v220, v222, v221, vcc\n"
        "v_cmp_gt_u32 vcc, 20, v216\n"
        "s_nop 1\n"
        "v_cndmask_b32 v160, v220, v218, vcc\n"
        "v_mov_b32 v218, 0x280\n"
        "v_cmp_gt_u32 vcc, 20, v216\n"
        "s_nop 1\n"
        "v_cndmask_b32 v220, 0, v218, vcc\n"
        "s_add_u32 s63, s63, s85\n"
        "buffer_load_dwordx4 v19, s[40:43], s62 offen offset:3072 lds\n"
        "s_add_u32 m0, s87, 16384\n"
        "s_nop 0\n"
        "buffer_load_dwordx4 v20, s[48:51], s63 offen lds\n"
        "v_mov_b32 v161, v160\n"
        "v_add_u32 v161, v161, v220\n"
        "v_lshlrev_b32 v162, 4, %[ln]\n"
        "s_mul_i32 s53, s53, 20\n"
        "s_add_u32 s53, s53, 4\n"
        "s_mul_i32 s53, s53, 16\n"
        "buffer_load_dwordx4 v21, s[48:51], s63 offen offset:1024 lds\n"
        "s_add_u32 s53, s53, 0x7800\n"
        "s_add_u32 s55, s55, 0xa000\n"
        "s_add_u32 s55, s55, 0x400\n"
        "s_add_u32 s55, s55, 0xffffff00\n"
        "v_mov_b32 v216, s53\n"
        "v_mov_b32 v218, s55\n"
        "buffer_load_dwordx4 v22, s[48:51], s63 offen offset:2048 lds\n"
        "v_cmp_gt_u32 vcc, 16, %[ln]\n"
        "s_nop 1\n"
        "v_cndmask_b32 v220, v218, v216, vcc\n"
        "v_lshlrev_b32 v163, 4, %[ln]\n"
        "v_add_u32 v163, v163, v220\n"
        "s_lshr_b32 s55, s52, 1\n"
        "s_mul_i32 s55, s55, 8\n"
        "s_mul_i32 s55, s55, 0x800\n"
        "buffer_load_dwordx4 v23, s[48:51], s63 offen offset:3072 lds\n"
        "s_mul_i32 s54, s54, 2\n"
        "s_mul_i32 s54, s54, 0x800\n"
        "s_add_u32 s54, s55, s54\n"
        "s_and_b32 s52, s52, 1\n"
        "s_mul_i32 s52, s52, 8\n"
        "s_mul_i32 s52, s52, 0x80\n"
        "s_waitcnt vmcnt(8) lgkmcnt(0)\n"
        "s_barrier\n"
        "s_add_u32 s81, s81, 128\n"
        "s_cmp_eq_u32 s81, 1280\n"
        "s_cselect_b32 s81, 0, s81\n"
        "s_add_u32 s62, s81, s84\n"
        "s_add_u32 m0, s86, 49152\n"
        "s_mul_i32 s63, s81, 1920\n"
        "buffer_load_dwordx4 v16, s[40:43], s62 offen lds\n"
        "s_add_u32 s52, s54, s52\n"
        "s_add_u32 s52, s52, 0x8080\n"
        "v_lshrrev_b32 v220, 3, %[ln]\n"
        "v_and_b32 v216, 1, v220\n"
        "v_and_b32 v218, 7, %[ln]\n"
        "v_lshl_add_u32 v164, v216, 11, s52\n"
        "buffer_load_dwordx4 v17, s[40:43], s62 offen offset:1024 lds\n"
        "v_lshl_add_u32 v164, v218, 7, v164\n"
        "v_xor_b32 v218, v220, v218\n"
        "v_lshlrev_b32 v165, 12, v220\n"
        "v_lshl_add_u32 v165, v218, 4, v165\n"
        "v_mul_u32_u24 v218, 0x199a, %[ln]\n"
        "v_lshrrev_b32 v218, 16, v218\n"
        "buffer_load_dwordx4 v18, s[40:43], s62 offen offset:2048 lds\n"
        "v_mul_u32_u24 v166, 0x460, v218\n"
        "v_lshl_add_u32 v166, %[ln], 4, v166\n"
        "v_mul_u32_u24 v218, 0x199a, %[ln]\n"
        "v_add_u32 v218, 0x66680, v218\n"
        "v_lshrrev_b32 v218, 16, v218\n"
        "v_mul_u32_u24 v167, 0x460, v218\n"
        "s_add_u32 s63, s63, s85\n"
        "buffer_load_dwordx4 v19, s[40:43], s62 offen offset:3072 lds\n"
        "s_add_u32 m0, s87, 32768\n"
        "s_nop 0\n"
        "buffer_load_dwordx4 v20, s[48:51], s63 offen lds\n"
        "v_lshl_add_u32 v167, %[ln], 4, v167\n"
        "v_add_u32 v167, 0x400, v167\n"
        "v_mul_u32_u24 v218, 0x199a, %[ln]\n"
        "v_add_u32 v218, 0xccd00, v218\n"
        "v_lshrrev_b32 v218, 16, v218\n"
        "v_mul_u32_u24 v168, 0x460, v218\n"
        "s_add_u32 s81, s81, 128\n"
        "s_cmp_eq_u32 s81, 1280\n"
        "s_cselect_b32 s81, 0, s81\n"
        "s_add_u32 s62, s81, s84\n"
        "buffer_load_dwordx4 v21, s[48:51], s63 offen offset:1024 lds\n"
        "v_lshl_add_u32 v168, %[ln], 4, v168\n"
        "v_add_u32 v168, 0x800, v168\n"
        "v_mul_u32_u24 v218, 0x199a, %[ln]\n"
        "v_add_u32 v218, 0x133380, v218\n"
        "v_lshrrev_b32 v218, 16, v218\n"
        "v_mul_u32_u24 v169, 0x460, v218\n"
        "buffer_load_dwordx4 v22, s[48:51], s63 offen offset:2048 lds\n"
        "v_lshl_add_u32 v169, %[ln], 4, v169\n"
        "v_add_u32 v169, 0xc00, v169\n"
        "v_mul_u32_u24 v218, 0x199a, %[ln]\n"
        "v_add_u32 v218, 0x199a00, v218\n"
        "v_lshrrev_b32 v218, 16, v218\n"
        "v_mul_u32_u24 v170, 0x460, v218\n"
        "buffer_load_dwordx4 v23, s[48:51], s63 offen offset:3072 lds\n"
        "v_lshl_add_u32 v170, %[ln], 4, v170\n"
        "v_add_u32 v170, 0x1000, v170\n"
        "s_mul_i32 s52, %[w], 0x6400\n"
        "v_and_b32 v218, 15, %[ln]\n"
        "v_lshrrev_b32 v218, 2, v218\n"
        "v_lshrrev_b32 v220, 4, %[ln]\n"
        "s_add_u32 m0, s86, 0\n"
        "s_mul_i32 s63, s81, 1920\n"
        "buffer_load_dwordx4 v16, s[40:43], s62 offen lds\n"
        "v_and_b32 v220, 1, v220\n"
        "v_and_b32 v216, 3, %[ln]\n"
        "v_mul_u32_u24 v171, 0xa0, v218\n"
        "v_mul_u32_u24 v219, 0xc80, v217\n"
        "v_add_u32 v171, v171, v219\n"
        "v_mul_u32_u24 v219, 0x280, v220\n"
        "ds_read_b128 v[40:43], v11 offset:16384\n"
        "ds_read_b128 v[44:47], v11 offset:18432\n"
        "buffer_load_dwordx4 v17, s[40:43], s62 offen offset:1024 lds\n"
        "v_add_u32 v171, v171, v219\n"
        "v_lshl_add_u32 v171, v216, 3, v171\n"
        "v_add_u32 v171, s52, v171\n"
        "ds_read_b128 v[172:175], v12 offset:16384\n"
        "ds_read_b128 v[152:155], v12 offset:18432\n"
        "buffer_load_dwordx4 v18, s[40:43], s62 offen offset:2048 lds\n"
        "s_add_u32 s63, s63, s85\n"
        "buffer_load_dwordx4 v19, s[40:43], s62 offen offset:3072 lds\n"
        "s_add_u32 m0, s87, 0\n"
        "s_waitcnt vmcnt(12) lgkmcnt(0)\n"
        "s_barrier\n"
        "buffer_load_dwordx4 v20, s[48:51], s63 offen lds\n"
        "s_add_u32 s81, s81, 128\n"
        "s_cmp_eq_u32 s81, 1280\n"
        "s_cselect_b32 s81, 0, s81\n"
        "s_add_u32 s62, s81, s84\n"
        "buffer_load_dwordx4 v21, s[48:51], s63 offen offset:1024 lds\n"
        "buffer_load_dwordx4 v22, s[48:51], s63 offen offset:2048 lds\n"
        "buffer_load_dwordx4 v23, s[48:51], s63 offen offset:3072 lds\n"
        "s_add_u32 m0, s86, 16384\n"
        "s_mul_i32 s63, s81, 1920\n"
        "buffer_load_dwordx4 v16, s[40:43], s62 offen lds\n"
        "ds_read_b128 v[48:51], v11 offset:32768\n"
        "ds_read_b128 v[52:55], v11 offset:34816\n"
        "buffer_load_dwordx4 v17, s[40:43], s62 offen offset:1024 lds\n"
        "ds_read_b128 v[56:59], v12 offset:32768\n"
        "ds_read_b128 v[60:63], v12 offset:34816\n"
        "buffer_load_dwordx4 v18, s[40:43], s62 offen offset:2048 lds\n"
        "s_add_u32 s63, s63, s85\n"
        "buffer_load_dwordx4 v19, s[40:43], s62 offen offset:3072 lds\n"
        "s_add_u32 m0, s87, 16384\n"
        "s_waitcnt vmcnt(12) lgkmcnt(0)\n"
        "s_barrier\n"
        "buffer_load_dwordx4 v20, s[48:51], s63 offen lds\n"
        "s_add_u32 s81, s81, 128\n"
        "s_cmp_eq_u32 s81, 1280\n"
        "s_cselect_b32 s81, 0, s81\n"
        "s_add_u32 s62, s81, s84\n"
        "buffer_load_dwordx4 v21, s[48:51], s63 offen offset:1024 lds\n"
        "buffer_load_dwordx4 v22, s[48:51], s63 offen offset:2048 lds\n"
        "buffer_load_dwordx4 v23, s[48:51], s63 offen offset:3072 lds\n"
        "s_add_u32 m0, s86, 32768\n"
        "s_mul_i32 s63, s81, 1920\n"
        "buffer_load_dwordx4 v16, s[40:43], s62 offen lds\n"
        "ds_read_b128 v[64:67], v11 offset:49152\n"
        "ds_read_b128 v[68:71], v11 offset:51200\n"
        "buffer_load_dwordx4 v17, s[40:43], s62 offen offset:1024 lds\n"
        "ds_read_b128 v[72:75], v12 offset:49152\n"
        "ds_read_b128 v[76:79], v12 offset:51200\n"
        "buffer_load_dwordx4 v18, s[40:43], s62 offen offset:2048 lds\n"
        "s_add_u32 s63, s63, s85\n"
        "buffer_load_dwordx4 v19, s[40:43], s62 offen offset:3072 lds\n"
        "s_add_u32 m0, s87, 32768\n"
        "s_waitcnt vmcnt(12) lgkmcnt(0)\n"
        "s_barrier\n"
        "buffer_load_dwordx4 v20, s[48:51], s63 offen lds\n"
        "s_add_u32 s81, s81, 128\n"
        "s_cmp_eq_u32 s81, 1280\n"
        "s_cselect_b32 s81, 0, s81\n"
        "s_add_u32 s62, s81, s84\n"
        "buffer_load_dwordx4 v21, s[48:51], s63 offen offset:1024 lds\n"
        "buffer_load_dwordx4 v22, s[48:51], s63 offen offset:2048 lds\n"
        "buffer_load_dwordx4 v23, s[48:51], s63 offen offset:3072 lds\n"
        "s_add_u32 m0, s86, 49152\n"
        "s_mul_i32 s63, s81, 1920\n"
        "buffer_load_dwordx4 v16, s[40:43], s62 offen lds\n"
        "ds_read_b128 v[80:83], v11\n"
        "ds_read_b128 v[84:87], v11 offset:2048\n"
        "buffer_load_dwordx4 v17, s[40:43], s62 offen offset:1024 lds\n"
        "ds_read_b128 v[88:91], v12\n"
        "ds_read_b128 v[92:95], v12 offset:2048\n"
        "buffer_load_dwordx4 v18, s[40:43], s62 offen offset:2048 lds\n"
        "s_add_u32 s63, s63, s85\n"
        "buffer_load_dwordx4 v19, s[40:43], s62 offen offset:3072 lds\n"
        "s_add_u32 m0, s87, 0\n"
        "s_waitcnt vmcnt(12) lgkmcnt(0)\n"
        "s_barrier\n"
        "buffer_load_dwordx4 v20, s[48:51], s63 offen lds\n"
        "s_add_u32 s81, s81, 128\n"
        "s_cmp_eq_u32 s81, 1280\n"
        "s_cselect_b32 s81, 0, s81\n"
        "s_add_u32 s62, s81, s84\n"
        "buffer_load_dwordx4 v21, s[48:51], s63 offen offset:1024 lds\n"
        "buffer_load_dwordx4 v22, s[48:51], s63 offen offset:2048 lds\n"
        "buffer_load_dwordx4 v23, s[48:51], s63 offen offset:3072 lds\n"
        "s_add_u32 m0, s86, 0\n"
        "s_mul_i32 s63, s81, 1920\n"
        "buffer_load_dwordx4 v16, s[40:43], s62 offen lds\n"
        "ds_read_b128 v[96:99], v11 offset:16384\n"
        "ds_read_b128 v[100:103], v11 offset:18432\n"
        "buffer_load_dwordx4 v17, s[40:43], s62 offen offset:1024 lds\n"
        "ds_read_b128 v[104:107], v12 offset:16384\n"
        "ds_read_b128 v[108:111], v12 offset:18432\n"
        "buffer_load_dwordx4 v18, s[40:43], s62 offen offset:2048 lds\n"
        "s_add_u32 s63, s63, s85\n"
        "buffer_load_dwordx4 v19, s[40:43], s62 offen offset:3072 lds\n"
        "s_add_u32 m0, s87, 16384\n"
        "s_waitcnt vmcnt(12) lgkmcnt(0)\n"
        "s_barrier\n"
        "buffer_load_dwordx4 v20, s[48:51], s63 offen lds\n"
        "s_add_u32 s81, s81, 128\n"
        "s_cmp_eq_u32 s81, 1280\n"
        "s_cselect_b32 s81, 0, s81\n"
        "s_add_u32 s62, s81, s84\n"
        "buffer_load_dwordx4 v21, s[48:51], s63 offen offset:1024 lds\n"
        "buffer_load_dwordx4 v22, s[48:51], s63 offen offset:2048 lds\n"
        "buffer_load_dwordx4 v23, s[48:51], s63 offen offset:3072 lds\n"
        "s_add_u32 m0, s86, 16384\n"
        "s_mul_i32 s63, s81, 1920\n"
        "buffer_load_dwordx4 v16, s[40:43], s62 offen lds\n"
        "ds_read_b128 v[112:115], v11 offset:32768\n"
        "ds_read_b128 v[116:119], v11 offset:34816\n"
        "buffer_load_dwordx4 v17, s[40:43], s62 offen offset:1024 lds\n"
        "ds_read_b128 v[120:123], v12 offset:32768\n"
        "ds_read_b128 v[124:127], v12 offset:34816\n"
        "buffer_load_dwordx4 v18, s[40:43], s62 offen offset:2048 lds\n"
        "s_add_u32 s63, s63, s85\n"
        "buffer_load_dwordx4 v19, s[40:43], s62 offen offset:3072 lds\n"
        "s_add_u32 m0, s87, 32768\n"
        "s_waitcnt vmcnt(12) lgkmcnt(0)\n"
        "s_barrier\n"
        "buffer_load_dwordx4 v20, s[48:51], s63 offen lds\n"
        "buffer_load_dwordx4 v21, s[48:51], s63 offen offset:1024 lds\n"
        "buffer_load_dwordx4 v22, s[48:51], s63 offen offset:2048 lds\n"
        "buffer_load_dwordx4 v23, s[48:51], s63 offen offset:3072 lds\n"
        "ds_read_b128 v[128:131], v11 offset:49152\n"
        "ds_read_b128 v[132:135], v11 offset:51200\n"
        "ds_read_b128 v[136:139], v12 offset:49152\n"
        "ds_read_b128 v[140:143], v12 offset:51200\n"
        "s_add_u32 m0, s90, 32768\n"
        "s_add_u32 s63, s82, s89\n"
        "buffer_load_dwordx4 v8, s[44:47], s63 offen lds\n"
        "buffer_load_dwordx4 v9, s[44:47], s63 offen offset:1024 lds\n"
        "s_mov_b32 exec_hi, 0\n"
        "buffer_load_dwordx4 v10, s[44:47], s63 offen offset:2048 lds\n"
        "s_mov_b32 exec_hi, -1\n"
        "s_add_u32 s82, s82, 128\n"
        "s_cmp_eq_u32 s82, 1280\n"
        "s_cselect_b32 s82, 0, s82\n"
        "s_waitcnt vmcnt(11) lgkmcnt(0)\n"
        "s_barrier\n"
        "s_add_u32 m0, s90, 43008\n"
        "s_add_u32 s63, s82, s89\n"
        "buffer_load_dwordx4 v8, s[44:47], s63 offen lds\n"
        "buffer_load_dwordx4 v9, s[44:47], s63 offen offset:1024 lds\n"
        "s_mov_b32 exec_hi, 0\n"
        "buffer_load_dwordx4 v10, s[44:47], s63 offen offset:2048 lds\n"
        "s_mov_b32 exec_hi, -1\n"
        "s_add_u32 s82, s82, 128\n"
        "s_cmp_eq_u32 s82, 1280\n"
        "s_cselect_b32 s82, 0, s82\n"
        "s_add_u32 m0, s90, 53248\n"
        "s_add_u32 s63, s82, s89\n"
        "buffer_load_dwordx4 v8, s[44:47], s63 offen lds\n"
        "buffer_load_dwordx4 v9, s[44:47], s63 offen offset:1024 lds\n"
        "s_mov_b32 exec_hi, 0\n"
        "buffer_load_dwordx4 v10, s[44:47], s63 offen offset:2048 lds\n"
        "s_mov_b32 exec_hi, -1\n"
        "s_add_u32 s82, s82, 128\n"
        "s_cmp_eq_u32 s82, 1280\n"
        "s_cselect_b32 s82, 0, s82\n"
        "ds_read_b128 v[144:147], v11\n"
        "ds_read_b128 v[148:151], v11 offset:2048\n"
        "ds_read_b128 v[176:179], v12\n"
        "ds_read_b128 v[180:183], v12 offset:2048\n"
        "s_add_u32 m0, s90, 63488\n"
        "s_add_u32 s63, s82, s89\n"
        "buffer_load_dwordx4 v8, s[44:47], s63 offen lds\n"
        "buffer_load_dwordx4 v9, s[44:47], s63 offen offset:1024 lds\n"
        "s_mov_b32 exec_hi, 0\n"
        "buffer_load_dwordx4 v10, s[44:47], s63 offen offset:2048 lds\n"
        "s_mov_b32 exec_hi, -1\n"
        "s_add_u32 s82, s82, 128\n"
        "s_cmp_eq_u32 s82, 1280\n"
        "s_cselect_b32 s82, 0, s82\n"
        "s_waitcnt vmcnt(12) lgkmcnt(0)\n"
        "s_barrier\n"
        "s_add_u32 m0, s90, 73728\n"
        "s_add_u32 s63, s82, s89\n"
        "buffer_load_dwordx4 v8, s[44:47], s63 offen lds\n"
        "buffer_load_dwordx4 v9, s[44:47], s63 offen offset:1024 lds\n"
        "s_mov_b32 exec_hi, 0\n"
        "buffer_load_dwordx4 v10, s[44:47], s63 offen offset:2048 lds\n"
        "s_mov_b32 exec_hi, -1\n"
        "s_add_u32 s82, s82, 128\n"
        "s_cmp_eq_u32 s82, 1280\n"
        "s_cselect_b32 s82, 0, s82\n"
        "s_add_u32 m0, s90, 83968\n"
        "s_add_u32 s63, s82, s89\n"
        "buffer_load_dwordx4 v8, s[44:47], s63 offen lds\n"
        "buffer_load_dwordx4 v9, s[44:47], s63 offen offset:1024 lds\n"
        "s_mov_b32 exec_hi, 0\n"
        "buffer_load_dwordx4 v10, s[44:47], s63 offen offset:2048 lds\n"
        "s_mov_b32 exec_hi, -1\n"
        "s_add_u32 s82, s82, 128\n"
        "s_cmp_eq_u32 s82, 1280\n"
        "s_cselect_b32 s82, 0, s82\n"
        "ds_read_b128 v[184:187], v11 offset:16384\n"
        "ds_read_b128 v[188:191], v11 offset:18432\n"
        "ds_read_b128 v[16:19], v12 offset:16384\n"
        "ds_read_b128 v[20:23], v12 offset:18432\n"
        "s_add_u32 m0, s90, 0\n"
        "s_add_u32 s63, s82, s89\n"
        "buffer_load_dwordx4 v8, s[44:47], s63 offen lds\n"
        "buffer_load_dwordx4 v9, s[44:47], s63 offen offset:1024 lds\n"
        "s_mov_b32 exec_hi, 0\n"
        "buffer_load_dwordx4 v10, s[44:47], s63 offen offset:2048 lds\n"
        "s_mov_b32 exec_hi, -1\n"
        "s_add_u32 s82, s82, 128\n"
        "s_cmp_eq_u32 s82, 1280\n"
        "s_cselect_b32 s82, 0, s82\n"
        "s_waitcnt vmcnt(63) lgkmcnt(0)\n"
        "s_barrier\n"
        "s_add_u32 m0, s90, 94208\n"
        "s_add_u32 s63, s82, s89\n"
        "buffer_load_dwordx4 v8, s[44:47], s63 offen lds\n"
        "buffer_load_dwordx4 v9, s[44:47], s63 offen offset:1024 lds\n"
        "s_mov_b32 exec_hi, 0\n"
        "buffer_load_dwordx4 v10, s[44:47], s63 offen offset:2048 lds\n"
        "s_mov_b32 exec_hi, -1\n"
        "s_add_u32 s82, s82, 128\n"
        "s_cmp_eq_u32 s82, 1280\n"
        "s_cselect_b32 s82, 0, s82\n"
        "s_add_u32 m0, s90, 104448\n"
        "s_add_u32 s63, s82, s89\n"
        "buffer_load_dwordx4 v8, s[44:47], s63 offen lds\n"
        "buffer_load_dwordx4 v9, s[44:47], s63 offen offset:1024 lds\n"
        "s_mov_b32 exec_hi, 0\n"
        "buffer_load_dwordx4 v10, s[44:47], s63 offen offset:2048 lds\n"
        "s_mov_b32 exec_hi, -1\n"
        "s_add_u32 s82, s82, 128\n"
        "s_nop 0\n"
        ".Lk_hop4:\n"
        "s_cbranch_execz .Lk_ret0\n"
        "s_cmp_eq_u32 s82, 1280\n"
        "s_cselect_b32 s82, 0, s82\n"
        "s_add_u32 m0, s90, 10240\n"
        "s_add_u32 s63, s82, s89\n"
        "buffer_load_dwordx4 v8, s[44:47], s63 offen lds\n"
        "buffer_load_dwordx4 v9, s[44:47], s63 offen offset:1024 lds\n"
        "s_mov_b32 exec_hi, 0\n"
        "buffer_load_dwordx4 v10, s[44:47], s63 offen offset:2048 lds\n"
        "s_mov_b32 exec_hi, -1\n"
        "s_add_u32 s82, s82, 128\n"
        "s_cmp_eq_u32 s82, 1280\n"
        "s_cselect_b32 s82, 0, s82\n"
        "v_readfirstlane_b32 s60, v192\n"
        "v_readfirstlane_b32 s61, v193\n"
        "s_nop 0\n"
        "s_add_u32 s60, s60, 0x7f4a7c15\n"
        "s_addc_u32 s61, s61, 0x9e3779b9\n"
        "s_waitcnt vmcnt(12) lgkmcnt(0)\n"
        "s_barrier\n"
        "s_setprio 0\n"
        "ds_read_b128 v[192:195], v13 offset:32768\n"
        "ds_read_b128 v[196:199], v13 offset:34816\n"
        "ds_read_b128 v[200:203], v13 offset:36864\n"
        "ds_read_b128 v[204:207], v13 offset:38912\n"
        "ds_read_b128 v[208:211], v13 offset:40960\n"
        "ds_read_b128 v[212:215], v14 offset:32768\n"
        "s_waitcnt lgkmcnt(5)\n"
        "v_mfma_f32_16x16x32_bf16 v[216:219], v[24:27], v[192:195], 0\n"
        "v_mfma_f32_16x16x32_bf16 v[220:223], v[28:31], v[192:195], 0\n"
        "ds_read_b128 v[192:195], v14 offset:34816\n"
        "s_waitcnt lgkmcnt(5)\n"
        "v_mfma_f32_16x16x32_bf16 v[224:227], v[24:27], v[196:199], 0\n"
        "v_mfma_f32_16x16x32_bf16 v[228:231], v[28:31], v[196:199], 0\n"
        "ds_read_b128 v[196:199], v14 offset:36864\n"
        "s_waitcnt lgkmcnt(5)\n"
        "v_mfma_f32_16x16x32_bf16 v[232:235], v[24:27], v[200:203], 0\n"
        "v_mfma_f32_16x16x32_bf16 v[236:239], v[28:31], v[200:203], 0\n"
        "ds_read_b128 v[200:203], v14 offset:38912\n"
        "s_waitcnt lgkmcnt(5)\n"
        "v_mfma_f32_16x16x32_bf16 v[240:243], v[24:27], v[204:207], 0\n"
        "v_mfma_f32_16x16x32_bf16 v[244:247], v[28:31], v[204:207], 0\n"
        "ds_read_b128 v[204:207], v14 offset:40960\n"
        "s_waitcnt lgkmcnt(5)\n"
        "v_mfma_f32_16x16x32_bf16 v[248:251], v[24:27], v[208:211], 0\n"
        "v_mfma_f32_16x16x32_bf16 v[252:255], v[28:31], v[208:211], 0\n"
        "ds_read_b128 v[208:211], v13 offset:43008\n"
        "s_waitcnt lgkmcnt(5)\n"
        "v_mfma_f32_16x16x32_bf16 v[216:219], v[32:35], v[212:215], v[216:219]\n"
        "v_mfma_f32_16x16x32_bf16 v[220:223], v[36:39], v[212:215], v[220:223]\n"
        "ds_read_b128 v[212:215], v13 offset:45056\n"
        "s_waitcnt lgkmcnt(5)\n"
        "v_mfma_f32_16x16x32_bf16 v[224:227], v[32:35], v[192:195], v[224:227]\n"
        "v_mfma_f32_16x16x32_bf16 v[228:231], v[36:39], v[192:195], v[228:231]\n"
        "ds_read_b128 v[192:195], v13 offset:47104\n"
        "s_waitcnt lgkmcnt(5)\n"
        "v_mfma_f32_16x16x32_bf16 v[232:235], v[32:35], v[196:199], v[232:235]\n"
        "v_mfma_f32_16x16x32_bf16 v[236:239], v[36:39], v[196:199], v[236:239]\n"
        "ds_read_b128 v[196:199], v13 offset:49152\n"
        "s_waitcnt lgkmcnt(5)\n"
        "v_mfma_f32_16x16x32_bf16 v[240:243], v[32:35], v[200:203], v[240:243]\n"
        "v_mfma_f32_16x16x32_bf16 v[244:247], v[36:39], v[200:203], v[244:247]\n"
        "ds_read_b128 v[200:203], v13 offset:51200\n"
        "s_waitcnt lgkmcnt(5)\n"
        "v_mfma_f32_16x16x32_bf16 v[248:251], v[32:35], v[204:207], v[248:251]\n"
        "v_mfma_f32_16x16x32_bf16 v[252:255], v[36:39], v[204:207], v[252:255]\n"
        "ds_read_b128 v[204:207], v14 offset:43008\n"
        "s_waitcnt lgkmcnt(5)\n"
        "v_mfma_f32_16x16x32_bf16 v[216:219], v[40:43], v[208:211], v[216:219]\n"
        "v_mfma_f32_16x16x32_bf16 v[220:223], v[44:47], v[208:211], v[220:223]\n"
        "ds_read_b128 v[208:211], v14 offset:45056\n"
        "s_waitcnt lgkmcnt(5)\n"
        "v_mfma_f32_16x16x32_bf16 v[224:227], v[40:43], v[212:215], v[224:227]\n"
        "v_mfma_f32_16x16x32_bf16 v[228:231], v[44:47], v[212:215], v[228:231]\n"
        "ds_read_b128 v[212:215], v14 offset:47104\n"
        "s_waitcnt lgkmcnt(5)\n"
        "v_mfma_f32_16x16x32_bf16 v[232:235], v[40:43], v[192:195], v[232:235]\n"
        "v_mfma_f32_16x16x32_bf16 v[236:239], v[44:47], v[192:195], v[236:239]\n"
        "ds_read_b128 v[192:195], v14 offset:49152\n"
        "s_waitcnt lgkmcnt(5)\n"
        "v_mfma_f32_16x16x32_bf16 v[240:243], v[40:43], v[196:199], v[240:243]\n"
        "v_mfma_f32_16x16x32_bf16 v[244:247], v[44:47], v[196:199], v[244:247]\n"
        "ds_read_b128 v[196:199], v14 offset:51200\n"
        "s_waitcnt lgkmcnt(5)\n"
        "v_mfma_f32_16x16x32_bf16 v[248:251], v[40:43], v[200:203], v[248:251]\n"
        "s_mov_b64 s[52:53], s[20:21]\n"
        "s_and_b32 s53, s53, 0xffff\n"
        "v_mfma_f32_16x16x32_bf16 v[252:255], v[44:47], v[200:203], v[252:255]\n"
        "ds_read_b128 v[200:203], v13 offset:53248\n"
        "s_waitcnt lgkmcnt(5)\n"
        "v_mfma_f32_16x16x32_bf16 v[216:219], v[172:175], v[204:207], v[216:219]\n"
        "s_mov_b32 s54, %[wsn]\n"
        "s_mov_b32 s55, 0x20000\n"
        "v_mfma_f32_16x16x32_bf16 v[220:223], v[152:155], v[204:207], v[220:223]\n"
        "ds_read_b128 v[204:207], v13 offset:55296\n"
        "s_waitcnt lgkmcnt(5)\n"
        "v_mfma_f32_16x16x32_bf16 v[224:227], v[172:175], v[208:211], v[224:227]\n"
        "s_mov_b64 s[48:49], s[30:31]\n"
        "s_and_b32 s49, s49, 0xffff\n"
        "v_mfma_f32_16x16x32_bf16 v[228:231], v[152:155], v[208:211], v[228:231]\n"
        "ds_read_b128 v[208:211], v13 offset:57344\n"
        "s_waitcnt lgkmcnt(5)\n"
        "v_mfma_f32_16x16x32_bf16 v[232:235], v[172:175], v[212:215], v[232:235]\n"
        "s_mov_b32 s50, 819200\n"
        "s_mov_b32 s51, 0x20000\n"
        "v_mfma_f32_16x16x32_bf16 v[236:239], v[152:155], v[212:215], v[236:239]\n"
        "ds_read_b128 v[212:215], v13 offset:59392\n"
        "s_waitcnt lgkmcnt(5)\n"
        "v_mfma_f32_16x16x32_bf16 v[240:243], v[172:175], v[192:195], v[240:243]\n"
        "s_mov_b64 s[44:45], s[26:27]\n"
        "s_and_b32 s45, s45, 0xffff\n"
        "v_mfma_f32_16x16x32_bf16 v[244:247], v[152:155], v[192:195], v[244:247]\n"
        "ds_read_b128 v[192:195], v13 offset:61440\n"
        "s_waitcnt lgkmcnt(5)\n"
        "v_mfma_f32_16x16x32_bf16 v[248:251], v[172:175], v[196:199], v[248:251]\n"
        "s_mov_b32 s46, 8388608\n"
        "s_mov_b32 s47, 0x20000\n"
        "v_mfma_f32_16x16x32_bf16 v[252:255], v[152:155], v[196:199], v[252:255]\n"
        "ds_read_b128 v[196:199], v14 offset:53248\n"
        "s_waitcnt lgkmcnt(5)\n"
        "v_mfma_f32_16x16x32_bf16 v[216:219], v[48:51], v[200:203], v[216:219]\n"
        "s_mul_i32 s40, %[w], 0x6400\n"
        "s_add_u32 s40, s40, 0xa00\n"
        "v_mfma_f32_16x16x32_bf16 v[220:223], v[52:55], v[200:203], v[220:223]\n"
        "ds_read_b128 v[200:203], v14 offset:55296\n"
        "s_waitcnt lgkmcnt(5)\n"
        "v_mfma_f32_16x16x32_bf16 v[224:227], v[48:51], v[204:207], v[224:227]\n"
        "v_and_b32 v24, 15, %[ln]\n"
        "v_lshrrev_b32 v25, 2, v24\n"
        "v_mfma_f32_16x16x32_bf16 v[228:231], v[52:55], v[204:207], v[228:231]\n"
        "ds_read_b128 v[204:207], v14 offset:57344\n"
        "s_waitcnt lgkmcnt(5)\n"
        "v_mfma_f32_16x16x32_bf16 v[232:235], v[48:51], v[208:211], v[232:235]\n"
        "v_lshrrev_b32 v26, 4, %[ln]\n"
        "v_and_b32 v27, 3, %[ln]\n"
        "v_mfma_f32_16x16x32_bf16 v[236:239], v[52:55], v[208:211], v[236:239]\n"
        "ds_read_b128 v[208:211], v14 offset:59392\n"
        "s_waitcnt lgkmcnt(5)\n"
        "v_mfma_f32_16x16x32_bf16 v[240:243], v[48:51], v[212:215], v[240:243]\n"
        "v_mul_u32_u24 v172, 0xa0, v25\n"
        "v_mul_u32_u24 v28, 0x1900, v26\n"
        "v_mfma_f32_16x16x32_bf16 v[244:247], v[52:55], v[212:215], v[244:247]\n"
        "ds_read_b128 v[212:215], v14 offset:61440\n"
        "s_waitcnt lgkmcnt(5)\n"
        "v_mfma_f32_16x16x32_bf16 v[248:251], v[48:51], v[192:195], v[248:251]\n"
        "v_add_u32 v172, v172, v28\n"
        "v_lshl_add_u32 v172, v27, 3, v172\n"
        "v_mfma_f32_16x16x32_bf16 v[252:255], v[52:55], v[192:195], v[252:255]\n"
        "ds_read_b128 v[192:195], v13 offset:63488\n"
        "s_waitcnt lgkmcnt(5)\n"
        "v_mfma_f32_16x16x32_bf16 v[216:219], v[56:59], v[196:199], v[216:219]\n"
        "v_add_u32 v172, s40, v172\n"
        "v_lshrrev_b32 v27, 1, %[ln]\n"
        "v_mfma_f32_16x16x32_bf16 v[220:223], v[60:63], v[196:199], v[220:223]\n"
        "v_add_u32 v13, 63488, v13\n"
        "ds_read_b128 v[196:199], v13 offset:2048\n"
        "s_waitcnt lgkmcnt(5)\n"
        "v_mfma_f32_16x16x32_bf16 v[224:227], v[56:59], v[200:203], v[224:227]\n"
        "v_and_b32 v25, 1, %[ln]\n"
        "v_lshlrev_b32 v28, 12, v27\n"
        "v_mfma_f32_16x16x32_bf16 v[228:231], v[60:63], v[200:203], v[228:231]\n"
        "ds_read_b128 v[200:203], v13 offset:4096\n"
        "s_waitcnt lgkmcnt(5)\n"
        "v_mfma_f32_16x16x32_bf16 v[232:235], v[56:59], v[204:207], v[232:235]\n"
        "v_lshl_add_u32 v28, v25, 7, v28\n"
        "v_mov_b32 v25, 0x7f000000\n"
        "v_mfma_f32_16x16x32_bf16 v[236:239], v[60:63], v[204:207], v[236:239]\n"
        "ds_read_b128 v[204:207], v13 offset:6144\n"
        "s_waitcnt lgkmcnt(5)\n"
        "v_mfma_f32_16x16x32_bf16 v[240:243], v[56:59], v[208:211], v[240:243]\n"
        "v_cmp_gt_u32 vcc, 16, %[ln]\n"
        "s_nop 1\n"
        "v_cndmask_b32 v173, v25, v28, vcc\n"
        "v_mfma_f32_16x16x32_bf16 v[244:247], v[60:63], v[208:211], v[244:247]\n"
        "ds_read_b128 v[208:211], v13 offset:8192\n"
        "s_waitcnt lgkmcnt(5)\n"
        "v_mfma_f32_16x16x32_bf16 v[248:251], v[56:59], v[212:215], v[248:251]\n"
        "s_lshr_b32 s40, %[b], 3\n"
        "s_and_b32 s40, s40, 7\n"
        "v_mfma_f32_16x16x32_bf16 v[252:255], v[60:63], v[212:215], v[252:255]\n"
        "ds_read_b128 v[212:215], v14 offset:63488\n"
        "s_waitcnt lgkmcnt(5)\n"
        "v_mfma_f32_16x16x32_bf16 v[216:219], v[64:67], v[192:195], v[216:219]\n"
        "s_mul_i32 s41, s40, 2\n"
        "s_lshr_b32 s42, %[w], 1\n"
        "v_mfma_f32_16x16x32_bf16 v[220:223], v[68:71], v[192:195], v[220:223]\n"
        "v_add_u32 v14, 63488, v14\n"
        "ds_read_b128 v[192:195], v14 offset:2048\n"
        "s_waitcnt lgkmcnt(5)\n"
        "v_mfma_f32_16x16x32_bf16 v[224:227], v[64:67], v[196:199], v[224:227]\n"
        "s_add_u32 s43, s41, s42\n"
        "s_mul_i32 s43, s43, 17\n"
        "v_mfma_f32_16x16x32_bf16 v[228:231], v[68:71], v[196:199], v[228:231]\n"
        "ds_read_b128 v[196:199], v14 offset:4096\n"
        "s_waitcnt lgkmcnt(5)\n"
        "v_mfma_f32_16x16x32_bf16 v[232:235], v[64:67], v[200:203], v[232:235]\n"
        "s_mul_i32 s43, s43, 0x80\n"
        "s_and_b32 s56, %[b], 7\n"
        "v_mfma_f32_16x16x32_bf16 v[236:239], v[68:71], v[200:203], v[236:239]\n"
        "ds_read_b128 v[200:203], v14 offset:6144\n"
        "s_waitcnt lgkmcnt(5)\n"
        "v_mfma_f32_16x16x32_bf16 v[240:243], v[64:67], v[204:207], v[240:243]\n"
        "s_lshr_b32 s57, s56, 1\n"
        "s_lshr_b32 s58, s57, 1\n"
        "v_mfma_f32_16x16x32_bf16 v[244:247], v[68:71], v[204:207], v[244:247]\n"
        "ds_read_b128 v[204:207], v14 offset:8192\n"
        "s_waitcnt lgkmcnt(5)\n"
        "v_mfma_f32_16x16x32_bf16 v[248:251], v[64:67], v[208:211], v[248:251]\n"
        "s_mul_i32 s59, s58, 8\n"
        "s_lshr_b32 s84, %[b], 6\n"
        "v_mfma_f32_16x16x32_bf16 v[252:255], v[68:71], v[208:211], v[252:255]\n"
        "ds_read_b128 v[208:211], v13 offset:10240\n"
        "s_waitcnt lgkmcnt(5)\n"
        "v_mfma_f32_16x16x32_bf16 v[216:219], v[72:75], v[212:215], v[216:219]\n"
        "s_mul_i32 s85, s84, 2\n"
        "s_add_u32 s85, s59, s85\n"
        "v_mfma_f32_16x16x32_bf16 v[220:223], v[76:79], v[212:215], v[220:223]\n"
        "ds_read_b128 v[212:215], v13 offset:12288\n"
        "s_waitcnt lgkmcnt(5)\n"
        "v_mfma_f32_16x16x32_bf16 v[224:227], v[72:75], v[192:195], v[224:227]\n"
        "s_and_b32 s59, %[w], 1\n"
        "s_add_u32 s85, s85, s59\n"
        "v_mfma_f32_16x16x32_bf16 v[228:231], v[76:79], v[192:195], v[228:231]\n"
        "ds_read_b128 v[192:195], v13 offset:14336\n"
        "s_waitcnt lgkmcnt(5)\n"
        "v_mfma_f32_16x16x32_bf16 v[232:235], v[72:75], v[196:199], v[232:235]\n"
        "s_mul_i32 s85, s85, 64\n"
        "s_and_b32 s86, s57, 1\n"
        "v_mfma_f32_16x16x32_bf16 v[236:239], v[76:79], v[196:199], v[236:239]\n"
        "ds_read_b128 v[196:199], v13 offset:16384\n"
        "s_waitcnt lgkmcnt(5)\n"
        "v_mfma_f32_16x16x32_bf16 v[240:243], v[72:75], v[200:203], v[240:243]\n"
        "s_mul_i32 s86, s86, 32\n"
        "s_add_u32 s86, s85, s86\n"
        "v_mfma_f32_16x16x32_bf16 v[244:247], v[76:79], v[200:203], v[244:247]\n"
        "ds_read_b128 v[200:203], v13 offset:18432\n"
        "s_waitcnt lgkmcnt(5)\n"
        "v_mfma_f32_16x16x32_bf16 v[248:251], v[72:75], v[204:207], v[248:251]\n"
        "s_mul_i32 s86, s86, 0x1400\n"
        "s_add_u32 s86, s43, s86\n"
        "v_mfma_f32_16x16x32_bf16 v[252:255], v[76:79], v[204:207], v[252:255]\n"
        "ds_read_b128 v[204:207], v14 offset:10240\n"
        "s_waitcnt lgkmcnt(5)\n"
        "v_mfma_f32_16x16x32_bf16 v[216:219], v[80:83], v[208:211], v[216:219]\n"
        "s_add_u32 s86, s86, 0x79a080\n"
        "s_add_u32 s86, s86, 0x7200\n"
        "v_mfma_f32_16x16x32_bf16 v[220:223], v[84:87], v[208:211], v[220:223]\n"
        "ds_read_b128 v[208:211], v14 offset:12288\n"
        "s_waitcnt lgkmcnt(5)\n"
        "v_mfma_f32_16x16x32_bf16 v[224:227], v[80:83], v[212:215], v[224:227]\n"
        "v_lshl_add_u32 v28, %[ln], 7, s86\n"
        "v_mov_b32 v25, 0x7f000000\n"
        "v_mfma_f32_16x16x32_bf16 v[228:231], v[84:87], v[212:215], v[228:231]\n"
        "ds_read_b128 v[212:215], v14 offset:14336\n"
        "s_waitcnt lgkmcnt(5)\n"
        "v_mfma_f32_16x16x32_bf16 v[232:235], v[80:83], v[192:195], v[232:235]\n"
        "v_cmp_gt_u32 vcc, 17, %[ln]\n"
        "s_nop 1\n"
        "v_cndmask_b32 v174, v25, v28, vcc\n"
        "v_mfma_f32_16x16x32_bf16 v[236:239], v[84:87], v[192:195], v[236:239]\n"
        "ds_read_b128 v[192:195], v14 offset:16384\n"
        "s_waitcnt lgkmcnt(5)\n"
        "v_mfma_f32_16x16x32_bf16 v[240:243], v[80:83], v[196:199], v[240:243]\n"
        "s_mul_i32 s86, s56, 4\n"
        "s_add_u32 s86, s86, s84\n"
        "v_mfma_f32_16x16x32_bf16 v[244:247], v[84:87], v[196:199], v[244:247]\n"
        "ds_read_b128 v[196:199], v14 offset:18432\n"
        "s_waitcnt lgkmcnt(5)\n"
        "v_mfma_f32_16x16x32_bf16 v[248:251], v[80:83], v[200:203], v[248:251]\n"
        "s_mul_i32 s43, s86, 20\n"
        "s_mul_i32 s43, s43, 16\n"
        "v_mfma_f32_16x16x32_bf16 v[252:255], v[84:87], v[200:203], v[252:255]\n"
        "ds_read_b128 v[200:203], v13 offset:20480\n"
        "s_waitcnt lgkmcnt(5)\n"
        "v_mfma_f32_16x16x32_bf16 v[216:219], v[88:91], v[204:207], v[216:219]\n"
        "s_add_u32 s43, s43, 0x400\n"
        "v_mul_u32_u24 v28, 52, %[ln]\n"
        "v_mfma_f32_16x16x32_bf16 v[220:223], v[92:95], v[204:207], v[220:223]\n"
        "ds_read_b128 v[204:207], v13 offset:22528\n"
        "s_waitcnt lgkmcnt(5)\n"
        "v_mfma_f32_16x16x32_bf16 v[224:227], v[88:91], v[208:211], v[224:227]\n"
        "v_lshrrev_b32 v28, 10, v28\n"
        "v_mul_u32_u24 v175, 0x26c0, v28\n"
        "v_mfma_f32_16x16x32_bf16 v[228:231], v[92:95], v[208:211], v[228:231]\n"
        "ds_read_b128 v[208:211], v13 offset:24576\n"
        "s_waitcnt lgkmcnt(5)\n"
        "v_mfma_f32_16x16x32_bf16 v[232:235], v[88:91], v[212:215], v[232:235]\n"
        "v_lshl_add_u32 v175, %[ln], 4, v175\n"
        "v_add_u32 v175, s43, v175\n"
        "v_mfma_f32_16x16x32_bf16 v[236:239], v[92:95], v[212:215], v[236:239]\n"
        "ds_read_b128 v[212:215], v13 offset:26624\n"
        "s_waitcnt lgkmcnt(5)\n"
        "v_mfma_f32_16x16x32_bf16 v[240:243], v[88:91], v[192:195], v[240:243]\n"
        "s_mul_i32 s43, s84, 4\n"
        "s_add_u32 s57, s43, s57\n"
        "v_mfma_f32_16x16x32_bf16 v[244:247], v[92:95], v[192:195], v[244:247]\n"
        "ds_read_b128 v[192:195], v13 offset:28672\n"
        "s_waitcnt lgkmcnt(5)\n"
        "v_mfma_f32_16x16x32_bf16 v[248:251], v[88:91], v[196:199], v[248:251]\n"
        "s_mul_i32 s57, s57, 64\n"
        "s_and_b32 s56, s56, 1\n"
        "v_mfma_f32_16x16x32_bf16 v[252:255], v[92:95], v[196:199], v[252:255]\n"
        "ds_read_b128 v[196:199], v14 offset:20480\n"
        "s_waitcnt lgkmcnt(5)\n"
        "v_mfma_f32_16x16x32_bf16 v[216:219], v[96:99], v[200:203], v[216:219]\n"
        "s_add_u32 s56, s41, s56\n"
        "s_mul_i32 s43, s56, 4\n"
        "v_mfma_f32_16x16x32_bf16 v[220:223], v[100:103], v[200:203], v[220:223]\n"
        "ds_read_b128 v[200:203], v14 offset:22528\n"
        "s_waitcnt lgkmcnt(5)\n"
        "v_mfma_f32_16x16x32_bf16 v[224:227], v[96:99], v[204:207], v[224:227]\n"
        "s_add_u32 s43, s57, s43\n"
        "s_add_u32 s43, s43, %[w]\n"
        "v_mfma_f32_16x16x32_bf16 v[228:231], v[100:103], v[204:207], v[228:231]\n"
        "ds_read_b128 v[204:207], v14 offset:24576\n"
        "s_waitcnt lgkmcnt(5)\n"
        "v_mfma_f32_16x16x32_bf16 v[232:235], v[96:99], v[208:211], v[232:235]\n"
        "s_mul_i32 s43, s43, 0x1400\n"
        "s_add_u32 s43, s43, 0x79a080\n"
        "v_mfma_f32_16x16x32_bf16 v[236:239], v[100:103], v[208:211], v[236:239]\n"
        "ds_read_b128 v[208:211], v14 offset:26624\n"
        "s_waitcnt lgkmcnt(5)\n"
        "v_mfma_f32_16x16x32_bf16 v[240:243], v[96:99], v[212:215], v[240:243]\n"
        "v_and_b32 v28, 1, v26\n"
        "v_lshrrev_b32 v25, 5, %[ln]\n"
        "v_mfma_f32_16x16x32_bf16 v[244:247], v[100:103], v[212:215], v[244:247]\n"
        "ds_read_b128 v[212:215], v14 offset:28672\n"
        "s_waitcnt lgkmcnt(5)\n"
        "v_mfma_f32_16x16x32_bf16 v[248:251], v[96:99], v[192:195], v[248:251]\n"
        "v_lshl_add_u32 v8, v28, 9, s43\n"
        "v_lshl_add_u32 v8, v25, 8, v8\n"
        "v_mfma_f32_16x16x32_bf16 v[252:255], v[100:103], v[192:195], v[252:255]\n"
        "v_add_u32 v13, 4294903808, v13\n"
        "s_waitcnt vmcnt(0)\n"
        "s_barrier\n"
        "ds_read_b128 v[192:195], v13\n"
        "s_waitcnt lgkmcnt(5)\n"
        "v_mfma_f32_16x16x32_bf16 v[216:219], v[104:107], v[196:199], v[216:219]\n"
        "v_lshl_add_u32 v8, v24, 4, v8\n"
        "v_add_u32 v27, s41, v24\n"
        "v_mfma_f32_16x16x32_bf16 v[220:223], v[108:111], v[196:199], v[220:223]\n"
        "ds_read_b128 v[196:199], v13 offset:2048\n"
        "s_waitcnt lgkmcnt(5)\n"
        "v_mfma_f32_16x16x32_bf16 v[224:227], v[104:107], v[200:203], v[224:227]\n"
        "v_mov_b32 v30, s56\n"
        "v_mfma_f32_16x16x32_bf16 v[228:231], v[108:111], v[200:203], v[228:231]\n"
        "ds_read_b128 v[200:203], v13 offset:4096\n"
        "s_waitcnt lgkmcnt(5)\n"
        "v_mfma_f32_16x16x32_bf16 v[232:235], v[104:107], v[204:207], v[232:235]\n"
        "v_cmp_gt_u32 vcc, 2, v24\n"
        "s_nop 1\n"
        "v_cndmask_b32 v29, v30, v27, vcc\n"
        "v_mfma_f32_16x16x32_bf16 v[236:239], v[108:111], v[204:207], v[236:239]\n"
        "ds_read_b128 v[204:207], v13 offset:6144\n"
        "s_waitcnt lgkmcnt(5)\n"
        "v_mfma_f32_16x16x32_bf16 v[240:243], v[104:107], v[208:211], v[240:243]\n"
        "v_add_u32 v27, 0xf0, v29\n"
        "s_mul_i32 s42, s42, 5\n"
        "v_mfma_f32_16x16x32_bf16 v[244:247], v[108:111], v[208:211], v[244:247]\n"
        "ds_read_b128 v[208:211], v13 offset:8192\n"
        "s_waitcnt lgkmcnt(5)\n"
        "v_mfma_f32_16x16x32_bf16 v[248:251], v[104:107], v[212:215], v[248:251]\n"
        "s_add_u32 s42, s42, 5\n"
        "s_lshr_b32 s41, s86, 1\n"
        "v_mfma_f32_16x16x32_bf16 v[252:255], v[108:111], v[212:215], v[252:255]\n"
        "v_add_u32 v14, 4294903808, v14\n"
        "ds_read_b128 v[212:215], v14\n"
        "s_waitcnt lgkmcnt(5)\n"
        "v_mfma_f32_16x16x32_bf16 v[216:219], v[112:115], v[192:195], v[216:219]\n"
        "s_mul_i32 s41, s41, 5\n"
        "s_and_b32 s84, s84, 1\n"
        "v_mfma_f32_16x16x32_bf16 v[220:223], v[116:119], v[192:195], v[220:223]\n"
        "ds_read_b128 v[192:195], v14 offset:2048\n"
        "s_waitcnt lgkmcnt(5)\n"
        "v_mfma_f32_16x16x32_bf16 v[224:227], v[112:115], v[196:199], v[224:227]\n"
        "s_mul_i32 s84, s84, 2\n"
        "s_add_u32 s84, s41, s84\n"
        "v_mfma_f32_16x16x32_bf16 v[228:231], v[116:119], v[196:199], v[228:231]\n"
        "ds_read_b128 v[196:199], v14 offset:4096\n"
        "s_waitcnt lgkmcnt(5)\n"
        "v_mfma_f32_16x16x32_bf16 v[232:235], v[112:115], v[200:203], v[232:235]\n"
        "s_lshr_b32 s41, s84, 4\n"
        "s_add_u32 s42, s42, s41\n"
        "v_mfma_f32_16x16x32_bf16 v[236:239], v[116:119], v[200:203], v[236:239]\n"
        "ds_read_b128 v[200:203], v14 offset:6144\n"
        "s_waitcnt lgkmcnt(5)\n"
        "v_mfma_f32_16x16x32_bf16 v[240:243], v[112:115], v[204:207], v[240:243]\n"
        "s_add_u32 s84, s84, 2\n"
        "s_lshr_b32 s84, s84, 4\n"
        "v_mfma_f32_16x16x32_bf16 v[244:247], v[116:119], v[204:207], v[244:247]\n"
        "ds_read_b128 v[204:207], v14 offset:8192\n"
        "s_waitcnt lgkmcnt(5)\n"
        "v_mfma_f32_16x16x32_bf16 v[248:251], v[112:115], v[208:211], v[248:251]\n"
        "s_sub_u32 s41, s84, s41\n"
        "s_mul_i32 s41, s59, s41\n"
        "v_mfma_f32_16x16x32_bf16 v[252:255], v[116:119], v[208:211], v[252:255]\n"
        "v_add_u32 v13, 94208, v13\n"
        "ds_read_b128 v[208:211], v13\n"
        "s_waitcnt lgkmcnt(5)\n"
        "v_mfma_f32_16x16x32_bf16 v[216:219], v[120:123], v[212:215], v[216:219]\n"
        "s_add_u32 s41, s42, s41\n"
        "s_mul_i32 s41, s41, 16\n"
        "v_mfma_f32_16x16x32_bf16 v[220:223], v[124:127], v[212:215], v[220:223]\n"
        "ds_read_b128 v[212:215], v13 offset:2048\n"
        "s_waitcnt lgkmcnt(5)\n"
        "v_mfma_f32_16x16x32_bf16 v[224:227], v[120:123], v[192:195], v[224:227]\n"
        "v_add_u32 v29, s41, v24\n"
        "v_mfma_f32_16x16x32_bf16 v[228:231], v[124:127], v[192:195], v[228:231]\n"
        "ds_read_b128 v[192:195], v13 offset:4096\n"
        "s_waitcnt lgkmcnt(5)\n"
        "v_mfma_f32_16x16x32_bf16 v[232:235], v[120:123], v[196:199], v[232:235]\n"
        "v_cmp_gt_u32 vcc, 4, v24\n"
        "s_nop 1\n"
        "v_cndmask_b32 v30, v29, v27, vcc\n"
        "v_mfma_f32_16x16x32_bf16 v[236:239], v[124:127], v[196:199], v[236:239]\n"
        "ds_read_b128 v[196:199], v13 offset:6144\n"
        "s_waitcnt lgkmcnt(5)\n"
        "v_mfma_f32_16x16x32_bf16 v[240:243], v[120:123], v[200:203], v[240:243]\n"
        "v_lshlrev_b32 v9, 7, v30\n"
        "v_add_u32 v9, 0x80, v9\n"
        "v_mfma_f32_16x16x32_bf16 v[244:247], v[124:127], v[200:203], v[244:247]\n"
        "ds_read_b128 v[200:203], v13 offset:8192\n"
        "s_waitcnt lgkmcnt(5)\n"
        "v_mfma_f32_16x16x32_bf16 v[248:251], v[120:123], v[204:207], v[248:251]\n"
        "s_mul_i32 s56, s56, 2\n"
        "s_add_u32 s58, s56, s58\n"
        "v_mfma_f32_16x16x32_bf16 v[252:255], v[124:127], v[204:207], v[252:255]\n"
        "v_add_u32 v14, 94208, v14\n"
        "ds_read_b128 v[204:207], v14\n"
        "s_waitcnt lgkmcnt(5)\n"
        "v_mfma_f32_16x16x32_bf16 v[216:219], v[128:131], v[208:211], v[216:219]\n"
        "s_lshr_b32 s58, s58, 2\n"
        "s_mul_i32 s58, s58, 32\n"
        "v_mfma_f32_16x16x32_bf16 v[220:223], v[132:135], v[208:211], v[220:223]\n"
        "ds_read_b128 v[208:211], v14 offset:2048\n"
        "s_waitcnt lgkmcnt(5)\n"
        "v_mfma_f32_16x16x32_bf16 v[224:227], v[128:131], v[212:215], v[224:227]\n"
        "s_mul_i32 s56, %[w], 8\n"
        "s_add_u32 s56, s58, s56\n"
        "v_mfma_f32_16x16x32_bf16 v[228:231], v[132:135], v[212:215], v[228:231]\n"
        "ds_read_b128 v[212:215], v14 offset:4096\n"
        "s_waitcnt lgkmcnt(5)\n"
        "v_mfma_f32_16x16x32_bf16 v[232:235], v[128:131], v[192:195], v[232:235]\n"
        "v_add_u32 v30, s56, %[ln]\n"
        "s_mul_i32 s40, s40, 32\n"
        "v_mfma_f32_16x16x32_bf16 v[236:239], v[132:135], v[192:195], v[236:239]\n"
        "ds_read_b128 v[192:195], v14 offset:6144\n"
        "s_waitcnt lgkmcnt(5)\n"
        "v_mfma_f32_16x16x32_bf16 v[240:243], v[128:131], v[196:199], v[240:243]\n"
        "v_add_u32 v29, 1, %[ln]\n"
        "v_and_b32 v29, 3, v29\n"
        "v_mfma_f32_16x16x32_bf16 v[244:247], v[132:135], v[196:199], v[244:247]\n"
        "ds_read_b128 v[196:199], v14 offset:8192\n"
        "s_waitcnt lgkmcnt(5)\n"
        "v_mfma_f32_16x16x32_bf16 v[248:251], v[128:131], v[200:203], v[248:251]\n"
        "v_lshlrev_b32 v27, 3, v29\n"
        "v_xor_b32 v27, s86, v27\n"
        "v_mfma_f32_16x16x32_bf16 v[252:255], v[132:135], v[200:203], v[252:255]\n"
        "ds_read_b128 v[200:203], v13 offset:10240\n"
        "s_waitcnt lgkmcnt(5)\n"
        "v_mfma_f32_16x16x32_bf16 v[216:219], v[136:139], v[204:207], v[216:219]\n"
        "v_add_u32 v29, s40, v27\n"
        "v_mfma_f32_16x16x32_bf16 v[220:223], v[140:143], v[204:207], v[220:223]\n"
        "ds_read_b128 v[204:207], v13 offset:12288\n"
        "s_waitcnt lgkmcnt(5)\n"
        "v_mfma_f32_16x16x32_bf16 v[224:227], v[136:139], v[208:211], v[224:227]\n"
        "v_cmp_gt_u32 vcc, 8, %[ln]\n"
        "s_nop 1\n"
        "v_cndmask_b32 v27, v29, v30, vcc\n"
        "v_mfma_f32_16x16x32_bf16 v[228:231], v[140:143], v[208:211], v[228:231]\n"
        "ds_read_b128 v[208:211], v13 offset:14336\n"
        "s_waitcnt lgkmcnt(5)\n"
        "v_mfma_f32_16x16x32_bf16 v[232:235], v[136:139], v[212:215], v[232:235]\n"
        "v_lshlrev_b32 v10, 7, v27\n"
        "v_add_u32 v10, 0x10080, v10\n"
        "v_mfma_f32_16x16x32_bf16 v[236:239], v[140:143], v[212:215], v[236:239]\n"
        "ds_read_b128 v[212:215], v13 offset:16384\n"
        "s_waitcnt lgkmcnt(5)\n"
        "v_mfma_f32_16x16x32_bf16 v[240:243], v[136:139], v[192:195], v[240:243]\n"
        "v_mul_u32_u24 v11, 0x280, v25\n"
        "v_lshl_add_u32 v11, v28, 8, v11\n"
        "v_mfma_f32_16x16x32_bf16 v[244:247], v[140:143], v[192:195], v[244:247]\n"
        "ds_read_b128 v[192:195], v13 offset:18432\n"
        "s_waitcnt lgkmcnt(5)\n"
        "v_mfma_f32_16x16x32_bf16 v[248:251], v[136:139], v[196:199], v[248:251]\n"
        "v_lshl_add_u32 v11, v24, 4, v11\n"
        "v_mul_u32_u24 v12, 0x500, v26\n"
        "v_mfma_f32_16x16x32_bf16 v[252:255], v[140:143], v[196:199], v[252:255]\n"
        "ds_read_b128 v[196:199], v14 offset:10240\n"
        "s_waitcnt lgkmcnt(5)\n"
        "v_mfma_f32_16x16x32_bf16 v[216:219], v[144:147], v[200:203], v[216:219]\n"
        "v_lshl_add_u32 v12, v24, 3, v12\n"
        "v_add_u32 v12, 0x200, v12\n"
        "v_mfma_f32_16x16x32_bf16 v[220:223], v[148:151], v[200:203], v[220:223]\n"
        "ds_read_b128 v[200:203], v14 offset:12288\n"
        "s_waitcnt lgkmcnt(5)\n"
        "v_mfma_f32_16x16x32_bf16 v[224:227], v[144:147], v[204:207], v[224:227]\n"
        "s_mul_i32 s40, %[w], 0x1400\n"
        "s_add_u32 s40, s40, 0x21800\n"
        "v_mfma_f32_16x16x32_bf16 v[228:231], v[148:151], v[204:207], v[228:231]\n"
        "ds_read_b128 v[204:207], v14 offset:14336\n"
        "s_waitcnt lgkmcnt(5)\n"
        "v_mfma_f32_16x16x32_bf16 v[232:235], v[144:147], v[208:211], v[232:235]\n"
        "v_lshl_add_u32 v48, %[ln], 3, s40\n"
        "v_mov_b32 v24, 0\n"
        "v_mfma_f32_16x16x32_bf16 v[236:239], v[148:151], v[208:211], v[236:239]\n"
        "ds_read_b128 v[208:211], v14 offset:16384\n"
        "s_waitcnt lgkmcnt(5)\n"
        "v_mfma_f32_16x16x32_bf16 v[240:243], v[144:147], v[212:215], v[240:243]\n"
        "v_mov_b32 v25, 0\n"
        "v_mov_b32 v26, 0\n"
        "v_mfma_f32_16x16x32_bf16 v[244:247], v[148:151], v[212:215], v[244:247]\n"
        "ds_read_b128 v[212:215], v14 offset:18432\n"
        "s_waitcnt lgkmcnt(5)\n"
        "v_mfma_f32_16x16x32_bf16 v[248:251], v[144:147], v[192:195], v[248:251]\n"
        "v_mov_b32 v27, 0\n"
        "v_mov_b32 v28, 0\n"
        "v_mfma_f32_16x16x32_bf16 v[252:255], v[148:151], v[192:195], v[252:255]\n"
        "v_add_u32 v13, 4294883328, v13\n"
        "ds_read_b128 v[192:195], v13\n"
        "s_waitcnt lgkmcnt(5)\n"
        "v_mfma_f32_16x16x32_bf16 v[216:219], v[176:179], v[196:199], v[216:219]\n"
        "v_mov_b32 v29, 0\n"
        "v_mov_b32 v30, 0\n"
        "v_mfma_f32_16x16x32_bf16 v[220:223], v[180:183], v[196:199], v[220:223]\n"
        "ds_read_b128 v[196:199], v13 offset:2048\n"
        "s_waitcnt lgkmcnt(5)\n"
        "v_mfma_f32_16x16x32_bf16 v[224:227], v[176:179], v[200:203], v[224:227]\n"
        "v_mov_b32 v31, 0\n"
        "v_mov_b32 v32, 0\n"
        "v_mfma_f32_16x16x32_bf16 v[228:231], v[180:183], v[200:203], v[228:231]\n"
        "ds_read_b128 v[200:203], v13 offset:4096\n"
        "s_waitcnt lgkmcnt(5)\n"
        "v_mfma_f32_16x16x32_bf16 v[232:235], v[176:179], v[204:207], v[232:235]\n"
        "v_mov_b32 v33, 0\n"
        "v_mov_b32 v34, 0\n"
        "v_mfma_f32_16x16x32_bf16 v[236:239], v[180:183], v[204:207], v[236:239]\n"
        "ds_read_b128 v[204:207], v13 offset:6144\n"
        "s_waitcnt lgkmcnt(5)\n"
        "v_mfma_f32_16x16x32_bf16 v[240:243], v[176:179], v[208:211], v[240:243]\n"
        "v_mov_b32 v35, 0\n"
        "v_mov_b32 v36, 0\n"
        "v_mfma_f32_16x16x32_bf16 v[244:247], v[180:183], v[208:211], v[244:247]\n"
        "ds_read_b128 v[208:211], v13 offset:8192\n"
        "s_waitcnt lgkmcnt(5)\n"
        "v_mfma_f32_16x16x32_bf16 v[248:251], v[176:179], v[212:215], v[248:251]\n"
        "v_mov_b32 v37, 0\n"
        "v_mov_b32 v38, 0\n"
        "v_mfma_f32_16x16x32_bf16 v[252:255], v[180:183], v[212:215], v[252:255]\n"
        "v_add_u32 v14, 4294883328, v14\n"
        "ds_read_b128 v[212:215], v14\n"
        "s_waitcnt lgkmcnt(5)\n"
        "v_mfma_f32_16x16x32_bf16 v[216:219], v[184:187], v[192:195], v[216:219]\n"
        "v_mov_b32 v39, 0\n"
        "v_mov_b32 v40, 0\n"
        "v_mfma_f32_16x16x32_bf16 v[220:223], v[188:191], v[192:195], v[220:223]\n"
        "ds_read_b128 v[192:195], v14 offset:2048\n"
        "s_waitcnt lgkmcnt(5)\n"
        "v_mfma_f32_16x16x32_bf16 v[224:227], v[184:187], v[196:199], v[224:227]\n"
        "v_mov_b32 v41, 0\n"
        "v_mov_b32 v42, 0\n"
        "v_mfma_f32_16x16x32_bf16 v[228:231], v[188:191], v[196:199], v[228:231]\n"
        "ds_read_b128 v[196:199], v14 offset:4096\n"
        "s_waitcnt lgkmcnt(5)\n"
        "v_mfma_f32_16x16x32_bf16 v[232:235], v[184:187], v[200:203], v[232:235]\n"
        "v_mov_b32 v43, 0\n"
        "v_mov_b32 v44, 0\n"
        "v_mfma_f32_16x16x32_bf16 v[236:239], v[188:191], v[200:203], v[236:239]\n"
        "ds_read_b128 v[200:203], v14 offset:6144\n"
        "s_waitcnt lgkmcnt(5)\n"
        "v_mfma_f32_16x16x32_bf16 v[240:243], v[184:187], v[204:207], v[240:243]\n"
        "v_mov_b32 v45, 0\n"
        "v_mov_b32 v46, 0\n"
        "v_mfma_f32_16x16x32_bf16 v[244:247], v[188:191], v[204:207], v[244:247]\n"
        "ds_read_b128 v[204:207], v14 offset:8192\n"
        "s_waitcnt lgkmcnt(5)\n"
        "v_mfma_f32_16x16x32_bf16 v[248:251], v[184:187], v[208:211], v[248:251]\n"
        "v_mov_b32 v47, 0\n"
        "v_mov_b32 v152, 0xdddddddd\n"
        "v_mfma_f32_16x16x32_bf16 v[252:255], v[188:191], v[208:211], v[252:255]\n"
        "s_waitcnt lgkmcnt(4)\n"
        "v_mfma_f32_16x16x32_bf16 v[216:219], v[16:19], v[212:215], v[216:219]\n"
        "v_mfma_f32_16x16x32_bf16 v[220:223], v[20:23], v[212:215], v[220:223]\n"
        "s_waitcnt lgkmcnt(3)\n"
        "v_mfma_f32_16x16x32_bf16 v[224:227], v[16:19], v[192:195], v[224:227]\n"
        "v_mfma_f32_16x16x32_bf16 v[228:231], v[20:23], v[192:195], v[228:231]\n"
        "s_waitcnt lgkmcnt(2)\n"
        "v_mfma_f32_16x16x32_bf16 v[232:235], v[16:19], v[196:199], v[232:235]\n"
        "v_mfma_f32_16x16x32_bf16 v[236:239], v[20:23], v[196:199], v[236:239]\n"
        "s_waitcnt lgkmcnt(1)\n"
        "v_mfma_f32_16x16x32_bf16 v[240:243], v[16:19], v[200:203], v[240:243]\n"
        "v_mfma_f32_16x16x32_bf16 v[244:247], v[20:23], v[200:203], v[244:247]\n"
        "s_waitcnt lgkmcnt(0)\n"
        "v_mfma_f32_16x16x32_bf16 v[248:251], v[16:19], v[204:207], v[248:251]\n"
        "v_mfma_f32_16x16x32_bf16 v[252:255], v[20:23], v[204:207], v[252:255]\n"
        "v_mov_b32 v13, v48\n"
        "v_mov_b32 v16, 0\n"
        "v_mov_b32 v17, 0\n"
        "v_mov_b32 v18, 0\n"
        "v_mov_b32 v19, 0\n"
        "v_mov_b32 v20, 0\n"
        "v_mov_b32 v21, 0\n"
        "v_mov_b32 v22, 0\n"
        "v_mov_b32 v23, 0\n"
        "v_cvt_pk_bf16_f32 v176, v216, v217\n"
        "v_cvt_pk_bf16_f32 v177, v218, v219\n"
        "v_cvt_pk_bf16_f32 v178, v220, v221\n"
        "v_cvt_pk_bf16_f32 v179, v222, v223\n"
        "v_cvt_pk_bf16_f32 v180, v224, v225\n"
        "v_cvt_pk_bf16_f32 v181, v226, v227\n"
        "v_cvt_pk_bf16_f32 v182, v228, v229\n"
        "v_cvt_pk_bf16_f32 v183, v230, v231\n"
        "v_permlane16_swap_b32 v176, v178\n"
        "v_permlane16_swap_b32 v177, v179\n"
        "v_cvt_pk_bf16_f32 v184, v232, v233\n"
        "v_cvt_pk_bf16_f32 v185, v234, v235\n"
        "v_cvt_pk_bf16_f32 v186, v236, v237\n"
        "v_cvt_pk_bf16_f32 v187, v238, v239\n"
        "v_permlane16_swap_b32 v180, v182\n"
        "v_permlane16_swap_b32 v181, v183\n"
        "v_cvt_pk_bf16_f32 v188, v240, v241\n"
        "v_cvt_pk_bf16_f32 v189, v242, v243\n"
        "v_cvt_pk_bf16_f32 v190, v244, v245\n"
        "v_cvt_pk_bf16_f32 v191, v246, v247\n"
        "v_permlane16_swap_b32 v184, v186\n"
        "v_permlane16_swap_b32 v185, v187\n"
        "v_cvt_pk_bf16_f32 v192, v248, v249\n"
        "v_cvt_pk_bf16_f32 v193, v250, v251\n"
        "v_cvt_pk_bf16_f32 v194, v252, v253\n"
        "v_cvt_pk_bf16_f32 v195, v254, v255\n"
        "v_permlane16_swap_b32 v188, v190\n"
        "v_permlane16_swap_b32 v189, v191\n"
        "s_nop 1\n"
        "v_permlane16_swap_b32 v192, v194\n"
        "v_permlane16_swap_b32 v193, v195\n"
        "s_barrier\n"
        "s_setprio 2\n"
        "ds_read_b128 v[112:115], v156\n"
        "ds_read_b64 v[116:117], v157\n"
        "ds_read_b128 v[124:127], v158\n"
        "s_nop 0\n"
        ".Lk_hop0:\n"
        "s_cbranch_execz .Lk_pad3\n"
        "ds_read_b64 v[128:129], v159\n"
        "ds_read_b128 v[118:121], v156 offset:1024\n"
        "ds_read_b64 v[122:123], v157 offset:512\n"
        "ds_read_b128 v[130:133], v158 offset:2848\n"
        "ds_read_b64 v[134:135], v159 offset:2848\n"
        "ds_read_b128 v[136:139], v160\n"
        "ds_read_b128 v[140:143], v161\n"
        "s_add_u32 m0, s71, 22784\n"
        "s_add_u32 s56, s70, 655360\n"
        "s_add_u32 s57, s56, s83\n"
        "buffer_load_dwordx4 v162, s[52:55], s57 offen sc1 lds\n"
        "s_waitcnt lgkmcnt(0)\n"
        "s_mov_b32 exec_lo, 0\n"
        "v_mov_b32 v116, 0\n"
        "v_mov_b32 v117, 0\n"
        "v_mov_b32 v122, 0\n"
        "v_mov_b32 v123, 0\n"
        "s_mov_b32 exec_lo, -1\n"
        "v_mfma_f32_32x32x16_bf16 v[48:63], v[124:127], v[112:115], 0\n"
        "v_mfma_f32_32x32x8_bf16 v[48:63], v[128:129], v[116:117], v[48:63]\n"
        "buffer_load_dwordx4 v175, s[52:55], s56 offen offset:1024 sc1 lds\n"
        "s_nop 11\n"
        "v_mfma_f32_32x32x16_bf16 v[64:79], v[124:127], v[118:121], 0\n"
        "v_exp_f32 v48, v48\n"
        "v_exp_f32 v49, v49\n"
        "v_exp_f32 v50, v50\n"
        "v_exp_f32 v51, v51\n"
        "v_mfma_f32_32x32x8_bf16 v[64:79], v[128:129], v[122:123], v[64:79]\n"
        "s_mov_b32 exec_hi, 0xffff\n"
        "buffer_load_dwordx4 v163, s[52:55], s56 offen offset:2048 sc1 lds\n"
        "s_mov_b32 exec_hi, -1\n"
        "v_exp_f32 v52, v52\n"
        "v_exp_f32 v53, v53\n"
        "v_exp_f32 v54, v54\n"
        "v_exp_f32 v55, v55\n"
        "v_exp_f32 v56, v56\n"
        "v_exp_f32 v57, v57\n"
        "v_exp_f32 v58, v58\n"
        "v_exp_f32 v59, v59\n"
        "s_waitcnt lgkmcnt(0)\n"
        "v_smfmac_f32_32x32x32_bf16 v[16:31], v[136:139], v[48:55], v152\n"
        "s_add_u32 m0, s71, 25632\n"
        "s_add_u32 s56, s56, 81920\n"
        "s_add_u32 s57, s56, s83\n"
        "buffer_load_dwordx4 v162, s[52:55], s57 offen sc1 lds\n"
        "v_exp_f32 v60, v60\n"
        "v_exp_f32 v61, v61\n"
        "v_exp_f32 v62, v62\n"
        "v_exp_f32 v63, v63\n"
        "v_mfma_f32_32x32x16_bf16 v[80:95], v[130:133], v[112:115], 0\n"
        "ds_read_b128 v[144:147], v160 offset:2848\n"
        "v_exp_f32 v64, v64\n"
        "v_exp_f32 v65, v65\n"
        "v_exp_f32 v66, v66\n"
        "v_exp_f32 v67, v67\n"
        "v_mfma_f32_32x32x8_bf16 v[80:95], v[134:135], v[116:117], v[80:95]\n"
        "ds_read_b128 v[148:151], v161 offset:2848\n"
        "buffer_load_dwordx4 v175, s[52:55], s56 offen offset:1024 sc1 lds\n"
        "v_exp_f32 v68, v68\n"
        "v_exp_f32 v69, v69\n"
        "v_exp_f32 v70, v70\n"
        "v_exp_f32 v71, v71\n"
        "v_smfmac_f32_32x32x32_bf16 v[16:31], v[140:143], v[56:63], v152\n"
        "ds_read_b128 v[124:127], v158 offset:5696\n"
        "v_exp_f32 v72, v72\n"
        "v_exp_f32 v73, v73\n"
        "v_exp_f32 v74, v74\n"
        "v_exp_f32 v75, v75\n"
        "v_smfmac_f32_32x32x32_bf16 v[32:47], v[136:139], v[64:71], v152\n"
        "ds_read_b64 v[128:129], v159 offset:5696\n"
        "s_mov_b32 exec_hi, 0xffff\n"
        "buffer_load_dwordx4 v163, s[52:55], s56 offen offset:2048 sc1 lds\n"
        "s_mov_b32 exec_hi, -1\n"
        "v_exp_f32 v76, v76\n"
        "v_exp_f32 v77, v77\n"
        "v_exp_f32 v78, v78\n"
        "v_exp_f32 v79, v79\n"
        "v_mfma_f32_32x32x16_bf16 v[96:111], v[130:133], v[118:121], 0\n"
        "v_exp_f32 v80, v80\n"
        "v_exp_f32 v81, v81\n"
        "v_exp_f32 v82, v82\n"
        "v_exp_f32 v83, v83\n"
        "v_mfma_f32_32x32x8_bf16 v[96:111], v[134:135], v[122:123], v[96:111]\n"
        "s_mov_b64 exec, 0xff\n"
        "buffer_load_dwordx2 v[154:155], v9, s[52:55], 0 offen sc1\n"
        "s_mov_b64 exec, -1\n"
        "v_exp_f32 v84, v84\n"
        "v_exp_f32 v85, v85\n"
        "v_exp_f32 v86, v86\n"
        "v_exp_f32 v87, v87\n"
        "v_smfmac_f32_32x32x32_bf16 v[32:47], v[140:143], v[72:79], v152\n"
        "v_exp_f32 v88, v88\n"
        "v_exp_f32 v89, v89\n"
        "v_exp_f32 v90, v90\n"
        "v_exp_f32 v91, v91\n"
        "s_waitcnt lgkmcnt(0)\n"
        "v_smfmac_f32_32x32x32_bf16 v[16:31], v[144:147], v[80:87], v152\n"
        "v_lshlrev_b32 v153, 3, v162\n"
        "s_mov_b32 exec_hi, 0x3ffff\n"
        "buffer_load_dword v153, v153, s[48:51], s93 offen\n"
        "s_mov_b32 exec_hi, -1\n"
        "v_exp_f32 v92, v92\n"
        "v_exp_f32 v93, v93\n"
        "v_exp_f32 v94, v94\n"
        "v_exp_f32 v95, v95\n"
        "v_mfma_f32_32x32x16_bf16 v[48:63], v[124:127], v[112:115], 0\n"
        "ds_read_b128 v[136:139], v160 offset:5696\n"
        "v_exp_f32 v96, v96\n"
        "v_exp_f32 v97, v97\n"
        "v_exp_f32 v98, v98\n"
        "v_exp_f32 v99, v99\n"
        "v_mfma_f32_32x32x8_bf16 v[48:63], v[128:129], v[116:117], v[48:63]\n"
        "ds_read_b128 v[140:143], v161 offset:5696\n"
        "buffer_load_dword v153, v173, s[44:47], s75 offen offset:256\n"
        "v_exp_f32 v100, v100\n"
        "v_exp_f32 v101, v101\n"
        "v_exp_f32 v102, v102\n"
        "v_exp_f32 v103, v103\n"
        "v_smfmac_f32_32x32x32_bf16 v[16:31], v[148:151], v[88:95], v152\n"
        "ds_read_b128 v[130:133], v158 offset:8544\n"
        "v_exp_f32 v104, v104\n"
        "v_exp_f32 v105, v105\n"
        "v_exp_f32 v106, v106\n"
        "v_exp_f32 v107, v107\n"
        "v_smfmac_f32_32x32x32_bf16 v[32:47], v[144:147], v[96:103], v152\n"
        "ds_read_b64 v[134:135], v159 offset:8544\n"
        "v_exp_f32 v108, v108\n"
        "v_exp_f32 v109, v109\n"
        "v_exp_f32 v110, v110\n"
        "v_exp_f32 v111, v111\n"
        "v_mfma_f32_32x32x16_bf16 v[64:79], v[124:127], v[118:121], 0\n"
        "v_exp_f32 v48, v48\n"
        "v_exp_f32 v49, v49\n"
        "v_exp_f32 v50, v50\n"
        "v_exp_f32 v51, v51\n"
        "v_mfma_f32_32x32x8_bf16 v[64:79], v[128:129], v[122:123], v[64:79]\n"
        "v_exp_f32 v52, v52\n"
        "v_exp_f32 v53, v53\n"
        "v_exp_f32 v54, v54\n"
        "v_exp_f32 v55, v55\n"
        "v_smfmac_f32_32x32x32_bf16 v[32:47], v[148:151], v[104:111], v152\n"
        "v_exp_f32 v56, v56\n"
        "v_exp_f32 v57, v57\n"
        "v_exp_f32 v58, v58\n"
        "v_exp_f32 v59, v59\n"
        "s_waitcnt lgkmcnt(0)\n"
        "v_smfmac_f32_32x32x32_bf16 v[16:31], v[136:139], v[48:55], v152\n"
        "s_setprio 0\n"
        "v_exp_f32 v60, v60\n"
        "v_exp_f32 v61, v61\n"
        "v_exp_f32 v62, v62\n"
        "v_exp_f32 v63, v63\n"
        "v_mfma_f32_32x32x16_bf16 v[80:95], v[130:133], v[112:115], 0\n"
        "ds_read_b128 v[144:147], v160 offset:8544\n"
        "v_exp_f32 v64, v64\n"
        "v_exp_f32 v65, v65\n"
        "v_exp_f32 v66, v66\n"
        "v_exp_f32 v67, v67\n"
        "v_mfma_f32_32x32x8_bf16 v[80:95], v[134:135], v[116:117], v[80:95]\n"
        "ds_read_b128 v[148:151], v161 offset:8544\n"
        "v_exp_f32 v68, v68\n"
        "v_exp_f32 v69, v69\n"
        "v_exp_f32 v70, v70\n"
        "v_exp_f32 v71, v71\n"
        "v_smfmac_f32_32x32x32_bf16 v[16:31], v[140:143], v[56:63], v152\n"
        "v_exp_f32 v72, v72\n"
        "v_exp_f32 v73, v73\n"
        "v_exp_f32 v74, v74\n"
        "v_exp_f32 v75, v75\n"
        "v_smfmac_f32_32x32x32_bf16 v[32:47], v[136:139], v[64:71], v152\n"
        "s_waitcnt vmcnt(2) lgkmcnt(0)\n"
        "s_mov_b64 exec, 0xff\n"
        "s_mov_b32 s58, 0\n"
        "17:\n"
        "v_cmp_eq_u64 vcc, s[60:61], v[154:155]\n"
        "s_nop 1\n"
        "s_cmp_eq_u64 vcc, exec\n"
        "s_cbranch_scc1 18f\n"
        "s_sleep 2\n"
        "buffer_load_dwordx2 v[154:155], v9, s[52:55], 0 offen sc1\n"
        "s_add_u32 s58, s58, 1\n"
        "s_cmp_lt_u32 s58, 262144\n"
        "s_waitcnt vmcnt(0)\n"
        "s_cbranch_scc1 17b\n"
        "18:\n"
        "s_mov_b64 exec, -1\n"
        "s_barrier\n"
        "s_setprio 2\n"
        "ds_read_b128 v[124:127], v158 offset:22784\n"
        "ds_read_b64 v[128:129], v159 offset:22784\n"
        "v_mov_b32 v154, s60\n"
        "v_mov_b32 v155, s61\n"
        "s_mov_b64 exec, 1\n"
        "buffer_store_dwordx2 v[154:155], v162, s[52:55], s91 offen sc1\n"
        "s_mov_b64 exec, -1\n"
        "v_exp_f32 v76, v76\n"
        "v_exp_f32 v77, v77\n"
        "v_exp_f32 v78, v78\n"
        "v_exp_f32 v79, v79\n"
        "v_mfma_f32_32x32x16_bf16 v[96:111], v[130:133], v[118:121], 0\n"
        "s_add_u32 m0, s71, 0\n"
        "s_add_u32 s56, s70, 1310720\n"
        "s_add_u32 s57, s56, s83\n"
        "buffer_load_dwordx4 v162, s[52:55], s57 offen sc1 lds\n"
        "v_exp_f32 v80, v80\n"
        "v_exp_f32 v81, v81\n"
        "v_exp_f32 v82, v82\n"
        "v_exp_f32 v83, v83\n"
        "v_mfma_f32_32x32x8_bf16 v[96:111], v[134:135], v[122:123], v[96:111]\n"
        "buffer_load_dwordx4 v175, s[52:55], s56 offen offset:1024 sc1 lds\n"
        "v_exp_f32 v84, v84\n"
        "v_exp_f32 v85, v85\n"
        "v_exp_f32 v86, v86\n"
        "v_exp_f32 v87, v87\n"
        "v_smfmac_f32_32x32x32_bf16 v[32:47], v[140:143], v[72:79], v152\n"
        "s_mov_b32 exec_hi, 0xffff\n"
        "buffer_load_dwordx4 v163, s[52:55], s56 offen offset:2048 sc1 lds\n"
        "s_mov_b32 exec_hi, -1\n"
        "v_exp_f32 v88, v88\n"
        "v_exp_f32 v89, v89\n"
        "v_exp_f32 v90, v90\n"
        "v_exp_f32 v91, v91\n"
        "s_waitcnt lgkmcnt(0)\n"
        "v_smfmac_f32_32x32x32_bf16 v[16:31], v[144:147], v[80:87], v152\n"
        "s_add_u32 m0, s71, 2848\n"
        "s_add_u32 s56, s56, 81920\n"
        "s_add_u32 s57, s56, s83\n"
        "buffer_load_dwordx4 v162, s[52:55], s57 offen sc1 lds\n"
        "v_exp_f32 v92, v92\n"
        "v_exp_f32 v93, v93\n"
        "v_exp_f32 v94, v94\n"
        "v_exp_f32 v95, v95\n"
        "v_mfma_f32_32x32x16_bf16 v[48:63], v[124:127], v[112:115], 0\n"
        "ds_read_b128 v[136:139], v160 offset:22784\n"
        "v_exp_f32 v96, v96\n"
        "v_exp_f32 v97, v97\n"
        "v_exp_f32 v98, v98\n"
        "v_exp_f32 v99, v99\n"
        "v_mfma_f32_32x32x8_bf16 v[48:63], v[128:129], v[116:117], v[48:63]\n"
        "ds_read_b128 v[140:143], v161 offset:22784\n"
        "buffer_load_dwordx4 v175, s[52:55], s56 offen offset:1024 sc1 lds\n"
        "v_exp_f32 v100, v100\n"
        "v_exp_f32 v101, v101\n"
        "v_exp_f32 v102, v102\n"
        "v_exp_f32 v103, v103\n"
        "v_smfmac_f32_32x32x32_bf16 v[16:31], v[148:151], v[88:95], v152\n"
        "ds_read_b128 v[130:133], v158 offset:25632\n"
        "s_mov_b32 exec_hi, 0xffff\n"
        "buffer_load_dwordx4 v163, s[52:55], s56 offen offset:2048 sc1 lds\n"
        "s_mov_b32 exec_hi, -1\n"
        "v_exp_f32 v104, v104\n"
        "v_exp_f32 v105, v105\n"
        "v_exp_f32 v106, v106\n"
        "v_exp_f32 v107, v107\n"
        "v_smfmac_f32_32x32x32_bf16 v[32:47], v[144:147], v[96:103], v152\n"
        "ds_read_b64 v[134:135], v159 offset:25632\n"
        "s_movk_i32 s56, 0x1000\n"
        "buffer_store_dwordx4 v[176:179], v8, s[52:55], 0 offen sc1\n"
        "v_exp_f32 v108, v108\n"
        "v_exp_f32 v109, v109\n"
        "v_exp_f32 v110, v110\n"
        "v_exp_f32 v111, v111\n"
        "v_mfma_f32_32x32x16_bf16 v[64:79], v[124:127], v[118:121], 0\n"
        "buffer_store_dwordx4 v[180:183], v8, s[52:55], 0 offen offset:1024 sc1\n"
        "v_exp_f32 v48, v48\n"
        "v_exp_f32 v49, v49\n"
        "v_exp_f32 v50, v50\n"
        "v_exp_f32 v51, v51\n"
        "v_mfma_f32_32x32x8_bf16 v[64:79], v[128:129], v[122:123], v[64:79]\n"
        "v_exp_f32 v52, v52\n"
        "v_exp_f32 v53, v53\n"
        "v_exp_f32 v54, v54\n"
        "v_exp_f32 v55, v55\n"
        "v_smfmac_f32_32x32x32_bf16 v[32:47], v[148:151], v[104:111], v152\n"
        "buffer_store_dwordx4 v[184:187], v8, s[52:55], 0 offen offset:2048 sc1\n"
        "v_exp_f32 v56, v56\n"
        "v_exp_f32 v57, v57\n"
        "v_exp_f32 v58, v58\n"
        "v_exp_f32 v59, v59\n"
        "s_waitcnt lgkmcnt(0)\n"
        "v_smfmac_f32_32x32x32_bf16 v[16:31], v[136:139], v[48:55], v152\n"
        "buffer_store_dwordx4 v[188:191], v8, s[52:55], 0 offen offset:3072 sc1\n"
        "v_exp_f32 v60, v60\n"
        "v_exp_f32 v61, v61\n"
        "v_exp_f32 v62, v62\n"
        "v_exp_f32 v63, v63\n"
        "v_mfma_f32_32x32x16_bf16 v[80:95], v[130:133], v[112:115], 0\n"
        "ds_read_b128 v[144:147], v160 offset:25632\n"
        "buffer_store_dwordx4 v[192:195], v8, s[52:55], s56 offen sc1\n"
        "v_exp_f32 v64, v64\n"
        "v_exp_f32 v65, v65\n"
        "v_exp_f32 v66, v66\n"
        "v_exp_f32 v67, v67\n"
        "v_mfma_f32_32x32x8_bf16 v[80:95], v[134:135], v[116:117], v[80:95]\n"
        "ds_read_b128 v[148:151], v161 offset:25632\n"
        "s_add_u32 m0, s69, 0\n"
        "s_add_u32 s56, s68, 0\n"
        "buffer_load_dwordx4 v166, s[48:51], s56 offen lds\n"
        "v_exp_f32 v68, v68\n"
        "v_exp_f32 v69, v69\n"
        "v_exp_f32 v70, v70\n"
        "v_exp_f32 v71, v71\n"
        "v_smfmac_f32_32x32x32_bf16 v[16:31], v[140:143], v[56:63], v152\n"
        "ds_read_b128 v[124:127], v158 offset:28480\n"
        "s_add_u32 m0, s69, 1024\n"
        "s_add_u32 s56, s68, 0\n"
        "buffer_load_dwordx4 v167, s[48:51], s56 offen lds\n"
        "v_exp_f32 v72, v72\n"
        "v_exp_f32 v73, v73\n"
        "v_exp_f32 v74, v74\n"
        "v_exp_f32 v75, v75\n"
        "v_smfmac_f32_32x32x32_bf16 v[32:47], v[136:139], v[64:71], v152\n"
        "ds_read_b64 v[128:129], v159 offset:28480\n"
        "v_exp_f32 v76, v76\n"
        "v_exp_f32 v77, v77\n"
        "v_exp_f32 v78, v78\n"
        "v_exp_f32 v79, v79\n"
        "v_mfma_f32_32x32x16_bf16 v[96:111], v[130:133], v[118:121], 0\n"
        "s_add_u32 m0, s69, 2048\n"
        "s_add_u32 s56, s68, 0\n"
        "buffer_load_dwordx4 v168, s[48:51], s56 offen lds\n"
        "v_exp_f32 v80, v80\n"
        "v_exp_f32 v81, v81\n"
        "v_exp_f32 v82, v82\n"
        "v_exp_f32 v83, v83\n"
        "v_mfma_f32_32x32x8_bf16 v[96:111], v[134:135], v[122:123], v[96:111]\n"
        "s_add_u32 m0, s69, 3072\n"
        "s_add_u32 s56, s68, 0\n"
        "buffer_load_dwordx4 v169, s[48:51], s56 offen lds\n"
        "v_exp_f32 v84, v84\n"
        "v_exp_f32 v85, v85\n"
        "v_exp_f32 v86, v86\n"
        "v_exp_f32 v87, v87\n"
        "v_smfmac_f32_32x32x32_bf16 v[32:47], v[140:143], v[72:79], v152\n"
        "s_add_u32 m0, s69, 4096\n"
        "s_add_u32 s56, s68, 0\n"
        "buffer_load_dwordx4 v170, s[48:51], s56 offen lds\n"
        "v_exp_f32 v88, v88\n"
        "v_exp_f32 v89, v89\n"
        "v_exp_f32 v90, v90\n"
        "v_exp_f32 v91, v91\n"
        "s_waitcnt lgkmcnt(0)\n"
        "v_smfmac_f32_32x32x32_bf16 v[16:31], v[144:147], v[80:87], v152\n"
        "buffer_load_dword v153, v173, s[44:47], s75 offen offset:512\n"
        "v_mov_b32 v252, -1\n"
        "v_mov_b32 v253, -1\n"
        "v_mov_b32 v254, -1\n"
        "v_mov_b32 v255, -1\n"
        "s_add_u32 s56, s92, 0\n"
        "buffer_store_dwordx4 v[252:255], v162, s[52:55], s56 offen sc1\n"
        "v_exp_f32 v92, v92\n"
        "v_exp_f32 v93, v93\n"
        "v_exp_f32 v94, v94\n"
        "v_exp_f32 v95, v95\n"
        "v_mfma_f32_32x32x16_bf16 v[48:63], v[124:127], v[112:115], 0\n"
        "ds_read_b128 v[136:139], v160 offset:28480\n"
        "v_exp_f32 v96, v96\n"
        "v_exp_f32 v97, v97\n"
        "v_exp_f32 v98, v98\n"
        "v_exp_f32 v99, v99\n"
        "v_mfma_f32_32x32x8_bf16 v[48:63], v[128:129], v[116:117], v[48:63]\n"
        "ds_read_b128 v[140:143], v161 offset:28480\n"
        "v_exp_f32 v100, v100\n"
        "v_exp_f32 v101, v101\n"
        "v_exp_f32 v102, v102\n"
        "v_exp_f32 v103, v103\n"
        "v_smfmac_f32_32x32x32_bf16 v[16:31], v[148:151], v[88:95], v152\n"
        "ds_read_b128 v[130:133], v158 offset:31328\n"
        "v_exp_f32 v104, v104\n"
        "v_exp_f32 v105, v105\n"
        "v_exp_f32 v106, v106\n"
        "v_exp_f32 v107, v107\n"
        "v_smfmac_f32_32x32x32_bf16 v[32:47], v[144:147], v[96:103], v152\n"
        "ds_read_b64 v[134:135], v159 offset:31328\n"
        "v_exp_f32 v108, v108\n"
        "v_exp_f32 v109, v109\n"
        "v_exp_f32 v110, v110\n"
        "v_exp_f32 v111, v111\n"
        "v_mfma_f32_32x32x16_bf16 v[64:79], v[124:127], v[118:121], 0\n"
        "v_exp_f32 v48, v48\n"
        "v_exp_f32 v49, v49\n"
        "v_exp_f32 v50, v50\n"
        "v_exp_f32 v51, v51\n"
        "v_mfma_f32_32x32x8_bf16 v[64:79], v[128:129], v[122:123], v[64:79]\n"
        "v_exp_f32 v52, v52\n"
        "v_exp_f32 v53, v53\n"
        "v_exp_f32 v54, v54\n"
        "v_exp_f32 v55, v55\n"
        "v_smfmac_f32_32x32x32_bf16 v[32:47], v[148:151], v[104:111], v152\n"
        "v_exp_f32 v56, v56\n"
        "v_exp_f32 v57, v57\n"
        "v_exp_f32 v58, v58\n"
        "v_exp_f32 v59, v59\n"
        "s_waitcnt lgkmcnt(0)\n"
        "v_smfmac_f32_32x32x32_bf16 v[16:31], v[136:139], v[48:55], v152\n"
        "s_setprio 0\n"
        "v_exp_f32 v60, v60\n"
        "v_exp_f32 v61, v61\n"
        "v_exp_f32 v62, v62\n"
        "v_exp_f32 v63, v63\n"
        "v_mfma_f32_32x32x16_bf16 v[80:95], v[130:133], v[112:115], 0\n"
        "ds_read_b128 v[144:147], v160 offset:31328\n"
        "v_exp_f32 v64, v64\n"
        "v_exp_f32 v65, v65\n"
        "v_exp_f32 v66, v66\n"
        "v_exp_f32 v67, v67\n"
        "v_mfma_f32_32x32x8_bf16 v[80:95], v[134:135], v[116:117], v[80:95]\n"
        "ds_read_b128 v[148:151], v161 offset:31328\n"
        "v_exp_f32 v68, v68\n"
        "v_exp_f32 v69, v69\n"
        "v_exp_f32 v70, v70\n"
        "v_exp_f32 v71, v71\n"
        "v_smfmac_f32_32x32x32_bf16 v[16:31], v[140:143], v[56:63], v152\n"
        "v_exp_f32 v72, v72\n"
        "v_exp_f32 v73, v73\n"
        "v_exp_f32 v74, v74\n"
        "v_exp_f32 v75, v75\n"
        "v_smfmac_f32_32x32x32_bf16 v[32:47], v[136:139], v[64:71], v152\n"
        "s_waitcnt vmcnt(11) lgkmcnt(0)\n"
        "s_barrier\n"
        "s_setprio 2\n"
        "ds_read_b128 v[124:127], v158\n"
        "ds_read_b64 v[128:129], v159\n"
        "s_cmp_lg_u32 s78, 0\n"
        "s_cbranch_scc1 13f\n"
        "v_lshlrev_b32 v184, 3, v162\n"
        "v_add_u32 v185, 8192, v184\n"
        "v_add_u32 v186, 16384, v184\n"
        "v_add_u32 v187, 24576, v184\n"
        "buffer_load_dwordx2 v[176:177], v184, s[52:55], 0 offen offset:128 sc1\n"
        "buffer_load_dwordx2 v[178:179], v185, s[52:55], 0 offen offset:128 sc1\n"
        "buffer_load_dwordx2 v[180:181], v186, s[52:55], 0 offen offset:128 sc1\n"
        "buffer_load_dwordx2 v[182:183], v187, s[52:55], 0 offen offset:128 sc1\n"
        "13:\n"
        "v_exp_f32 v76, v76\n"
        "v_exp_f32 v77, v77\n"
        "v_exp_f32 v78, v78\n"
        "v_exp_f32 v79, v79\n"
        "v_mfma_f32_32x32x16_bf16 v[96:111], v[130:133], v[118:121], 0\n"
        "s_add_u32 m0, s71, 22784\n"
        "s_add_u32 s56, s70, 1966080\n"
        "s_add_u32 s57, s56, s83\n"
        "buffer_load_dwordx4 v162, s[52:55], s57 offen sc1 lds\n"
        "v_exp_f32 v80, v80\n"
        "v_exp_f32 v81, v81\n"
        "v_exp_f32 v82, v82\n"
        "v_exp_f32 v83, v83\n"
        "v_mfma_f32_32x32x8_bf16 v[96:111], v[134:135], v[122:123], v[96:111]\n"
        "buffer_load_dwordx4 v175, s[52:55], s56 offen offset:1024 sc1 lds\n"
        "v_exp_f32 v84, v84\n"
        "v_exp_f32 v85, v85\n"
        "v_exp_f32 v86, v86\n"
        "v_exp_f32 v87, v87\n"
        "v_smfmac_f32_32x32x32_bf16 v[32:47], v[140:143], v[72:79], v152\n"
        "s_mov_b32 exec_hi, 0xffff\n"
        "buffer_load_dwordx4 v163, s[52:55], s56 offen offset:2048 sc1 lds\n"
        "s_mov_b32 exec_hi, -1\n"
        "v_exp_f32 v88, v88\n"
        "v_exp_f32 v89, v89\n"
        "v_exp_f32 v90, v90\n"
        "v_exp_f32 v91, v91\n"
        "s_waitcnt lgkmcnt(0)\n"
        "v_smfmac_f32_32x32x32_bf16 v[16:31], v[144:147], v[80:87], v152\n"
        "s_add_u32 m0, s71, 25632\n"
        "s_add_u32 s56, s56, 81920\n"
        "s_add_u32 s57, s56, s83\n"
        "buffer_load_dwordx4 v162, s[52:55], s57 offen sc1 lds\n"
        "v_exp_f32 v92, v92\n"
        "v_exp_f32 v93, v93\n"
        "v_exp_f32 v94, v94\n"
        "v_exp_f32 v95, v95\n"
        "v_mfma_f32_32x32x16_bf16 v[48:63], v[124:127], v[112:115], 0\n"
        "ds_read_b128 v[136:139], v160\n"
        "buffer_load_dwordx4 v175, s[52:55], s56 offen offset:1024 sc1 lds\n"
        "v_exp_f32 v96, v96\n"
        "v_exp_f32 v97, v97\n"
        "v_exp_f32 v98, v98\n"
        "v_exp_f32 v99, v99\n"
        "v_mfma_f32_32x32x8_bf16 v[48:63], v[128:129], v[116:117], v[48:63]\n"
        "ds_read_b128 v[140:143], v161\n"
        "s_mov_b32 exec_hi, 0xffff\n"
        "buffer_load_dwordx4 v163, s[52:55], s56 offen offset:2048 sc1 lds\n"
        "s_mov_b32 exec_hi, -1\n"
        "v_exp_f32 v100, v100\n"
        "v_exp_f32 v101, v101\n"
        "v_exp_f32 v102, v102\n"
        "v_exp_f32 v103, v103\n"
        "v_smfmac_f32_32x32x32_bf16 v[16:31], v[148:151], v[88:95], v152\n"
        "ds_read_b128 v[130:133], v158 offset:2848\n"
        "s_mov_b64 exec, 0xffff\n"
        "buffer_load_dwordx2 v[154:155], v9, s[52:55], 0 offen sc1\n"
        "s_mov_b64 exec, -1\n"
        "v_exp_f32 v104, v104\n"
        "v_exp_f32 v105, v105\n"
        "v_exp_f32 v106, v106\n"
        "v_exp_f32 v107, v107\n"
        "v_smfmac_f32_32x32x32_bf16 v[32:47], v[144:147], v[96:103], v152\n"
        "ds_read_b64 v[134:135], v159 offset:2848\n"
        "s_add_u32 m0, s69, 5120\n"
        "s_add_u32 s56, s68, 40960\n"
        "buffer_load_dwordx4 v166, s[48:51], s56 offen lds\n"
        "v_exp_f32 v108, v108\n"
        "v_exp_f32 v109, v109\n"
        "v_exp_f32 v110, v110\n"
        "v_exp_f32 v111, v111\n"
        "v_mfma_f32_32x32x16_bf16 v[64:79], v[124:127], v[118:121], 0\n"
        "s_add_u32 m0, s69, 6144\n"
        "s_add_u32 s56, s68, 40960\n"
        "buffer_load_dwordx4 v167, s[48:51], s56 offen lds\n"
        "v_exp_f32 v48, v48\n"
        "v_exp_f32 v49, v49\n"
        "v_exp_f32 v50, v50\n"
        "v_exp_f32 v51, v51\n"
        "v_mfma_f32_32x32x8_bf16 v[64:79], v[128:129], v[122:123], v[64:79]\n"
        "s_add_u32 m0, s69, 7168\n"
        "s_add_u32 s56, s68, 40960\n"
        "buffer_load_dwordx4 v168, s[48:51], s56 offen lds\n"
        "v_exp_f32 v52, v52\n"
        "v_exp_f32 v53, v53\n"
        "v_exp_f32 v54, v54\n"
        "v_exp_f32 v55, v55\n"
        "v_smfmac_f32_32x32x32_bf16 v[32:47], v[148:151], v[104:111], v152\n"
        "s_add_u32 m0, s69, 8192\n"
        "s_add_u32 s56, s68, 40960\n"
        "buffer_load_dwordx4 v169, s[48:51], s56 offen lds\n"
        "v_exp_f32 v56, v56\n"
        "v_exp_f32 v57, v57\n"
        "v_exp_f32 v58, v58\n"
        "v_exp_f32 v59, v59\n"
        "s_waitcnt lgkmcnt(0)\n"
        "v_smfmac_f32_32x32x32_bf16 v[16:31], v[136:139], v[48:55], v152\n"
        "s_add_u32 m0, s69, 9216\n"
        "s_add_u32 s56, s68, 40960\n"
        "buffer_load_dwordx4 v170, s[48:51], s56 offen lds\n"
        "v_exp_f32 v60, v60\n"
        "v_exp_f32 v61, v61\n"
        "v_exp_f32 v62, v62\n"
        "v_exp_f32 v63, v63\n"
        "v_mfma_f32_32x32x16_bf16 v[80:95], v[130:133], v[112:115], 0\n"
        "ds_read_b128 v[144:147], v160 offset:2848\n"
        "s_add_u32 m0, s69, 10240\n"
        "s_add_u32 s56, s68, 81920\n"
        "buffer_load_dwordx4 v166, s[48:51], s56 offen lds\n"
        "v_exp_f32 v64, v64\n"
        "v_exp_f32 v65, v65\n"
        "v_exp_f32 v66, v66\n"
        "v_exp_f32 v67, v67\n"
        "v_mfma_f32_32x32x8_bf16 v[80:95], v[134:135], v[116:117], v[80:95]\n"
        "ds_read_b128 v[148:151], v161 offset:2848\n"
        "s_add_u32 m0, s69, 11264\n"
        "s_add_u32 s56, s68, 81920\n"
        "buffer_load_dwordx4 v167, s[48:51], s56 offen lds\n"
        "v_exp_f32 v68, v68\n"
        "v_exp_f32 v69, v69\n"
        "v_exp_f32 v70, v70\n"
        "v_exp_f32 v71, v71\n"
        "v_smfmac_f32_32x32x32_bf16 v[16:31], v[140:143], v[56:63], v152\n"
        "ds_read_b128 v[124:127], v158 offset:5696\n"
        "s_add_u32 m0, s69, 12288\n"
        "s_add_u32 s56, s68, 81920\n"
        "buffer_load_dwordx4 v168, s[48:51], s56 offen lds\n"
        "v_exp_f32 v72, v72\n"
        "v_exp_f32 v73, v73\n"
        "v_exp_f32 v74, v74\n"
        "v_exp_f32 v75, v75\n"
        "v_smfmac_f32_32x32x32_bf16 v[32:47], v[136:139], v[64:71], v152\n"
        "ds_read_b64 v[128:129], v159 offset:5696\n"
        "s_add_u32 m0, s69, 13312\n"
        "s_add_u32 s56, s68, 81920\n"
        "buffer_load_dwordx4 v169, s[48:51], s56 offen lds\n"
        "v_exp_f32 v76, v76\n"
        "v_exp_f32 v77, v77\n"
        "v_exp_f32 v78, v78\n"
        "v_exp_f32 v79, v79\n"
        "v_mfma_f32_32x32x16_bf16 v[96:111], v[130:133], v[118:121], 0\n"
        "s_add_u32 m0, s69, 14336\n"
        "s_add_u32 s56, s68, 81920\n"
        "buffer_load_dwordx4 v170, s[48:51], s56 offen lds\n"
        "v_exp_f32 v80, v80\n"
        "v_exp_f32 v81, v81\n"
        "v_exp_f32 v82, v82\n"
        "v_exp_f32 v83, v83\n"
        "v_mfma_f32_32x32x8_bf16 v[96:111], v[134:135], v[122:123], v[96:111]\n"
        "buffer_load_dword v153, v173, s[44:47], s75 offen offset:768\n"
        "v_exp_f32 v84, v84\n"
        "v_exp_f32 v85, v85\n"
        "v_exp_f32 v86, v86\n"
        "v_exp_f32 v87, v87\n"
        "v_smfmac_f32_32x32x32_bf16 v[32:47], v[140:143], v[72:79], v152\n"
        "s_add_u32 s56, s92, 1024\n"
        "buffer_store_dwordx4 v[252:255], v162, s[52:55], s56 offen sc1\n"
        "v_exp_f32 v88, v88\n"
        "v_exp_f32 v89, v89\n"
        "v_exp_f32 v90, v90\n"
        "v_exp_f32 v91, v91\n"
        "s_waitcnt lgkmcnt(0)\n"
        "v_smfmac_f32_32x32x32_bf16 v[16:31], v[144:147], v[80:87], v152\n"
        "s_add_u32 s56, s92, 2048\n"
        "buffer_store_dwordx4 v[252:255], v162, s[52:55], s56 offen sc1\n"
        "v_exp_f32 v92, v92\n"
        "v_exp_f32 v93, v93\n"
        "v_exp_f32 v94, v94\n"
        "v_exp_f32 v95, v95\n"
        "v_mfma_f32_32x32x16_bf16 v[48:63], v[124:127], v[112:115], 0\n"
        "ds_read_b128 v[136:139], v160 offset:5696\n"
        "v_exp_f32 v96, v96\n"
        "v_exp_f32 v97, v97\n"
        "v_exp_f32 v98, v98\n"
        "v_exp_f32 v99, v99\n"
        "v_mfma_f32_32x32x8_bf16 v[48:63], v[128:129], v[116:117], v[48:63]\n"
        "ds_read_b128 v[140:143], v161 offset:5696\n"
        "v_exp_f32 v100, v100\n"
        "v_exp_f32 v101, v101\n"
        "v_exp_f32 v102, v102\n"
        "v_exp_f32 v103, v103\n"
        "v_smfmac_f32_32x32x32_bf16 v[16:31], v[148:151], v[88:95], v152\n"
        "ds_read_b128 v[130:133], v158 offset:8544\n"
        "v_exp_f32 v104, v104\n"
        "v_exp_f32 v105, v105\n"
        "v_exp_f32 v106, v106\n"
        "v_exp_f32 v107, v107\n"
        "v_smfmac_f32_32x32x32_bf16 v[32:47], v[144:147], v[96:103], v152\n"
        "ds_read_b64 v[134:135], v159 offset:8544\n"
        "v_exp_f32 v108, v108\n"
        "v_exp_f32 v109, v109\n"
        "v_exp_f32 v110, v110\n"
        "v_exp_f32 v111, v111\n"
        "v_mfma_f32_32x32x16_bf16 v[64:79], v[124:127], v[118:121], 0\n"
        "v_exp_f32 v48, v48\n"
        "v_exp_f32 v49, v49\n"
        "v_exp_f32 v50, v50\n"
        "v_exp_f32 v51, v51\n"
        "v_mfma_f32_32x32x8_bf16 v[64:79], v[128:129], v[122:123], v[64:79]\n"
        "v_exp_f32 v52, v52\n"
        "v_exp_f32 v53, v53\n"
        "v_exp_f32 v54, v54\n"
        "v_exp_f32 v55, v55\n"
        "v_smfmac_f32_32x32x32_bf16 v[32:47], v[148:151], v[104:111], v152\n"
        "v_exp_f32 v56, v56\n"
        "v_exp_f32 v57, v57\n"
        "v_exp_f32 v58, v58\n"
        "v_exp_f32 v59, v59\n"
        "s_waitcnt lgkmcnt(0)\n"
        "v_smfmac_f32_32x32x32_bf16 v[16:31], v[136:139], v[48:55], v152\n"
        "s_setprio 0\n"
        "v_exp_f32 v60, v60\n"
        "v_exp_f32 v61, v61\n"
        "v_exp_f32 v62, v62\n"
        "v_exp_f32 v63, v63\n"
        "v_mfma_f32_32x32x16_bf16 v[80:95], v[130:133], v[112:115], 0\n"
        "ds_read_b128 v[144:147], v160 offset:8544\n"
        "v_exp_f32 v64, v64\n"
        "v_exp_f32 v65, v65\n"
        "v_exp_f32 v66, v66\n"
        "v_exp_f32 v67, v67\n"
        "v_mfma_f32_32x32x8_bf16 v[80:95], v[134:135], v[116:117], v[80:95]\n"
        "ds_read_b128 v[148:151], v161 offset:8544\n"
        "v_exp_f32 v68, v68\n"
        "v_exp_f32 v69, v69\n"
        "v_exp_f32 v70, v70\n"
        "v_exp_f32 v71, v71\n"
        "v_smfmac_f32_32x32x32_bf16 v[16:31], v[140:143], v[56:63], v152\n"
        "v_exp_f32 v72, v72\n"
        "v_exp_f32 v73, v73\n"
        "v_exp_f32 v74, v74\n"
        "v_exp_f32 v75, v75\n"
        "v_smfmac_f32_32x32x32_bf16 v[32:47], v[136:139], v[64:71], v152\n"
        "s_waitcnt vmcnt(13) lgkmcnt(0)\n"
        "s_mov_b64 exec, 0xffff\n"
        "s_mov_b32 s58, 0\n"
        "17:\n"
        "v_cmp_eq_u64 vcc, s[60:61], v[154:155]\n"
        "s_nop 1\n"
        "s_cmp_eq_u64 vcc, exec\n"
        "s_cbranch_scc1 18f\n"
        "s_sleep 2\n"
        "buffer_load_dwordx2 v[154:155], v9, s[52:55], 0 offen sc1\n"
        "s_add_u32 s58, s58, 1\n"
        "s_cmp_lt_u32 s58, 262144\n"
        "s_waitcnt vmcnt(0)\n"
        "s_cbranch_scc1 17b\n"
        "18:\n"
        "s_mov_b64 exec, -1\n"
        "s_barrier\n"
        "s_setprio 2\n"
        "ds_read_b128 v[124:127], v158 offset:22784\n"
        "ds_read_b64 v[128:129], v159 offset:22784\n"
        "s_cmp_lg_u32 s78, 0\n"
        "s_cbranch_scc1 16f\n"
        "s_mov_b32 s58, 0\n"
        "14:\n"
        "v_cmp_eq_u64 vcc, s[60:61], v[176:177]\n"
        "v_cmp_eq_u64 s[62:63], s[60:61], v[178:179]\n"
        "s_nop 1\n"
        "s_and_b64 vcc, vcc, s[62:63]\n"
        "v_cmp_eq_u64 s[62:63], s[60:61], v[180:181]\n"
        "s_nop 1\n"
        "s_and_b64 vcc, vcc, s[62:63]\n"
        "v_cmp_eq_u64 s[62:63], s[60:61], v[182:183]\n"
        "s_nop 1\n"
        "s_and_b64 vcc, vcc, s[62:63]\n"
        "s_nop 1\n"
        "s_cmp_eq_u64 vcc, exec\n"
        "s_cbranch_scc1 15f\n"
        "s_sleep 2\n"
        "buffer_load_dwordx2 v[176:177], v184, s[52:55], 0 offen offset:128 sc1\n"
        "buffer_load_dwordx2 v[178:179], v185, s[52:55], 0 offen offset:128 sc1\n"
        "buffer_load_dwordx2 v[180:181], v186, s[52:55], 0 offen offset:128 sc1\n"
        "buffer_load_dwordx2 v[182:183], v187, s[52:55], 0 offen offset:128 sc1\n"
        "s_add_u32 s58, s58, 1\n"
        "s_cmp_lt_u32 s58, 262144\n"
        "s_waitcnt vmcnt(0)\n"
        "s_cbranch_scc1 14b\n"
        "15:\n"
        "v_mov_b32 v176, s60\n"
        "v_mov_b32 v177, s61\n"
        "s_mov_b64 exec, 1\n"
        "buffer_store_dwordx2 v[176:177], v162, s[52:55], s78 offen sc1\n"
        "s_mov_b64 exec, -1\n"
        "16:\n"
        "v_exp_f32 v76, v76\n"
        "v_exp_f32 v77, v77\n"
        "v_exp_f32 v78, v78\n"
        "v_exp_f32 v79, v79\n"
        "v_mfma_f32_32x32x16_bf16 v[96:111], v[130:133], v[118:121], 0\n"
        "v_mov_b32 v154, s60\n"
        "v_mov_b32 v155, s61\n"
        "s_mov_b64 exec, 1\n"
        "buffer_store_dwordx2 v[154:155], v162, s[52:55], s72 offen sc1\n"
        "s_mov_b64 exec, -1\n"
        "v_exp_f32 v80, v80\n"
        "v_exp_f32 v81, v81\n"
        "v_exp_f32 v82, v82\n"
        "v_exp_f32 v83, v83\n"
        "v_mfma_f32_32x32x8_bf16 v[96:111], v[134:135], v[122:123], v[96:111]\n"
        "s_add_u32 m0, s71, 0\n"
        "s_add_u32 s56, s70, 2621440\n"
        "s_add_u32 s57, s56, s83\n"
        "buffer_load_dwordx4 v162, s[52:55], s57 offen sc1 lds\n"
        "v_exp_f32 v84, v84\n"
        "v_exp_f32 v85, v85\n"
        "v_exp_f32 v86, v86\n"
        "v_exp_f32 v87, v87\n"
        "v_smfmac_f32_32x32x32_bf16 v[32:47], v[140:143], v[72:79], v152\n"
        "buffer_load_dwordx4 v175, s[52:55], s56 offen offset:1024 sc1 lds\n"
        "v_exp_f32 v88, v88\n"
        "v_exp_f32 v89, v89\n"
        "v_exp_f32 v90, v90\n"
        "v_exp_f32 v91, v91\n"
        "s_waitcnt lgkmcnt(0)\n"
        "v_smfmac_f32_32x32x32_bf16 v[16:31], v[144:147], v[80:87], v152\n"
        "s_mov_b32 exec_hi, 0xffff\n"
        "buffer_load_dwordx4 v163, s[52:55], s56 offen offset:2048 sc1 lds\n"
        "s_mov_b32 exec_hi, -1\n"
        "v_exp_f32 v92, v92\n"
        "v_exp_f32 v93, v93\n"
        "v_exp_f32 v94, v94\n"
        "v_exp_f32 v95, v95\n"
        "v_mfma_f32_32x32x16_bf16 v[48:63], v[124:127], v[112:115], 0\n"
        "ds_read_b128 v[136:139], v160 offset:22784\n"
        "s_add_u32 m0, s71, 2848\n"
        "s_nop 0\n"
        ".Lk_pad3:\n"
        "s_cbranch_execz .Lk_ret\n"
        "s_add_u32 s56, s56, 81920\n"
        "s_add_u32 s57, s56, s83\n"
        "buffer_load_dwordx4 v162, s[52:55], s57 offen sc1 lds\n"
        "v_exp_f32 v96, v96\n"
        "v_exp_f32 v97, v97\n"
        "v_exp_f32 v98, v98\n"
        "v_exp_f32 v99, v99\n"
        "v_mfma_f32_32x32x8_bf16 v[48:63], v[128:129], v[116:117], v[48:63]\n"
        "ds_read_b128 v[140:143], v161 offset:22784\n"
        "buffer_load_dwordx4 v175, s[52:55], s56 offen offset:1024 sc1 lds\n"
        "v_exp_f32 v100, v100\n"
        "v_exp_f32 v101, v101\n"
        "v_exp_f32 v102, v102\n"
        "v_exp_f32 v103, v103\n"
        "v_smfmac_f32_32x32x32_bf16 v[16:31], v[148:151], v[88:95], v152\n"
        "ds_read_b128 v[130:133], v158 offset:25632\n"
        "s_mov_b32 exec_hi, 0xffff\n"
        "buffer_load_dwordx4 v163, s[52:55], s56 offen offset:2048 sc1 lds\n"
        "s_mov_b32 exec_hi, -1\n"
        "v_exp_f32 v104, v104\n"
        "v_exp_f32 v105, v105\n"
        "v_exp_f32 v106, v106\n"
        "v_exp_f32 v107, v107\n"
        "v_smfmac_f32_32x32x32_bf16 v[32:47], v[144:147], v[96:103], v152\n"
        "ds_read_b64 v[134:135], v159 offset:25632\n"
        "s_add_u32 m0, s69, 15360\n"
        "s_add_u32 s56, s68, 122880\n"
        "buffer_load_dwordx4 v166, s[48:51], s56 offen lds\n"
        "v_exp_f32 v108, v108\n"
        "v_exp_f32 v109, v109\n"
        "v_exp_f32 v110, v110\n"
        "v_exp_f32 v111, v111\n"
        "v_mfma_f32_32x32x16_bf16 v[64:79], v[124:127], v[118:121], 0\n"
        "s_add_u32 m0, s69, 16384\n"
        "s_add_u32 s56, s68, 122880\n"
        "buffer_load_dwordx4 v167, s[48:51], s56 offen lds\n"
        "v_exp_f32 v48, v48\n"
        "v_exp_f32 v49, v49\n"
        "v_exp_f32 v50, v50\n"
        "v_exp_f32 v51, v51\n"
        "v_mfma_f32_32x32x8_bf16 v[64:79], v[128:129], v[122:123], v[64:79]\n"
        "s_add_u32 m0, s69, 17408\n"
        "s_add_u32 s56, s68, 122880\n"
        "buffer_load_dwordx4 v168, s[48:51], s56 offen lds\n"
        "v_exp_f32 v52, v52\n"
        "v_exp_f32 v53, v53\n"
        "v_exp_f32 v54, v54\n"
        "v_exp_f32 v55, v55\n"
        "v_smfmac_f32_32x32x32_bf16 v[32:47], v[148:151], v[104:111], v152\n"
        "s_add_u32 m0, s69, 18432\n"
        "s_add_u32 s56, s68, 122880\n"
        "buffer_load_dwordx4 v169, s[48:51], s56 offen lds\n"
        "v_exp_f32 v56, v56\n"
        "v_exp_f32 v57, v57\n"
        "v_exp_f32 v58, v58\n"
        "v_exp_f32 v59, v59\n"
        "s_waitcnt lgkmcnt(0)\n"
        "v_smfmac_f32_32x32x32_bf16 v[16:31], v[136:139], v[48:55], v152\n"
        "s_add_u32 m0, s69, 19456\n"
        "s_add_u32 s56, s68, 122880\n"
        "buffer_load_dwordx4 v170, s[48:51], s56 offen lds\n"
        "v_exp_f32 v60, v60\n"
        "v_exp_f32 v61, v61\n"
        "v_exp_f32 v62, v62\n"
        "v_exp_f32 v63, v63\n"
        "v_mfma_f32_32x32x16_bf16 v[80:95], v[130:133], v[112:115], 0\n"
        "ds_read_b128 v[144:147], v160 offset:25632\n"
        "s_add_u32 m0, s69, 20480\n"
        "s_add_u32 s56, s68, 163840\n"
        "buffer_load_dwordx4 v166, s[48:51], s56 offen lds\n"
        "v_exp_f32 v64, v64\n"
        "v_exp_f32 v65, v65\n"
        "v_exp_f32 v66, v66\n"
        "v_exp_f32 v67, v67\n"
        "v_mfma_f32_32x32x8_bf16 v[80:95], v[134:135], v[116:117], v[80:95]\n"
        "ds_read_b128 v[148:151], v161 offset:25632\n"
        "s_add_u32 m0, s69, 21504\n"
        "s_add_u32 s56, s68, 163840\n"
        "buffer_load_dwordx4 v167, s[48:51], s56 offen lds\n"
        "v_exp_f32 v68, v68\n"
        "v_exp_f32 v69, v69\n"
        "v_exp_f32 v70, v70\n"
        "v_exp_f32 v71, v71\n"
        "v_smfmac_f32_32x32x32_bf16 v[16:31], v[140:143], v[56:63], v152\n"
        "ds_read_b128 v[124:127], v158 offset:28480\n"
        "s_add_u32 m0, s69, 22528\n"
        "s_add_u32 s56, s68, 163840\n"
        "buffer_load_dwordx4 v168, s[48:51], s56 offen lds\n"
        "v_exp_f32 v72, v72\n"
        "v_exp_f32 v73, v73\n"
        "v_exp_f32 v74, v74\n"
        "v_exp_f32 v75, v75\n"
        "v_smfmac_f32_32x32x32_bf16 v[32:47], v[136:139], v[64:71], v152\n"
        "ds_read_b64 v[128:129], v159 offset:28480\n"
        "s_add_u32 m0, s69, 23552\n"
        "s_add_u32 s56, s68, 163840\n"
        "buffer_load_dwordx4 v169, s[48:51], s56 offen lds\n"
        "v_exp_f32 v76, v76\n"
        "v_exp_f32 v77, v77\n"
        "v_exp_f32 v78, v78\n"
        "v_exp_f32 v79, v79\n"
        "v_mfma_f32_32x32x16_bf16 v[96:111], v[130:133], v[118:121], 0\n"
        "s_add_u32 m0, s69, 24576\n"
        "s_add_u32 s56, s68, 163840\n"
        "buffer_load_dwordx4 v170, s[48:51], s56 offen lds\n"
        "v_exp_f32 v80, v80\n"
        "v_exp_f32 v81, v81\n"
        "v_exp_f32 v82, v82\n"
        "v_exp_f32 v83, v83\n"
        "v_mfma_f32_32x32x8_bf16 v[96:111], v[134:135], v[122:123], v[96:111]\n"
        "buffer_load_dword v153, v173, s[44:47], s75 offen offset:1024\n"
        "v_exp_f32 v84, v84\n"
        "v_exp_f32 v85, v85\n"
        "v_exp_f32 v86, v86\n"
        "v_exp_f32 v87, v87\n"
        "v_smfmac_f32_32x32x32_bf16 v[32:47], v[140:143], v[72:79], v152\n"
        "s_add_u32 s56, s92, 3072\n"
        "buffer_store_dwordx4 v[252:255], v162, s[52:55], s56 offen sc1\n"
        "v_exp_f32 v88, v88\n"
        "v_exp_f32 v89, v89\n"
        "v_exp_f32 v90, v90\n"
        "v_exp_f32 v91, v91\n"
        "s_waitcnt lgkmcnt(0)\n"
        "v_smfmac_f32_32x32x32_bf16 v[16:31], v[144:147], v[80:87], v152\n"
        "s_add_u32 s56, s92, 4096\n"
        "buffer_store_dwordx4 v[252:255], v162, s[52:55], s56 offen sc1\n"
        "v_exp_f32 v92, v92\n"
        "v_exp_f32 v93, v93\n"
        "v_exp_f32 v94, v94\n"
        "v_exp_f32 v95, v95\n"
        "v_mfma_f32_32x32x16_bf16 v[48:63], v[124:127], v[112:115], 0\n"
        "ds_read_b128 v[136:139], v160 offset:28480\n"
        "v_exp_f32 v96, v96\n"
        "v_exp_f32 v97, v97\n"
        "v_exp_f32 v98, v98\n"
        "v_exp_f32 v99, v99\n"
        "v_mfma_f32_32x32x8_bf16 v[48:63], v[128:129], v[116:117], v[48:63]\n"
        "ds_read_b128 v[140:143], v161 offset:28480\n"
        "v_exp_f32 v100, v100\n"
        "v_exp_f32 v101, v101\n"
        "v_exp_f32 v102, v102\n"
        "v_exp_f32 v103, v103\n"
        "v_smfmac_f32_32x32x32_bf16 v[16:31], v[148:151], v[88:95], v152\n"
        "ds_read_b128 v[130:133], v158 offset:31328\n"
        "v_exp_f32 v104, v104\n"
        "v_exp_f32 v105, v105\n"
        "v_exp_f32 v106, v106\n"
        "v_exp_f32 v107, v107\n"
        "v_smfmac_f32_32x32x32_bf16 v[32:47], v[144:147], v[96:103], v152\n"
        "ds_read_b64 v[134:135], v159 offset:31328\n"
        "v_exp_f32 v108, v108\n"
        "v_exp_f32 v109, v109\n"
        "v_exp_f32 v110, v110\n"
        "v_exp_f32 v111, v111\n"
        "v_mfma_f32_32x32x16_bf16 v[64:79], v[124:127], v[118:121], 0\n"
        "v_exp_f32 v48, v48\n"
        "v_exp_f32 v49, v49\n"
        "v_exp_f32 v50, v50\n"
        "v_exp_f32 v51, v51\n"
        "v_mfma_f32_32x32x8_bf16 v[64:79], v[128:129], v[122:123], v[64:79]\n"
        "v_exp_f32 v52, v52\n"
        "v_exp_f32 v53, v53\n"
        "v_exp_f32 v54, v54\n"
        "v_exp_f32 v55, v55\n"
        "v_smfmac_f32_32x32x32_bf16 v[32:47], v[148:151], v[104:111], v152\n"
        "v_exp_f32 v56, v56\n"
        "v_exp_f32 v57, v57\n"
        "v_exp_f32 v58, v58\n"
        "v_exp_f32 v59, v59\n"
        "s_waitcnt lgkmcnt(0)\n"
        "v_smfmac_f32_32x32x32_bf16 v[16:31], v[136:139], v[48:55], v152\n"
        "s_setprio 0\n"
        "v_exp_f32 v60, v60\n"
        "v_exp_f32 v61, v61\n"
        "v_exp_f32 v62, v62\n"
        "v_exp_f32 v63, v63\n"
        "v_mfma_f32_32x32x16_bf16 v[80:95], v[130:133], v[112:115], 0\n"
        "ds_read_b128 v[144:147], v160 offset:31328\n"
        "v_exp_f32 v64, v64\n"
        "v_exp_f32 v65, v65\n"
        "v_exp_f32 v66, v66\n"
        "v_exp_f32 v67, v67\n"
        "v_mfma_f32_32x32x8_bf16 v[80:95], v[134:135], v[116:117], v[80:95]\n"
        "ds_read_b128 v[148:151], v161 offset:31328\n"
        "v_exp_f32 v68, v68\n"
        "v_exp_f32 v69, v69\n"
        "v_exp_f32 v70, v70\n"
        "v_exp_f32 v71, v71\n"
        "v_smfmac_f32_32x32x32_bf16 v[16:31], v[140:143], v[56:63], v152\n"
        "v_exp_f32 v72, v72\n"
        "v_exp_f32 v73, v73\n"
        "v_exp_f32 v74, v74\n"
        "v_exp_f32 v75, v75\n"
        "v_smfmac_f32_32x32x32_bf16 v[32:47], v[136:139], v[64:71], v152\n"
        "s_waitcnt vmcnt(3) lgkmcnt(0)\n"
        "s_barrier\n"
        "s_setprio 2\n"
        "ds_read_b128 v[124:127], v158\n"
        "ds_read_b64 v[128:129], v159\n"
        "s_add_u32 m0, s71, 22784\n"
        "s_add_u32 s56, s70, 3276800\n"
        "s_add_u32 s57, s56, s83\n"
        "buffer_load_dwordx4 v162, s[52:55], s57 offen sc1 lds\n"
        "v_exp_f32 v76, v76\n"
        "v_exp_f32 v77, v77\n"
        "v_exp_f32 v78, v78\n"
        "v_exp_f32 v79, v79\n"
        "v_mfma_f32_32x32x16_bf16 v[96:111], v[130:133], v[118:121], 0\n"
        "buffer_load_dwordx4 v175, s[52:55], s56 offen offset:1024 sc1 lds\n"
        "v_exp_f32 v80, v80\n"
        "v_exp_f32 v81, v81\n"
        "v_exp_f32 v82, v82\n"
        "v_exp_f32 v83, v83\n"
        "v_mfma_f32_32x32x8_bf16 v[96:111], v[134:135], v[122:123], v[96:111]\n"
        "v_exp_f32 v84, v84\n"
        "v_exp_f32 v85, v85\n"
        "v_exp_f32 v86, v86\n"
        "v_exp_f32 v87, v87\n"
        "v_smfmac_f32_32x32x32_bf16 v[32:47], v[140:143], v[72:79], v152\n"
        "s_mov_b32 exec_hi, 0xffff\n"
        "buffer_load_dwordx4 v163, s[52:55], s56 offen offset:2048 sc1 lds\n"
        "s_mov_b32 exec_hi, -1\n"
        "v_exp_f32 v88, v88\n"
        "v_exp_f32 v89, v89\n"
        "v_exp_f32 v90, v90\n"
        "v_exp_f32 v91, v91\n"
        "s_waitcnt lgkmcnt(0)\n"
        "v_smfmac_f32_32x32x32_bf16 v[16:31], v[144:147], v[80:87], v152\n"
        "ds_read_b64_tr_b16 v[176:177], v171\n"
        "ds_read_b64_tr_b16 v[178:179], v171 offset:1280\n"
        "ds_read_b64_tr_b16 v[180:181], v171 offset:32\n"
        "ds_read_b64_tr_b16 v[182:183], v171 offset:1312\n"
        "v_exp_f32 v92, v92\n"
        "v_exp_f32 v93, v93\n"
        "v_exp_f32 v94, v94\n"
        "v_exp_f32 v95, v95\n"
        "v_mfma_f32_32x32x16_bf16 v[48:63], v[124:127], v[112:115], 0\n"
        "ds_read_b128 v[136:139], v160\n"
        "s_add_u32 m0, s71, 25632\n"
        "s_add_u32 s56, s56, 81920\n"
        "s_add_u32 s57, s56, s83\n"
        "buffer_load_dwordx4 v162, s[52:55], s57 offen sc1 lds\n"
        "ds_read_b64_tr_b16 v[184:185], v171 offset:64\n"
        "ds_read_b64_tr_b16 v[186:187], v171 offset:1344\n"
        "ds_read_b64_tr_b16 v[188:189], v171 offset:96\n"
        "ds_read_b64_tr_b16 v[190:191], v171 offset:1376\n"
        "v_exp_f32 v96, v96\n"
        "v_exp_f32 v97, v97\n"
        "v_exp_f32 v98, v98\n"
        "v_exp_f32 v99, v99\n"
        "v_mfma_f32_32x32x8_bf16 v[48:63], v[128:129], v[116:117], v[48:63]\n"
        "ds_read_b128 v[140:143], v161\n"
        "buffer_load_dwordx4 v175, s[52:55], s56 offen offset:1024 sc1 lds\n"
        "ds_read_b64_tr_b16 v[192:193], v171 offset:128\n"
        "ds_read_b64_tr_b16 v[194:195], v171 offset:1408\n"
        "ds_read_b64_tr_b16 v[196:197], v171 offset:6400\n"
        "ds_read_b64_tr_b16 v[198:199], v171 offset:7680\n"
        "v_exp_f32 v100, v100\n"
        "v_exp_f32 v101, v101\n"
        "v_exp_f32 v102, v102\n"
        "v_exp_f32 v103, v103\n"
        "v_smfmac_f32_32x32x32_bf16 v[16:31], v[148:151], v[88:95], v152\n"
        "ds_read_b128 v[130:133], v158 offset:2848\n"
        "v_exp_f32 v104, v104\n"
        "v_exp_f32 v105, v105\n"
        "v_exp_f32 v106, v106\n"
        "v_exp_f32 v107, v107\n"
        "v_smfmac_f32_32x32x32_bf16 v[32:47], v[144:147], v[96:103], v152\n"
        "ds_read_b64 v[134:135], v159 offset:2848\n"
        "s_mov_b32 exec_hi, 0xffff\n"
        "buffer_load_dwordx4 v163, s[52:55], s56 offen offset:2048 sc1 lds\n"
        "s_mov_b32 exec_hi, -1\n"
        "v_exp_f32 v108, v108\n"
        "v_exp_f32 v109, v109\n"
        "v_exp_f32 v110, v110\n"
        "v_exp_f32 v111, v111\n"
        "v_mfma_f32_32x32x16_bf16 v[64:79], v[124:127], v[118:121], 0\n"
        "v_exp_f32 v48, v48\n"
        "v_exp_f32 v49, v49\n"
        "v_exp_f32 v50, v50\n"
        "v_exp_f32 v51, v51\n"
        "v_mfma_f32_32x32x8_bf16 v[64:79], v[128:129], v[122:123], v[64:79]\n"
        "s_mov_b64 exec, 0xffff\n"
        "buffer_load_dwordx2 v[154:155], v164, s[52:55], 0 offen sc1\n"
        "s_mov_b64 exec, -1\n"
        "v_exp_f32 v52, v52\n"
        "v_exp_f32 v53, v53\n"
        "v_exp_f32 v54, v54\n"
        "v_exp_f32 v55, v55\n"
        "v_smfmac_f32_32x32x32_bf16 v[32:47], v[148:151], v[104:111], v152\n"
        "s_add_u32 s56, s92, 5120\n"
        "buffer_store_dwordx4 v[252:255], v162, s[52:55], s56 offen sc1\n"
        "v_exp_f32 v56, v56\n"
        "v_exp_f32 v57, v57\n"
        "v_exp_f32 v58, v58\n"
        "v_exp_f32 v59, v59\n"
        "s_waitcnt lgkmcnt(0)\n"
        "v_smfmac_f32_32x32x32_bf16 v[16:31], v[136:139], v[48:55], v152\n"
        "ds_read_b64_tr_b16 v[200:201], v171 offset:6432\n"
        "ds_read_b64_tr_b16 v[202:203], v171 offset:7712\n"
        "ds_read_b64_tr_b16 v[204:205], v171 offset:6464\n"
        "ds_read_b64_tr_b16 v[206:207], v171 offset:7744\n"
        "v_exp_f32 v60, v60\n"
        "v_exp_f32 v61, v61\n"
        "v_exp_f32 v62, v62\n"
        "v_exp_f32 v63, v63\n"
        "v_mfma_f32_32x32x16_bf16 v[80:95], v[130:133], v[112:115], 0\n"
        "ds_read_b128 v[144:147], v160 offset:2848\n"
        "s_add_u32 s56, s92, 6144\n"
        "buffer_store_dwordx4 v[252:255], v162, s[52:55], s56 offen sc1\n"
        "ds_read_b64_tr_b16 v[208:209], v171 offset:6496\n"
        "ds_read_b64_tr_b16 v[210:211], v171 offset:7776\n"
        "ds_read_b64_tr_b16 v[212:213], v171 offset:6528\n"
        "ds_read_b64_tr_b16 v[214:215], v171 offset:7808\n"
        "v_exp_f32 v64, v64\n"
        "v_exp_f32 v65, v65\n"
        "v_exp_f32 v66, v66\n"
        "v_exp_f32 v67, v67\n"
        "v_mfma_f32_32x32x8_bf16 v[80:95], v[134:135], v[116:117], v[80:95]\n"
        "ds_read_b128 v[148:151], v161 offset:2848\n"
        "ds_read_b64_tr_b16 v[216:217], v171 offset:12800\n"
        "ds_read_b64_tr_b16 v[218:219], v171 offset:14080\n"
        "ds_read_b64_tr_b16 v[220:221], v171 offset:12832\n"
        "ds_read_b64_tr_b16 v[222:223], v171 offset:14112\n"
        "v_exp_f32 v68, v68\n"
        "v_exp_f32 v69, v69\n"
        "v_exp_f32 v70, v70\n"
        "v_exp_f32 v71, v71\n"
        "v_smfmac_f32_32x32x32_bf16 v[16:31], v[140:143], v[56:63], v152\n"
        "ds_read_b128 v[124:127], v158 offset:5696\n"
        "s_add_u32 s56, s92, 7168\n"
        "buffer_store_dwordx4 v[252:255], v162, s[52:55], s56 offen sc1\n"
        "v_exp_f32 v72, v72\n"
        "v_exp_f32 v73, v73\n"
        "v_exp_f32 v74, v74\n"
        "v_exp_f32 v75, v75\n"
        "v_smfmac_f32_32x32x32_bf16 v[32:47], v[136:139], v[64:71], v152\n"
        "ds_read_b64 v[128:129], v159 offset:5696\n"
        "s_add_u32 s56, s92, 8192\n"
        "buffer_store_dwordx4 v[252:255], v162, s[52:55], s56 offen sc1\n"
        "v_exp_f32 v76, v76\n"
        "v_exp_f32 v77, v77\n"
        "v_exp_f32 v78, v78\n"
        "v_exp_f32 v79, v79\n"
        "v_mfma_f32_32x32x16_bf16 v[96:111], v[130:133], v[118:121], 0\n"
        "v_exp_f32 v80, v80\n"
        "v_exp_f32 v81, v81\n"
        "v_exp_f32 v82, v82\n"
        "v_exp_f32 v83, v83\n"
        "v_mfma_f32_32x32x8_bf16 v[96:111], v[134:135], v[122:123], v[96:111]\n"
        "s_add_u32 s56, s92, 9216\n"
        "buffer_store_dwordx4 v[252:255], v162, s[52:55], s56 offen sc1\n"
        "v_exp_f32 v84, v84\n"
        "v_exp_f32 v85, v85\n"
        "v_exp_f32 v86, v86\n"
        "v_exp_f32 v87, v87\n"
        "v_smfmac_f32_32x32x32_bf16 v[32:47], v[140:143], v[72:79], v152\n"
        "v_exp_f32 v88, v88\n"
        "v_exp_f32 v89, v89\n"
        "v_exp_f32 v90, v90\n"
        "v_exp_f32 v91, v91\n"
        "s_waitcnt lgkmcnt(0)\n"
        "v_smfmac_f32_32x32x32_bf16 v[16:31], v[144:147], v[80:87], v152\n"
        "buffer_load_dword v153, v173, s[44:47], s75 offen offset:1280\n"
        "ds_read_b64_tr_b16 v[224:225], v171 offset:12864\n"
        "ds_read_b64_tr_b16 v[226:227], v171 offset:14144\n"
        "ds_read_b64_tr_b16 v[228:229], v171 offset:12896\n"
        "ds_read_b64_tr_b16 v[230:231], v171 offset:14176\n"
        "v_exp_f32 v92, v92\n"
        "v_exp_f32 v93, v93\n"
        "v_exp_f32 v94, v94\n"
        "v_exp_f32 v95, v95\n"
        "v_mfma_f32_32x32x16_bf16 v[48:63], v[124:127], v[112:115], 0\n"
        "ds_read_b128 v[136:139], v160 offset:5696\n"
        "ds_read_b64_tr_b16 v[232:233], v171 offset:12928\n"
        "ds_read_b64_tr_b16 v[234:235], v171 offset:14208\n"
        "ds_read_b64_tr_b16 v[236:237], v172\n"
        "ds_read_b64_tr_b16 v[238:239], v172 offset:3200\n"
        "v_exp_f32 v96, v96\n"
        "v_exp_f32 v97, v97\n"
        "v_exp_f32 v98, v98\n"
        "v_exp_f32 v99, v99\n"
        "v_mfma_f32_32x32x8_bf16 v[48:63], v[128:129], v[116:117], v[48:63]\n"
        "ds_read_b128 v[140:143], v161 offset:5696\n"
        "ds_read_b64_tr_b16 v[240:241], v172 offset:32\n"
        "ds_read_b64_tr_b16 v[242:243], v172 offset:3232\n"
        "ds_read_b64_tr_b16 v[244:245], v172 offset:64\n"
        "ds_read_b64_tr_b16 v[246:247], v172 offset:3264\n"
        "v_exp_f32 v100, v100\n"
        "v_exp_f32 v101, v101\n"
        "v_exp_f32 v102, v102\n"
        "v_exp_f32 v103, v103\n"
        "v_smfmac_f32_32x32x32_bf16 v[16:31], v[148:151], v[88:95], v152\n"
        "ds_read_b128 v[130:133], v158 offset:8544\n"
        "v_exp_f32 v104, v104\n"
        "v_exp_f32 v105, v105\n"
        "v_exp_f32 v106, v106\n"
        "v_exp_f32 v107, v107\n"
        "v_smfmac_f32_32x32x32_bf16 v[32:47], v[144:147], v[96:103], v152\n"
        "ds_read_b64 v[134:135], v159 offset:8544\n"
        "v_exp_f32 v108, v108\n"
        "v_exp_f32 v109, v109\n"
        "v_exp_f32 v110, v110\n"
        "v_exp_f32 v111, v111\n"
        "v_mfma_f32_32x32x16_bf16 v[64:79], v[124:127], v[118:121], 0\n"
        "v_exp_f32 v48, v48\n"
        "v_exp_f32 v49, v49\n"
        "v_exp_f32 v50, v50\n"
        "v_exp_f32 v51, v51\n"
        "v_mfma_f32_32x32x8_bf16 v[64:79], v[128:129], v[122:123], v[64:79]\n"
        "v_exp_f32 v52, v52\n"
        "v_exp_f32 v53, v53\n"
        "v_exp_f32 v54, v54\n"
        "v_exp_f32 v55, v55\n"
        "v_smfmac_f32_32x32x32_bf16 v[32:47], v[148:151], v[104:111], v152\n"
        "v_exp_f32 v56, v56\n"
        "v_exp_f32 v57, v57\n"
        "v_exp_f32 v58, v58\n"
        "v_exp_f32 v59, v59\n"
        "s_waitcnt lgkmcnt(0)\n"
        "v_smfmac_f32_32x32x32_bf16 v[16:31], v[136:139], v[48:55], v152\n"
        "ds_read_b64_tr_b16 v[248:249], v172 offset:96\n"
        "ds_read_b64_tr_b16 v[250:251], v172 offset:3296\n"
        "ds_read_b64_tr_b16 v[252:253], v172 offset:128\n"
        "ds_read_b64_tr_b16 v[254:255], v172 offset:3328\n"
        "s_setprio 0\n"
        "v_exp_f32 v60, v60\n"
        "v_exp_f32 v61, v61\n"
        "v_exp_f32 v62, v62\n"
        "v_exp_f32 v63, v63\n"
        "v_mfma_f32_32x32x16_bf16 v[80:95], v[130:133], v[112:115], 0\n"
        "ds_read_b128 v[144:147], v160 offset:8544\n"
        "v_exp_f32 v64, v64\n"
        "v_exp_f32 v65, v65\n"
        "v_exp_f32 v66, v66\n"
        "v_exp_f32 v67, v67\n"
        "v_mfma_f32_32x32x8_bf16 v[80:95], v[134:135], v[116:117], v[80:95]\n"
        "ds_read_b128 v[148:151], v161 offset:8544\n"
        "v_exp_f32 v68, v68\n"
        "v_exp_f32 v69, v69\n"
        "v_exp_f32 v70, v70\n"
        "v_exp_f32 v71, v71\n"
        "v_smfmac_f32_32x32x32_bf16 v[16:31], v[140:143], v[56:63], v152\n"
        "v_exp_f32 v72, v72\n"
        "v_exp_f32 v73, v73\n"
        "v_exp_f32 v74, v74\n"
        "v_exp_f32 v75, v75\n"
        "v_smfmac_f32_32x32x32_bf16 v[32:47], v[136:139], v[64:71], v152\n"
        "s_waitcnt vmcnt(6) lgkmcnt(0)\n"
        "s_barrier\n"
        "s_setprio 2\n"
        "ds_read_b128 v[124:127], v158 offset:22784\n"
        "ds_read_b64 v[128:129], v159 offset:22784\n"
        "s_mov_b64 exec, 0xffff\n"
        "s_mov_b32 s58, 0\n"
        "11:\n"
        "v_cmp_eq_u64 vcc, s[60:61], v[154:155]\n"
        "s_nop 1\n"
        "s_cmp_eq_u64 vcc, exec\n"
        "s_cbranch_scc1 12f\n"
        "s_sleep 2\n"
        "buffer_load_dwordx2 v[154:155], v164, s[52:55], 0 offen sc1\n"
        "s_add_u32 s58, s58, 1\n"
        "s_cmp_lt_u32 s58, 262144\n"
        "s_waitcnt vmcnt(0)\n"
        "s_cbranch_scc1 11b\n"
        "12:\n"
        "s_mov_b64 exec, -1\n"
        "v_exp_f32 v76, v76\n"
        "v_exp_f32 v77, v77\n"
        "v_exp_f32 v78, v78\n"
        "v_exp_f32 v79, v79\n"
        "v_mfma_f32_32x32x16_bf16 v[96:111], v[130:133], v[118:121], 0\n"
        "s_add_u32 m0, s71, 0\n"
        "s_add_u32 s56, s70, 3932160\n"
        "s_add_u32 s57, s56, s83\n"
        "buffer_load_dwordx4 v162, s[52:55], s57 offen sc1 lds\n"
        "v_exp_f32 v80, v80\n"
        "v_exp_f32 v81, v81\n"
        "v_exp_f32 v82, v82\n"
        "v_exp_f32 v83, v83\n"
        "v_mfma_f32_32x32x8_bf16 v[96:111], v[134:135], v[122:123], v[96:111]\n"
        "buffer_load_dwordx4 v175, s[52:55], s56 offen offset:1024 sc1 lds\n"
        "v_exp_f32 v84, v84\n"
        "v_exp_f32 v85, v85\n"
        "v_exp_f32 v86, v86\n"
        "v_exp_f32 v87, v87\n"
        "v_smfmac_f32_32x32x32_bf16 v[32:47], v[140:143], v[72:79], v152\n"
        "s_mov_b32 exec_hi, 0xffff\n"
        "buffer_load_dwordx4 v163, s[52:55], s56 offen offset:2048 sc1 lds\n"
        "s_mov_b32 exec_hi, -1\n"
        "v_exp_f32 v88, v88\n"
        "v_exp_f32 v89, v89\n"
        "v_exp_f32 v90, v90\n"
        "v_exp_f32 v91, v91\n"
        "s_waitcnt lgkmcnt(0)\n"
        "v_smfmac_f32_32x32x32_bf16 v[16:31], v[144:147], v[80:87], v152\n"
        "s_add_u32 m0, s71, 2848\n"
        "s_add_u32 s56, s56, 81920\n"
        "s_add_u32 s57, s56, s83\n"
        "buffer_load_dwordx4 v162, s[52:55], s57 offen sc1 lds\n"
        "v_exp_f32 v92, v92\n"
        "v_exp_f32 v93, v93\n"
        "v_exp_f32 v94, v94\n"
        "v_exp_f32 v95, v95\n"
        "v_mfma_f32_32x32x16_bf16 v[48:63], v[124:127], v[112:115], 0\n"
        "ds_read_b128 v[136:139], v160 offset:22784\n"
        "buffer_load_dwordx4 v175, s[52:55], s56 offen offset:1024 sc1 lds\n"
        "v_exp_f32 v96, v96\n"
        "v_exp_f32 v97, v97\n"
        "v_exp_f32 v98, v98\n"
        "v_exp_f32 v99, v99\n"
        "v_mfma_f32_32x32x8_bf16 v[48:63], v[128:129], v[116:117], v[48:63]\n"
        "ds_read_b128 v[140:143], v161 offset:22784\n"
        "s_mov_b32 exec_hi, 0xffff\n"
        "buffer_load_dwordx4 v163, s[52:55], s56 offen offset:2048 sc1 lds\n"
        "s_mov_b32 exec_hi, -1\n"
        "v_exp_f32 v100, v100\n"
        "v_exp_f32 v101, v101\n"
        "v_exp_f32 v102, v102\n"
        "v_exp_f32 v103, v103\n"
        "v_smfmac_f32_32x32x32_bf16 v[16:31], v[148:151], v[88:95], v152\n"
        "ds_read_b128 v[130:133], v158 offset:25632\n"
        "v_exp_f32 v104, v104\n"
        "v_exp_f32 v105, v105\n"
        "v_exp_f32 v106, v106\n"
        "v_exp_f32 v107, v107\n"
        "v_smfmac_f32_32x32x32_bf16 v[32:47], v[144:147], v[96:103], v152\n"
        "ds_read_b64 v[134:135], v159 offset:25632\n"
        "s_add_u32 m0, s66, 0\n"
        "s_add_u32 s56, s64, 0\n"
        "buffer_load_dwordx4 v165, s[44:47], s56 offen lds\n"
        "v_exp_f32 v108, v108\n"
        "v_exp_f32 v109, v109\n"
        "v_exp_f32 v110, v110\n"
        "v_exp_f32 v111, v111\n"
        "v_mfma_f32_32x32x16_bf16 v[64:79], v[124:127], v[118:121], 0\n"
        "s_add_u32 s56, s56, 31744\n"
        "buffer_load_dwordx4 v165, s[44:47], s56 offen offset:1024 lds\n"
        "v_exp_f32 v48, v48\n"
        "v_exp_f32 v49, v49\n"
        "v_exp_f32 v50, v50\n"
        "v_exp_f32 v51, v51\n"
        "v_mfma_f32_32x32x8_bf16 v[64:79], v[128:129], v[122:123], v[64:79]\n"
        "s_add_u32 s56, s56, 31744\n"
        "buffer_load_dwordx4 v165, s[44:47], s56 offen offset:2048 lds\n"
        "v_exp_f32 v52, v52\n"
        "v_exp_f32 v53, v53\n"
        "v_exp_f32 v54, v54\n"
        "v_exp_f32 v55, v55\n"
        "v_smfmac_f32_32x32x32_bf16 v[32:47], v[148:151], v[104:111], v152\n"
        "s_add_u32 s56, s56, 31744\n"
        "buffer_load_dwordx4 v165, s[44:47], s56 offen offset:3072 lds\n"
        "v_exp_f32 v56, v56\n"
        "v_exp_f32 v57, v57\n"
        "v_exp_f32 v58, v58\n"
        "v_exp_f32 v59, v59\n"
        "s_waitcnt lgkmcnt(0)\n"
        "v_smfmac_f32_32x32x32_bf16 v[16:31], v[136:139], v[48:55], v152\n"
        "s_add_u32 m0, s67, 0\n"
        "s_add_u32 s56, s65, 0\n"
        "buffer_load_dwordx4 v162, s[52:55], s56 offen sc1 lds\n"
        "v_exp_f32 v60, v60\n"
        "v_exp_f32 v61, v61\n"
        "v_exp_f32 v62, v62\n"
        "v_exp_f32 v63, v63\n"
        "v_mfma_f32_32x32x16_bf16 v[80:95], v[130:133], v[112:115], 0\n"
        "ds_read_b128 v[144:147], v160 offset:25632\n"
        "buffer_load_dwordx4 v162, s[52:55], s56 offen offset:1024 sc1 lds\n"
        "v_exp_f32 v64, v64\n"
        "v_exp_f32 v65, v65\n"
        "v_exp_f32 v66, v66\n"
        "v_exp_f32 v67, v67\n"
        "v_mfma_f32_32x32x8_bf16 v[80:95], v[134:135], v[116:117], v[80:95]\n"
        "ds_read_b128 v[148:151], v161 offset:25632\n"
        "v_exp_f32 v68, v68\n"
        "v_exp_f32 v69, v69\n"
        "v_exp_f32 v70, v70\n"
        "v_exp_f32 v71, v71\n"
        "v_smfmac_f32_32x32x32_bf16 v[16:31], v[140:143], v[56:63], v152\n"
        "ds_read_b128 v[124:127], v158 offset:28480\n"
        "buffer_load_dwordx4 v162, s[52:55], s56 offen offset:2048 sc1 lds\n"
        "v_exp_f32 v72, v72\n"
        "v_exp_f32 v73, v73\n"
        "v_exp_f32 v74, v74\n"
        "v_exp_f32 v75, v75\n"
        "v_smfmac_f32_32x32x32_bf16 v[32:47], v[136:139], v[64:71], v152\n"
        "ds_read_b64 v[128:129], v159 offset:28480\n"
        "buffer_load_dwordx4 v162, s[52:55], s56 offen offset:3072 sc1 lds\n"
        "v_exp_f32 v76, v76\n"
        "v_exp_f32 v77, v77\n"
        "v_exp_f32 v78, v78\n"
        "v_exp_f32 v79, v79\n"
        "v_mfma_f32_32x32x16_bf16 v[96:111], v[130:133], v[118:121], 0\n"
        "s_add_u32 m0, s67, 4096\n"
        "s_add_u32 s56, s56, 4096\n"
        "buffer_load_dwordx4 v162, s[52:55], s56 offen sc1 lds\n"
        "v_exp_f32 v80, v80\n"
        "v_exp_f32 v81, v81\n"
        "v_exp_f32 v82, v82\n"
        "v_exp_f32 v83, v83\n"
        "v_mfma_f32_32x32x8_bf16 v[96:111], v[134:135], v[122:123], v[96:111]\n"
        "buffer_load_dword v153, v173, s[44:47], s75 offen offset:1536\n"
        "v_exp_f32 v84, v84\n"
        "v_exp_f32 v85, v85\n"
        "v_exp_f32 v86, v86\n"
        "v_exp_f32 v87, v87\n"
        "v_smfmac_f32_32x32x32_bf16 v[32:47], v[140:143], v[72:79], v152\n"
        "s_mov_b32 s56, 0\n"
        "buffer_load_dword v153, v174, s[52:55], s56 offen sc1\n"
        "v_exp_f32 v88, v88\n"
        "v_exp_f32 v89, v89\n"
        "v_exp_f32 v90, v90\n"
        "v_exp_f32 v91, v91\n"
        "s_waitcnt lgkmcnt(0)\n"
        "v_smfmac_f32_32x32x32_bf16 v[16:31], v[144:147], v[80:87], v152\n"
        "s_mov_b32 s56, 33280\n"
        "buffer_load_dword v153, v174, s[52:55], s56 offen sc1\n"
        "v_exp_f32 v92, v92\n"
        "v_exp_f32 v93, v93\n"
        "v_exp_f32 v94, v94\n"
        "v_exp_f32 v95, v95\n"
        "v_mfma_f32_32x32x16_bf16 v[48:63], v[124:127], v[112:115], 0\n"
        "ds_read_b128 v[136:139], v160 offset:28480\n"
        "v_exp_f32 v96, v96\n"
        "v_exp_f32 v97, v97\n"
        "v_exp_f32 v98, v98\n"
        "v_exp_f32 v99, v99\n"
        "v_mfma_f32_32x32x8_bf16 v[48:63], v[128:129], v[116:117], v[48:63]\n"
        "ds_read_b128 v[140:143], v161 offset:28480\n"
        "v_exp_f32 v100, v100\n"
        "v_exp_f32 v101, v101\n"
        "v_exp_f32 v102, v102\n"
        "v_exp_f32 v103, v103\n"
        "v_smfmac_f32_32x32x32_bf16 v[16:31], v[148:151], v[88:95], v152\n"
        "ds_read_b128 v[130:133], v158 offset:31328\n"
        "v_exp_f32 v104, v104\n"
        "v_exp_f32 v105, v105\n"
        "v_exp_f32 v106, v106\n"
        "v_exp_f32 v107, v107\n"
        "v_smfmac_f32_32x32x32_bf16 v[32:47], v[144:147], v[96:103], v152\n"
        "ds_read_b64 v[134:135], v159 offset:31328\n"
        "v_exp_f32 v108, v108\n"
        "v_exp_f32 v109, v109\n"
        "v_exp_f32 v110, v110\n"
        "v_exp_f32 v111, v111\n"
        "v_mfma_f32_32x32x16_bf16 v[64:79], v[124:127], v[118:121], 0\n"
        "v_exp_f32 v48, v48\n"
        "v_exp_f32 v49, v49\n"
        "v_exp_f32 v50, v50\n"
        "v_exp_f32 v51, v51\n"
        "v_mfma_f32_32x32x8_bf16 v[64:79], v[128:129], v[122:123], v[64:79]\n"
        "v_exp_f32 v52, v52\n"
        "v_exp_f32 v53, v53\n"
        "v_exp_f32 v54, v54\n"
        "v_exp_f32 v55, v55\n"
        "v_smfmac_f32_32x32x32_bf16 v[32:47], v[148:151], v[104:111], v152\n"
        "v_exp_f32 v56, v56\n"
        "v_exp_f32 v57, v57\n"
        "v_exp_f32 v58, v58\n"
        "v_exp_f32 v59, v59\n"
        "s_waitcnt lgkmcnt(0)\n"
        "v_smfmac_f32_32x32x32_bf16 v[16:31], v[136:139], v[48:55], v152\n"
        "s_setprio 0\n"
        "v_exp_f32 v60, v60\n"
        "v_exp_f32 v61, v61\n"
        "v_exp_f32 v62, v62\n"
        "v_exp_f32 v63, v63\n"
        "v_mfma_f32_32x32x16_bf16 v[80:95], v[130:133], v[112:115], 0\n"
        "ds_read_b128 v[144:147], v160 offset:31328\n"
        "v_exp_f32 v64, v64\n"
        "v_exp_f32 v65, v65\n"
        "v_exp_f32 v66, v66\n"
        "v_exp_f32 v67, v67\n"
        "v_mfma_f32_32x32x8_bf16 v[80:95], v[134:135], v[116:117], v[80:95]\n"
        "ds_read_b128 v[148:151], v161 offset:31328\n"
        "v_exp_f32 v68, v68\n"
        "v_exp_f32 v69, v69\n"
        "v_exp_f32 v70, v70\n"
        "v_exp_f32 v71, v71\n"
        "v_smfmac_f32_32x32x32_bf16 v[16:31], v[140:143], v[56:63], v152\n"
        "v_exp_f32 v72, v72\n"
        "v_exp_f32 v73, v73\n"
        "v_exp_f32 v74, v74\n"
        "v_exp_f32 v75, v75\n"
        "v_smfmac_f32_32x32x32_bf16 v[32:47], v[136:139], v[64:71], v152\n"
        "s_waitcnt vmcnt(12) lgkmcnt(0)\n"
        "s_barrier\n"
        "s_setprio 2\n"
        "ds_read_b128 v[124:127], v158\n"
        "ds_read_b64 v[128:129], v159\n"
        "s_add_u32 m0, s71, 22784\n"
        "s_add_u32 s56, s70, 4587520\n"
        "s_add_u32 s57, s56, s83\n"
        "buffer_load_dwordx4 v162, s[52:55], s57 offen sc1 lds\n"
        "v_exp_f32 v76, v76\n"
        "v_exp_f32 v77, v77\n"
        "v_exp_f32 v78, v78\n"
        "v_exp_f32 v79, v79\n"
        "v_mfma_f32_32x32x16_bf16 v[96:111], v[130:133], v[118:121], 0\n"
        "buffer_load_dwordx4 v175, s[52:55], s56 offen offset:1024 sc1 lds\n"
        "v_exp_f32 v80, v80\n"
        "v_exp_f32 v81, v81\n"
        "v_exp_f32 v82, v82\n"
        "v_exp_f32 v83, v83\n"
        "v_mfma_f32_32x32x8_bf16 v[96:111], v[134:135], v[122:123], v[96:111]\n"
        "s_mov_b32 exec_hi, 0xffff\n"
        "buffer_load_dwordx4 v163, s[52:55], s56 offen offset:2048 sc1 lds\n"
        "s_mov_b32 exec_hi, -1\n"
        "v_exp_f32 v84, v84\n"
        "v_exp_f32 v85, v85\n"
        "v_exp_f32 v86, v86\n"
        "v_exp_f32 v87, v87\n"
        "v_smfmac_f32_32x32x32_bf16 v[32:47], v[140:143], v[72:79], v152\n"
        "s_add_u32 m0, s71, 25632\n"
        "s_add_u32 s56, s56, 81920\n"
        "s_add_u32 s57, s56, s83\n"
        "buffer_load_dwordx4 v162, s[52:55], s57 offen sc1 lds\n"
        "v_exp_f32 v88, v88\n"
        "v_exp_f32 v89, v89\n"
        "v_exp_f32 v90, v90\n"
        "v_exp_f32 v91, v91\n"
        "s_waitcnt lgkmcnt(0)\n"
        "v_smfmac_f32_32x32x32_bf16 v[16:31], v[144:147], v[80:87], v152\n"
        "buffer_load_dwordx4 v175, s[52:55], s56 offen offset:1024 sc1 lds\n"
        "v_exp_f32 v92, v92\n"
        "v_exp_f32 v93, v93\n"
        "v_exp_f32 v94, v94\n"
        "v_exp_f32 v95, v95\n"
        "v_mfma_f32_32x32x16_bf16 v[48:63], v[124:127], v[112:115], 0\n"
        "ds_read_b128 v[136:139], v160\n"
        "v_exp_f32 v96, v96\n"
        "v_exp_f32 v97, v97\n"
        "v_exp_f32 v98, v98\n"
        ".Lk_hop1:\n"
        "s_cbranch_execz .Lk_pad5\n"
        "v_exp_f32 v99, v99\n"
        "v_mfma_f32_32x32x8_bf16 v[48:63], v[128:129], v[116:117], v[48:63]\n"
        "ds_read_b128 v[140:143], v161\n"
        "s_mov_b32 exec_hi, 0xffff\n"
        "buffer_load_dwordx4 v163, s[52:55], s56 offen offset:2048 sc1 lds\n"
        "s_mov_b32 exec_hi, -1\n"
        "v_exp_f32 v100, v100\n"
        "v_exp_f32 v101, v101\n"
        "v_exp_f32 v102, v102\n"
        "v_exp_f32 v103, v103\n"
        "v_smfmac_f32_32x32x32_bf16 v[16:31], v[148:151], v[88:95], v152\n"
        "ds_read_b128 v[130:133], v158 offset:2848\n"
        "s_add_u32 m0, s66, 16384\n"
        "s_add_u32 s56, s64, 128\n"
        "buffer_load_dwordx4 v165, s[44:47], s56 offen lds\n"
        "v_exp_f32 v104, v104\n"
        "v_exp_f32 v105, v105\n"
        "v_exp_f32 v106, v106\n"
        "v_exp_f32 v107, v107\n"
        "v_smfmac_f32_32x32x32_bf16 v[32:47], v[144:147], v[96:103], v152\n"
        "ds_read_b64 v[134:135], v159 offset:2848\n"
        "s_add_u32 s56, s56, 31744\n"
        "buffer_load_dwordx4 v165, s[44:47], s56 offen offset:1024 lds\n"
        "v_exp_f32 v108, v108\n"
        "v_exp_f32 v109, v109\n"
        "v_exp_f32 v110, v110\n"
        "v_exp_f32 v111, v111\n"
        "v_mfma_f32_32x32x16_bf16 v[64:79], v[124:127], v[118:121], 0\n"
        "s_add_u32 s56, s56, 31744\n"
        "buffer_load_dwordx4 v165, s[44:47], s56 offen offset:2048 lds\n"
        "v_exp_f32 v48, v48\n"
        "v_exp_f32 v49, v49\n"
        "v_exp_f32 v50, v50\n"
        "v_exp_f32 v51, v51\n"
        "v_mfma_f32_32x32x8_bf16 v[64:79], v[128:129], v[122:123], v[64:79]\n"
        "v_exp_f32 v52, v52\n"
        "v_exp_f32 v53, v53\n"
        "v_exp_f32 v54, v54\n"
        "v_exp_f32 v55, v55\n"
        "v_smfmac_f32_32x32x32_bf16 v[32:47], v[148:151], v[104:111], v152\n"
        "s_add_u32 s56, s56, 31744\n"
        "buffer_load_dwordx4 v165, s[44:47], s56 offen offset:3072 lds\n"
        "v_exp_f32 v56, v56\n"
        "v_exp_f32 v57, v57\n"
        "v_exp_f32 v58, v58\n"
        "v_exp_f32 v59, v59\n"
        "s_waitcnt lgkmcnt(0)\n"
        "v_smfmac_f32_32x32x32_bf16 v[16:31], v[136:139], v[48:55], v152\n"
        "s_add_u32 m0, s67, 20480\n"
        "s_add_u32 s56, s65, 10240\n"
        "buffer_load_dwordx4 v162, s[52:55], s56 offen sc1 lds\n"
        "v_exp_f32 v60, v60\n"
        "v_exp_f32 v61, v61\n"
        "v_exp_f32 v62, v62\n"
        "v_exp_f32 v63, v63\n"
        "v_mfma_f32_32x32x16_bf16 v[80:95], v[130:133], v[112:115], 0\n"
        "ds_read_b128 v[144:147], v160 offset:2848\n"
        "buffer_load_dwordx4 v162, s[52:55], s56 offen offset:1024 sc1 lds\n"
        "v_exp_f32 v64, v64\n"
        "v_exp_f32 v65, v65\n"
        "v_exp_f32 v66, v66\n"
        "v_exp_f32 v67, v67\n"
        "v_mfma_f32_32x32x8_bf16 v[80:95], v[134:135], v[116:117], v[80:95]\n"
        "ds_read_b128 v[148:151], v161 offset:2848\n"
        "buffer_load_dwordx4 v162, s[52:55], s56 offen offset:2048 sc1 lds\n"
        "v_exp_f32 v68, v68\n"
        "v_exp_f32 v69, v69\n"
        "v_exp_f32 v70, v70\n"
        "v_exp_f32 v71, v71\n"
        "v_smfmac_f32_32x32x32_bf16 v[16:31], v[140:143], v[56:63], v152\n"
        "ds_read_b128 v[124:127], v158 offset:5696\n"
        "buffer_load_dwordx4 v162, s[52:55], s56 offen offset:3072 sc1 lds\n"
        "v_exp_f32 v72, v72\n"
        "v_exp_f32 v73, v73\n"
        "v_exp_f32 v74, v74\n"
        "v_exp_f32 v75, v75\n"
        "v_smfmac_f32_32x32x32_bf16 v[32:47], v[136:139], v[64:71], v152\n"
        "ds_read_b64 v[128:129], v159 offset:5696\n"
        "v_exp_f32 v76, v76\n"
        "v_exp_f32 v77, v77\n"
        "v_exp_f32 v78, v78\n"
        "v_exp_f32 v79, v79\n"
        "v_mfma_f32_32x32x16_bf16 v[96:111], v[130:133], v[118:121], 0\n"
        "s_add_u32 m0, s67, 24576\n"
        "s_add_u32 s56, s56, 4096\n"
        "buffer_load_dwordx4 v162, s[52:55], s56 offen sc1 lds\n"
        "v_exp_f32 v80, v80\n"
        "v_exp_f32 v81, v81\n"
        "v_exp_f32 v82, v82\n"
        "v_exp_f32 v83, v83\n"
        "v_mfma_f32_32x32x8_bf16 v[96:111], v[134:135], v[122:123], v[96:111]\n"
        "buffer_load_dword v153, v173, s[44:47], s75 offen offset:1792\n"
        "v_exp_f32 v84, v84\n"
        "v_exp_f32 v85, v85\n"
        "v_exp_f32 v86, v86\n"
        "v_exp_f32 v87, v87\n"
        "v_smfmac_f32_32x32x32_bf16 v[32:47], v[140:143], v[72:79], v152\n"
        "s_mov_b32 s56, 66560\n"
        "buffer_load_dword v153, v174, s[52:55], s56 offen sc1\n"
        "v_exp_f32 v88, v88\n"
        "v_exp_f32 v89, v89\n"
        "v_exp_f32 v90, v90\n"
        "v_exp_f32 v91, v91\n"
        "s_waitcnt lgkmcnt(0)\n"
        "v_smfmac_f32_32x32x32_bf16 v[16:31], v[144:147], v[80:87], v152\n"
        "global_load_dword v153, v15, %[pc]\n"
        "v_exp_f32 v92, v92\n"
        "v_exp_f32 v93, v93\n"
        "v_exp_f32 v94, v94\n"
        "v_exp_f32 v95, v95\n"
        "v_mfma_f32_32x32x16_bf16 v[48:63], v[124:127], v[112:115], 0\n"
        "ds_read_b128 v[136:139], v160 offset:5696\n"
        "v_exp_f32 v96, v96\n"
        "v_exp_f32 v97, v97\n"
        "v_exp_f32 v98, v98\n"
        "v_exp_f32 v99, v99\n"
        "v_mfma_f32_32x32x8_bf16 v[48:63], v[128:129], v[116:117], v[48:63]\n"
        "ds_read_b128 v[140:143], v161 offset:5696\n"
        "v_exp_f32 v100, v100\n"
        "v_exp_f32 v101, v101\n"
        "v_exp_f32 v102, v102\n"
        "v_exp_f32 v103, v103\n"
        "v_smfmac_f32_32x32x32_bf16 v[16:31], v[148:151], v[88:95], v152\n"
        "ds_read_b128 v[130:133], v158 offset:8544\n"
        "v_exp_f32 v104, v104\n"
        "v_exp_f32 v105, v105\n"
        "v_exp_f32 v106, v106\n"
        "v_exp_f32 v107, v107\n"
        "v_smfmac_f32_32x32x32_bf16 v[32:47], v[144:147], v[96:103], v152\n"
        "ds_read_b64 v[134:135], v159 offset:8544\n"
        "v_exp_f32 v108, v108\n"
        "v_exp_f32 v109, v109\n"
        "v_exp_f32 v110, v110\n"
        "v_exp_f32 v111, v111\n"
        "v_mfma_f32_32x32x16_bf16 v[64:79], v[124:127], v[118:121], 0\n"
        "v_exp_f32 v48, v48\n"
        "v_exp_f32 v49, v49\n"
        "v_exp_f32 v50, v50\n"
        "v_exp_f32 v51, v51\n"
        "v_mfma_f32_32x32x8_bf16 v[64:79], v[128:129], v[122:123], v[64:79]\n"
        "v_exp_f32 v52, v52\n"
        "v_exp_f32 v53, v53\n"
        "v_exp_f32 v54, v54\n"
        "v_exp_f32 v55, v55\n"
        "v_smfmac_f32_32x32x32_bf16 v[32:47], v[148:151], v[104:111], v152\n"
        "v_exp_f32 v56, v56\n"
        "v_exp_f32 v57, v57\n"
        "v_exp_f32 v58, v58\n"
        "v_exp_f32 v59, v59\n"
        "s_waitcnt lgkmcnt(0)\n"
        "v_smfmac_f32_32x32x32_bf16 v[16:31], v[136:139], v[48:55], v152\n"
        "s_setprio 0\n"
        "v_exp_f32 v60, v60\n"
        "v_exp_f32 v61, v61\n"
        "v_exp_f32 v62, v62\n"
        "v_exp_f32 v63, v63\n"
        "v_mfma_f32_32x32x16_bf16 v[80:95], v[130:133], v[112:115], 0\n"
        "ds_read_b128 v[144:147], v160 offset:8544\n"
        "v_exp_f32 v64, v64\n"
        "v_exp_f32 v65, v65\n"
        "v_exp_f32 v66, v66\n"
        "v_exp_f32 v67, v67\n"
        "v_mfma_f32_32x32x8_bf16 v[80:95], v[134:135], v[116:117], v[80:95]\n"
        "ds_read_b128 v[148:151], v161 offset:8544\n"
        "v_exp_f32 v68, v68\n"
        "v_exp_f32 v69, v69\n"
        "v_exp_f32 v70, v70\n"
        "v_exp_f32 v71, v71\n"
        "v_smfmac_f32_32x32x32_bf16 v[16:31], v[140:143], v[56:63], v152\n"
        "v_exp_f32 v72, v72\n"
        "v_exp_f32 v73, v73\n"
        "v_exp_f32 v74, v74\n"
        "v_exp_f32 v75, v75\n"
        "v_smfmac_f32_32x32x32_bf16 v[32:47], v[136:139], v[64:71], v152\n"
        "s_waitcnt vmcnt(12) lgkmcnt(0)\n"
        "s_barrier\n"
        "s_setprio 2\n"
        "ds_read_b128 v[124:127], v158 offset:22784\n"
        "ds_read_b64 v[128:129], v159 offset:22784\n"
        "s_add_u32 m0, s66, 32768\n"
        "s_add_u32 s56, s64, 256\n"
        "buffer_load_dwordx4 v165, s[44:47], s56 offen lds\n"
        "v_exp_f32 v76, v76\n"
        "v_exp_f32 v77, v77\n"
        "v_exp_f32 v78, v78\n"
        "v_exp_f32 v79, v79\n"
        "v_mfma_f32_32x32x16_bf16 v[96:111], v[130:133], v[118:121], 0\n"
        "v_exp_f32 v80, v80\n"
        "v_exp_f32 v81, v81\n"
        "v_exp_f32 v82, v82\n"
        "v_exp_f32 v83, v83\n"
        "v_mfma_f32_32x32x8_bf16 v[96:111], v[134:135], v[122:123], v[96:111]\n"
        "s_add_u32 s56, s56, 31744\n"
        "buffer_load_dwordx4 v165, s[44:47], s56 offen offset:1024 lds\n"
        "v_exp_f32 v84, v84\n"
        "v_exp_f32 v85, v85\n"
        "v_exp_f32 v86, v86\n"
        "v_exp_f32 v87, v87\n"
        "v_smfmac_f32_32x32x32_bf16 v[32:47], v[140:143], v[72:79], v152\n"
        "v_exp_f32 v88, v88\n"
        "v_exp_f32 v89, v89\n"
        "v_exp_f32 v90, v90\n"
        "v_exp_f32 v91, v91\n"
        "s_waitcnt lgkmcnt(0)\n"
        "v_smfmac_f32_32x32x32_bf16 v[16:31], v[144:147], v[80:87], v152\n"
        "s_add_u32 s56, s56, 31744\n"
        "buffer_load_dwordx4 v165, s[44:47], s56 offen offset:2048 lds\n"
        "v_exp_f32 v92, v92\n"
        "v_exp_f32 v93, v93\n"
        "v_exp_f32 v94, v94\n"
        "v_exp_f32 v95, v95\n"
        "v_mfma_f32_32x32x16_bf16 v[48:63], v[124:127], v[112:115], 0\n"
        "ds_read_b128 v[136:139], v160 offset:22784\n"
        "v_exp_f32 v96, v96\n"
        "v_exp_f32 v97, v97\n"
        "v_exp_f32 v98, v98\n"
        "v_exp_f32 v99, v99\n"
        "v_mfma_f32_32x32x8_bf16 v[48:63], v[128:129], v[116:117], v[48:63]\n"
        "ds_read_b128 v[140:143], v161 offset:22784\n"
        "s_add_u32 s56, s56, 31744\n"
        "buffer_load_dwordx4 v165, s[44:47], s56 offen offset:3072 lds\n"
        "v_exp_f32 v100, v100\n"
        "v_exp_f32 v101, v101\n"
        "v_exp_f32 v102, v102\n"
        "v_exp_f32 v103, v103\n"
        "v_smfmac_f32_32x32x32_bf16 v[16:31], v[148:151], v[88:95], v152\n"
        "ds_read_b128 v[130:133], v158 offset:25632\n"
        "v_exp_f32 v104, v104\n"
        "v_exp_f32 v105, v105\n"
        "v_exp_f32 v106, v106\n"
        "v_exp_f32 v107, v107\n"
        "v_smfmac_f32_32x32x32_bf16 v[32:47], v[144:147], v[96:103], v152\n"
        "ds_read_b64 v[134:135], v159 offset:25632\n"
        "s_add_u32 m0, s67, 40960\n"
        "s_add_u32 s56, s65, 20480\n"
        "buffer_load_dwordx4 v162, s[52:55], s56 offen sc1 lds\n"
        "v_exp_f32 v108, v108\n"
        "v_exp_f32 v109, v109\n"
        "v_exp_f32 v110, v110\n"
        "v_exp_f32 v111, v111\n"
        "v_mfma_f32_32x32x16_bf16 v[64:79], v[124:127], v[118:121], 0\n"
        "v_exp_f32 v48, v48\n"
        "v_exp_f32 v49, v49\n"
        "v_exp_f32 v50, v50\n"
        "v_exp_f32 v51, v51\n"
        "v_mfma_f32_32x32x8_bf16 v[64:79], v[128:129], v[122:123], v[64:79]\n"
        "v_exp_f32 v52, v52\n"
        "v_exp_f32 v53, v53\n"
        "v_exp_f32 v54, v54\n"
        "v_exp_f32 v55, v55\n"
        "v_smfmac_f32_32x32x32_bf16 v[32:47], v[148:151], v[104:111], v152\n"
        "buffer_load_dwordx4 v162, s[52:55], s56 offen offset:1024 sc1 lds\n"
        "v_exp_f32 v56, v56\n"
        "v_exp_f32 v57, v57\n"
        "v_exp_f32 v58, v58\n"
        "v_exp_f32 v59, v59\n"
        "s_waitcnt lgkmcnt(0)\n"
        "v_smfmac_f32_32x32x32_bf16 v[16:31], v[136:139], v[48:55], v152\n"
        "v_exp_f32 v60, v60\n"
        "v_exp_f32 v61, v61\n"
        "v_exp_f32 v62, v62\n"
        "v_exp_f32 v63, v63\n"
        "v_mfma_f32_32x32x16_bf16 v[80:95], v[130:133], v[112:115], 0\n"
        "ds_read_b128 v[144:147], v160 offset:25632\n"
        "buffer_load_dwordx4 v162, s[52:55], s56 offen offset:2048 sc1 lds\n"
        "v_exp_f32 v64, v64\n"
        "v_exp_f32 v65, v65\n"
        "v_exp_f32 v66, v66\n"
        "v_exp_f32 v67, v67\n"
        "v_mfma_f32_32x32x8_bf16 v[80:95], v[134:135], v[116:117], v[80:95]\n"
        "ds_read_b128 v[148:151], v161 offset:25632\n"
        "v_exp_f32 v68, v68\n"
        "v_exp_f32 v69, v69\n"
        "v_exp_f32 v70, v70\n"
        "v_exp_f32 v71, v71\n"
        "v_smfmac_f32_32x32x32_bf16 v[16:31], v[140:143], v[56:63], v152\n"
        "ds_read_b128 v[124:127], v158 offset:28480\n"
        "buffer_load_dwordx4 v162, s[52:55], s56 offen offset:3072 sc1 lds\n"
        "v_exp_f32 v72, v72\n"
        "v_exp_f32 v73, v73\n"
        "v_exp_f32 v74, v74\n"
        "v_exp_f32 v75, v75\n"
        "v_smfmac_f32_32x32x32_bf16 v[32:47], v[136:139], v[64:71], v152\n"
        "ds_read_b64 v[128:129], v159 offset:28480\n"
        "v_exp_f32 v76, v76\n"
        "v_exp_f32 v77, v77\n"
        "v_exp_f32 v78, v78\n"
        "v_exp_f32 v79, v79\n"
        "v_mfma_f32_32x32x16_bf16 v[96:111], v[130:133], v[118:121], 0\n"
        "s_add_u32 m0, s67, 45056\n"
        "s_add_u32 s56, s56, 4096\n"
        "buffer_load_dwordx4 v162, s[52:55], s56 offen sc1 lds\n"
        "v_exp_f32 v80, v80\n"
        "v_exp_f32 v81, v81\n"
        "v_exp_f32 v82, v82\n"
        "v_exp_f32 v83, v83\n"
        "v_mfma_f32_32x32x8_bf16 v[96:111], v[134:135], v[122:123], v[96:111]\n"
        "v_exp_f32 v84, v84\n"
        "v_exp_f32 v85, v85\n"
        "v_exp_f32 v86, v86\n"
        "v_exp_f32 v87, v87\n"
        "v_smfmac_f32_32x32x32_bf16 v[32:47], v[140:143], v[72:79], v152\n"
        "s_mov_b32 s56, 99840\n"
        "buffer_load_dword v153, v174, s[52:55], s56 offen sc1\n"
        "v_exp_f32 v88, v88\n"
        "v_exp_f32 v89, v89\n"
        "v_exp_f32 v90, v90\n"
        "v_exp_f32 v91, v91\n"
        "s_waitcnt lgkmcnt(0)\n"
        "v_smfmac_f32_32x32x32_bf16 v[16:31], v[144:147], v[80:87], v152\n"
        "v_exp_f32 v92, v92\n"
        "v_exp_f32 v93, v93\n"
        "v_exp_f32 v94, v94\n"
        "v_exp_f32 v95, v95\n"
        "v_mfma_f32_32x32x16_bf16 v[48:63], v[124:127], v[112:115], 0\n"
        "ds_read_b128 v[136:139], v160 offset:28480\n"
        "v_exp_f32 v96, v96\n"
        "v_exp_f32 v97, v97\n"
        "v_exp_f32 v98, v98\n"
        "v_exp_f32 v99, v99\n"
        "v_mfma_f32_32x32x8_bf16 v[48:63], v[128:129], v[116:117], v[48:63]\n"
        "ds_read_b128 v[140:143], v161 offset:28480\n"
        "v_exp_f32 v100, v100\n"
        "v_exp_f32 v101, v101\n"
        "v_exp_f32 v102, v102\n"
        "v_exp_f32 v103, v103\n"
        "v_smfmac_f32_32x32x32_bf16 v[16:31], v[148:151], v[88:95], v152\n"
        "ds_read_b128 v[130:133], v158 offset:31328\n"
        "v_exp_f32 v104, v104\n"
        "v_exp_f32 v105, v105\n"
        "v_exp_f32 v106, v106\n"
        "v_exp_f32 v107, v107\n"
        "v_smfmac_f32_32x32x32_bf16 v[32:47], v[144:147], v[96:103], v152\n"
        "ds_read_b64 v[134:135], v159 offset:31328\n"
        "v_exp_f32 v108, v108\n"
        "v_exp_f32 v109, v109\n"
        "v_exp_f32 v110, v110\n"
        "v_exp_f32 v111, v111\n"
        "v_mfma_f32_32x32x16_bf16 v[64:79], v[124:127], v[118:121], 0\n"
        "v_exp_f32 v48, v48\n"
        "v_exp_f32 v49, v49\n"
        "v_exp_f32 v50, v50\n"
        "v_exp_f32 v51, v51\n"
        "v_mfma_f32_32x32x8_bf16 v[64:79], v[128:129], v[122:123], v[64:79]\n"
        "v_exp_f32 v52, v52\n"
        "v_exp_f32 v53, v53\n"
        "v_exp_f32 v54, v54\n"
        "v_exp_f32 v55, v55\n"
        "v_smfmac_f32_32x32x32_bf16 v[32:47], v[148:151], v[104:111], v152\n"
        "v_exp_f32 v56, v56\n"
        "v_exp_f32 v57, v57\n"
        "v_exp_f32 v58, v58\n"
        "v_exp_f32 v59, v59\n"
        "s_waitcnt lgkmcnt(0)\n"
        "v_smfmac_f32_32x32x32_bf16 v[16:31], v[136:139], v[48:55], v152\n"
        "s_setprio 0\n"
        "v_exp_f32 v60, v60\n"
        "v_exp_f32 v61, v61\n"
        "v_exp_f32 v62, v62\n"
        "v_exp_f32 v63, v63\n"
        "v_mfma_f32_32x32x16_bf16 v[80:95], v[130:133], v[112:115], 0\n"
        "ds_read_b128 v[144:147], v160 offset:31328\n"
        "v_exp_f32 v64, v64\n"
        "v_exp_f32 v65, v65\n"
        "v_exp_f32 v66, v66\n"
        "v_exp_f32 v67, v67\n"
        "v_mfma_f32_32x32x8_bf16 v[80:95], v[134:135], v[116:117], v[80:95]\n"
        "ds_read_b128 v[148:151], v161 offset:31328\n"
        "v_exp_f32 v68, v68\n"
        "v_exp_f32 v69, v69\n"
        "v_exp_f32 v70, v70\n"
        "v_exp_f32 v71, v71\n"
        "v_smfmac_f32_32x32x32_bf16 v[16:31], v[140:143], v[56:63], v152\n"
        "v_exp_f32 v72, v72\n"
        "v_exp_f32 v73, v73\n"
        "v_exp_f32 v74, v74\n"
        "v_exp_f32 v75, v75\n"
        "v_smfmac_f32_32x32x32_bf16 v[32:47], v[136:139], v[64:71], v152\n"
        "s_waitcnt vmcnt(10) lgkmcnt(0)\n"
        "s_barrier\n"
        "s_setprio 2\n"
        "v_exp_f32 v76, v76\n"
        "v_exp_f32 v77, v77\n"
        "v_exp_f32 v78, v78\n"
        "v_exp_f32 v79, v79\n"
        "v_mfma_f32_32x32x16_bf16 v[96:111], v[130:133], v[118:121], 0\n"
        "v_exp_f32 v80, v80\n"
        "v_exp_f32 v81, v81\n"
        "v_exp_f32 v82, v82\n"
        "v_exp_f32 v83, v83\n"
        "v_mfma_f32_32x32x8_bf16 v[96:111], v[134:135], v[122:123], v[96:111]\n"
        "v_exp_f32 v84, v84\n"
        "v_exp_f32 v85, v85\n"
        "v_exp_f32 v86, v86\n"
        "v_exp_f32 v87, v87\n"
        "v_smfmac_f32_32x32x32_bf16 v[32:47], v[140:143], v[72:79], v152\n"
        "v_exp_f32 v88, v88\n"
        "v_exp_f32 v89, v89\n"
        "v_exp_f32 v90, v90\n"
        "v_exp_f32 v91, v91\n"
        "s_waitcnt lgkmcnt(0)\n"
        "v_smfmac_f32_32x32x32_bf16 v[16:31], v[144:147], v[80:87], v152\n"
        "v_exp_f32 v92, v92\n"
        "v_exp_f32 v93, v93\n"
        "v_exp_f32 v94, v94\n"
        "v_exp_f32 v95, v95\n"
        "v_exp_f32 v96, v96\n"
        "v_exp_f32 v97, v97\n"
        "v_exp_f32 v98, v98\n"
        "v_exp_f32 v99, v99\n"
        "v_exp_f32 v100, v100\n"
        "v_exp_f32 v101, v101\n"
        "v_exp_f32 v102, v102\n"
        "v_exp_f32 v103, v103\n"
        "v_smfmac_f32_32x32x32_bf16 v[16:31], v[148:151], v[88:95], v152\n"
        "v_exp_f32 v104, v104\n"
        "v_exp_f32 v105, v105\n"
        "v_exp_f32 v106, v106\n"
        "v_exp_f32 v107, v107\n"
        "v_smfmac_f32_32x32x32_bf16 v[32:47], v[144:147], v[96:103], v152\n"
        "v_exp_f32 v108, v108\n"
        "v_exp_f32 v109, v109\n"
        "v_exp_f32 v110, v110\n"
        "v_exp_f32 v111, v111\n"
        "s_nop 0\n"
        "s_setprio 0\n"
        "v_smfmac_f32_32x32x32_bf16 v[32:47], v[148:151], v[104:111], v152\n"
        "v_add_u32 v80, s76, v162\n"
        "v_lshrrev_b32 v81, 2, v162\n"
        "v_add_u32 v81, s76, v81\n"
        "ds_write_b128 v80, v[16:19]\n"
        "ds_write_b128 v80, v[20:23] offset:1024\n"
        "ds_write_b128 v80, v[24:27] offset:2048\n"
        "ds_write_b32 v81, v28 offset:3072\n"
        "s_nop 4\n"
        "ds_write_b128 v80, v[32:35] offset:3328\n"
        "ds_write_b128 v80, v[36:39] offset:4352\n"
        "ds_write_b128 v80, v[40:43] offset:5376\n"
        "ds_write_b32 v81, v44 offset:6400\n"
        "s_waitcnt vmcnt(0) lgkmcnt(0)\n"
        "s_barrier\n"
        "s_add_u32 m0, s66, 0\n"
        "s_add_u32 s56, s64, 384\n"
        "buffer_load_dwordx4 v165, s[44:47], s56 offen lds\n"
        "s_add_u32 s56, s56, 31744\n"
        "buffer_load_dwordx4 v165, s[44:47], s56 offen offset:1024 lds\n"
        "s_add_u32 s56, s56, 31744\n"
        "buffer_load_dwordx4 v165, s[44:47], s56 offen offset:2048 lds\n"
        "s_add_u32 s56, s56, 31744\n"
        "buffer_load_dwordx4 v165, s[44:47], s56 offen offset:3072 lds\n"
        "s_add_u32 m0, s67, 0\n"
        "s_add_u32 s56, s65, 30720\n"
        "buffer_load_dwordx4 v162, s[52:55], s56 offen sc1 lds\n"
        "buffer_load_dwordx4 v162, s[52:55], s56 offen offset:1024 sc1 lds\n"
        "buffer_load_dwordx4 v162, s[52:55], s56 offen offset:2048 sc1 lds\n"
        "buffer_load_dwordx4 v162, s[52:55], s56 offen offset:3072 sc1 lds\n"
        "s_add_u32 m0, s67, 4096\n"
        "s_add_u32 s56, s56, 4096\n"
        "buffer_load_dwordx4 v162, s[52:55], s56 offen sc1 lds\n"
        "s_waitcnt vmcnt(9)\n"
        "s_barrier\n"
        "s_add_u32 m0, s66, 16384\n"
        "s_add_u32 s56, s64, 512\n"
        "buffer_load_dwordx4 v165, s[44:47], s56 offen lds\n"
        "s_add_u32 s56, s56, 31744\n"
        "buffer_load_dwordx4 v165, s[44:47], s56 offen offset:1024 lds\n"
        "s_add_u32 s56, s56, 31744\n"
        "buffer_load_dwordx4 v165, s[44:47], s56 offen offset:2048 lds\n"
        "s_add_u32 s56, s56, 31744\n"
        "buffer_load_dwordx4 v165, s[44:47], s56 offen offset:3072 lds\n"
        "s_add_u32 m0, s67, 20480\n"
        "s_add_u32 s56, s65, 40960\n"
        "buffer_load_dwordx4 v162, s[52:55], s56 offen sc1 lds\n"
        "buffer_load_dwordx4 v162, s[52:55], s56 offen offset:1024 sc1 lds\n"
        "buffer_load_dwordx4 v162, s[52:55], s56 offen offset:2048 sc1 lds\n"
        "buffer_load_dwordx4 v162, s[52:55], s56 offen offset:3072 sc1 lds\n"
        "s_add_u32 m0, s67, 24576\n"
        "s_add_u32 s56, s56, 4096\n"
        "buffer_load_dwordx4 v162, s[52:55], s56 offen sc1 lds\n"
        "s_waitcnt vmcnt(9)\n"
        "s_barrier\n"
        "ds_read_b32 v82, v81 offset:3072\n"
        "s_add_u32 m0, s66, 32768\n"
        "s_add_u32 s56, s64, 640\n"
        "buffer_load_dwordx4 v165, s[44:47], s56 offen lds\n"
        "s_add_u32 s56, s56, 31744\n"
        "buffer_load_dwordx4 v165, s[44:47], s56 offen offset:1024 lds\n"
        "s_add_u32 s56, s56, 31744\n"
        "buffer_load_dwordx4 v165, s[44:47], s56 offen offset:2048 lds\n"
        "s_add_u32 s56, s56, 31744\n"
        "buffer_load_dwordx4 v165, s[44:47], s56 offen offset:3072 lds\n"
        "s_add_u32 m0, s67, 40960\n"
        "s_add_u32 s56, s65, 51200\n"
        "buffer_load_dwordx4 v162, s[52:55], s56 offen sc1 lds\n"
        "buffer_load_dwordx4 v162, s[52:55], s56 offen offset:1024 sc1 lds\n"
        "buffer_load_dwordx4 v162, s[52:55], s56 offen offset:2048 sc1 lds\n"
        "buffer_load_dwordx4 v162, s[52:55], s56 offen offset:3072 sc1 lds\n"
        "s_add_u32 m0, s67, 45056\n"
        "s_add_u32 s56, s56, 4096\n"
        "buffer_load_dwordx4 v162, s[52:55], s56 offen sc1 lds\n"
        "s_mov_b32 m0, s80\n"
        "s_waitcnt lgkmcnt(0)\n"
        "v_readfirstlane_b32 %[bad], v82\n"
        : "=&{s[60:61]}"(epoch), "=&{v[8:15]}"(v3), "=&{v[156:171]}"(vat), "=&{v[176:207]}"(wf0), "=&{v[208:239]}"(wf1), "=&{v[240:255]}"(wf2),
          "=&{s[64:79]}"(sat), "=&{s[44:47]}"(rd), "=&{s[52:55]}"(rw), [bad] "=&s"(bad)
        : [b] "s"(b), [w] "s"(w), [ln] "v"(lane), [ka] "s"(__builtin_amdgcn_kernarg_segment_ptr()), [pc] "s"(pc), [wsn] "n"(WS_END)
        : "v16", "v17", "v18", "v19", "v20", "v21", "v22", "v23", "v24", "v25", "v26", "v27", "v28", "v29", "v30", "v31", "v32", "v33", "v34", "v35", "v36", "v37", "v38", "v39", "v40", "v41", "v42", "v43", "v44", "v45", "v46", "v47", "v48", "v49", "v50", "v51", "v52", "v53", "v54", "v55", "v56", "v57", "v58", "v59", "v60", "v61", "v62", "v63", "v64", "v65", "v66", "v67", "v68", "v69", "v70", "v71", "v72", "v73", "v74", "v75", "v76", "v77", "v78", "v79", "v80", "v81", "v82", "v83", "v84", "v85", "v86", "v87", "v88", "v89", "v90", "v91", "v92", "v93", "v94", "v95", "v96", "v97", "v98", "v99", "v100", "v101", "v102", "v103", "v104", "v105", "v106", "v107", "v108", "v109", "v110", "v111", "v112", "v113", "v114", "v115", "v116", "v117", "v118", "v119", "v120", "v121", "v122", "v123", "v124", "v125", "v126", "v127", "v128", "v129", "v130", "v131", "v132", "v133", "v134", "v135", "v136", "v137", "v138", "v139", "v140", "v141", "v142", "v143", "v144", "v145", "v146", "v147", "v148", "v149", "v150", "v151", "v152", "v153", "v154", "v155", "v172", "v173", "v174", "v175", "s16", "s17", "s18", "s19", "s20", "s21", "s22", "s23", "s24", "s25", "s26", "s27", "s28", "s29", "s30", "s31", "s40", "s41", "s42", "s43", "s48", "s49", "s50", "s51", "s56", "s57", "s58", "s59", "s62", "s63", "s80", "s81", "s82", "s83", "s84", "s85", "s86", "s87", "s88", "s89", "s90", "s91", "s92", "s93", "s94", "s95", "s96", "s97", "scc", "vcc", "memory");
        asm volatile("" : "+v"(lane));
        if (__builtin_expect(__builtin_amdgcn_readfirstlane(bad) != 0, 0)) {
            const int head = (b & 7) * 4 + (b >> 6), qb = (b >> 3) & 7;
            const rsrc_t rs = __builtin_amdgcn_make_buffer_rsrc(ws, 0, WS_END, 0x00020000);
#pragma unroll 1
            for (int qt = 0; qt < 2; ++qt) {
                const int t = qb * 256 + w * 64 + qt * 32 + (lane & 31), h = lane >> 5;
                const f32x16 o = attn_true_max(rs, WS_Q + (t >> 5) * 40960 + (head >> 1) * 2560, head, lane);
                const float sc = (1.f - __uint_as_float(__builtin_amdgcn_raw_buffer_load_b32(rs, WS_G + t * 4, 0, 16))) / (h ? o[8] : o[12]);
                const unsigned ob = WS_O + ((t >> 4) * 32 + head) * 640;
                __builtin_amdgcn_raw_buffer_store_b128(u32x4{pk(o[0] * sc, o[1] * sc), pk(o[2] * sc, o[3] * sc), pk(o[4] * sc, o[5] * sc), pk(o[6] * sc, o[7] * sc)},
                                                       rs, ob + h * 256 + (t & 15) * 16, 0, 16);
                __builtin_amdgcn_raw_buffer_store_b64(u32x2{pk(o[8] * sc, o[9] * sc), pk(o[10] * sc, o[11] * sc)}, rs, h ? OOB : ob + 512 + (t & 15) * 8, 0, 16);
            }
            asm volatile("s_waitcnt vmcnt(0)" ::: "memory");
        }
        asm volatile(
        "s_mov_b32 s80, m0\n"
        "v_mov_b32 v64, v165\n"
        "v_mov_b32 v65, v162\n"
        "s_waitcnt vmcnt(9)\n"
        "s_barrier\n"
        "s_add_u32 m0, s66, 0\n"
        "s_add_u32 s56, s64, 768\n"
        "buffer_load_dwordx4 v64, s[44:47], s56 offen lds\n"
        "s_add_u32 s56, s56, 31744\n"
        "buffer_load_dwordx4 v64, s[44:47], s56 offen offset:1024 lds\n"
        "s_add_u32 s56, s56, 31744\n"
        "buffer_load_dwordx4 v64, s[44:47], s56 offen offset:2048 lds\n"
        "s_add_u32 s56, s56, 31744\n"
        "buffer_load_dwordx4 v64, s[44:47], s56 offen offset:3072 lds\n"
        "s_add_u32 m0, s67, 0\n"
        "s_add_u32 s56, s65, 61440\n"
        "buffer_load_dwordx4 v65, s[52:55], s56 offen sc1 lds\n"
        "buffer_load_dwordx4 v65, s[52:55], s56 offen offset:1024 sc1 lds\n"
        "buffer_load_dwordx4 v65, s[52:55], s56 offen offset:2048 sc1 lds\n"
        "buffer_load_dwordx4 v65, s[52:55], s56 offen offset:3072 sc1 lds\n"
        "s_add_u32 m0, s67, 4096\n"
        "s_add_u32 s56, s56, 4096\n"
        "buffer_load_dwordx4 v65, s[52:55], s56 offen sc1 lds\n"
        "s_waitcnt vmcnt(9)\n"
        "s_barrier\n"
        "s_add_u32 m0, s66, 16384\n"
        "s_add_u32 s56, s64, 896\n"
        "buffer_load_dwordx4 v64, s[44:47], s56 offen lds\n"
        "s_add_u32 s56, s56, 31744\n"
        "buffer_load_dwordx4 v64, s[44:47], s56 offen offset:1024 lds\n"
        "s_add_u32 s56, s56, 31744\n"
        "buffer_load_dwordx4 v64, s[44:47], s56 offen offset:2048 lds\n"
        "s_add_u32 s56, s56, 31744\n"
        "buffer_load_dwordx4 v64, s[44:47], s56 offen offset:3072 lds\n"
        "s_add_u32 m0, s67, 20480\n"
        "s_add_u32 s56, s65, 71680\n"
        "buffer_load_dwordx4 v65, s[52:55], s56 offen sc1 lds\n"
        "buffer_load_dwordx4 v65, s[52:55], s56 offen offset:1024 sc1 lds\n"
        "buffer_load_dwordx4 v65, s[52:55], s56 offen offset:2048 sc1 lds\n"
        "buffer_load_dwordx4 v65, s[52:55], s56 offen offset:3072 sc1 lds\n"
        "s_add_u32 m0, s67, 24576\n"
        "s_add_u32 s56, s56, 4096\n"
        "buffer_load_dwordx4 v65, s[52:55], s56 offen sc1 lds\n"
        "s_waitcnt vmcnt(9)\n"
        "s_barrier\n"
        "s_add_u32 m0, s66, 32768\n"
        "s_add_u32 s56, s64, 1024\n"
        "buffer_load_dwordx4 v64, s[44:47], s56 offen lds\n"
        "s_add_u32 s56, s56, 31744\n"
        "buffer_load_dwordx4 v64, s[44:47], s56 offen offset:1024 lds\n"
        "s_add_u32 s56, s56, 31744\n"
        "buffer_load_dwordx4 v64, s[44:47], s56 offen offset:2048 lds\n"
        "s_add_u32 s56, s56, 31744\n"
        "buffer_load_dwordx4 v64, s[44:47], s56 offen offset:3072 lds\n"
        "s_add_u32 m0, s67, 40960\n"
        "s_add_u32 s56, s65, 81920\n"
        "buffer_load_dwordx4 v65, s[52:55], s56 offen sc1 lds\n"
        "buffer_load_dwordx4 v65, s[52:55], s56 offen offset:1024 sc1 lds\n"
        "buffer_load_dwordx4 v65, s[52:55], s56 offen offset:2048 sc1 lds\n"
        "buffer_load_dwordx4 v65, s[52:55], s56 offen offset:3072 sc1 lds\n"
        "s_add_u32 m0, s67, 45056\n"
        "s_add_u32 s56, s56, 4096\n"
        "buffer_load_dwordx4 v65, s[52:55], s56 offen sc1 lds\n"
        "v_mov_b32 v56, s60\n"
        "v_mov_b32 v57, s61\n"
        "s_mov_b64 exec, 1\n"
        "buffer_store_dwordx2 v[56:57], v65, s[52:55], s73 offen sc1\n"
        "s_mov_b64 exec, -1\n"
        "s_waitcnt vmcnt(10)\n"
        "s_barrier\n"
        "s_add_u32 m0, s66, 0\n"
        "s_add_u32 s56, s64, 1152\n"
        "buffer_load_dwordx4 v64, s[44:47], s56 offen lds\n"
        "s_add_u32 s56, s56, 31744\n"
        "buffer_load_dwordx4 v64, s[44:47], s56 offen offset:1024 lds\n"
        "s_add_u32 s56, s56, 31744\n"
        "buffer_load_dwordx4 v64, s[44:47], s56 offen offset:2048 lds\n"
        "s_add_u32 s56, s56, 31744\n"
        "buffer_load_dwordx4 v64, s[44:47], s56 offen offset:3072 lds\n"
        "s_add_u32 m0, s67, 0\n"
        "s_add_u32 s56, s65, 92160\n"
        "buffer_load_dwordx4 v65, s[52:55], s56 offen sc1 lds\n"
        "buffer_load_dwordx4 v65, s[52:55], s56 offen offset:1024 sc1 lds\n"
        "buffer_load_dwordx4 v65, s[52:55], s56 offen offset:2048 sc1 lds\n"
        "buffer_load_dwordx4 v65, s[52:55], s56 offen offset:3072 sc1 lds\n"
        "s_add_u32 m0, s67, 4096\n"
        "s_add_u32 s56, s56, 4096\n"
        "buffer_load_dwordx4 v65, s[52:55], s56 offen sc1 lds\n"
        "s_waitcnt vmcnt(10)\n"
        "s_barrier\n"
        "s_add_u32 m0, s66, 16384\n"
        "s_add_u32 s56, s64, 1280\n"
        "buffer_load_dwordx4 v64, s[44:47], s56 offen lds\n"
        "s_add_u32 s56, s56, 31744\n"
        "buffer_load_dwordx4 v64, s[44:47], s56 offen offset:1024 lds\n"
        "s_add_u32 s56, s56, 31744\n"
        "buffer_load_dwordx4 v64, s[44:47], s56 offen offset:2048 lds\n"
        "s_add_u32 s56, s56, 31744\n"
        "buffer_load_dwordx4 v64, s[44:47], s56 offen offset:3072 lds\n"
        "s_add_u32 m0, s67, 20480\n"
        "s_add_u32 s56, s65, 102400\n"
        "buffer_load_dwordx4 v65, s[52:55], s56 offen sc1 lds\n"
        "buffer_load_dwordx4 v65, s[52:55], s56 offen offset:1024 sc1 lds\n"
        "buffer_load_dwordx4 v65, s[52:55], s56 offen offset:2048 sc1 lds\n"
        "buffer_load_dwordx4 v65, s[52:55], s56 offen offset:3072 sc1 lds\n"
        "s_add_u32 m0, s67, 24576\n"
        "s_add_u32 s56, s56, 4096\n"
        "buffer_load_dwordx4 v65, s[52:55], s56 offen sc1 lds\n"
        "s_waitcnt vmcnt(9)\n"
        "s_barrier\n"
        "s_add_u32 m0, s66, 32768\n"
        "s_add_u32 s56, s64, 1408\n"
        "buffer_load_dwordx4 v64, s[44:47], s56 offen lds\n"
        "ds_read_b64 v[68:69], v13\n"
        "s_add_u32 s56, s56, 31744\n"
        "buffer_load_dwordx4 v64, s[44:47], s56 offen offset:1024 lds\n"
        "ds_read_b64 v[70:71], v13 offset:512\n"
        "s_add_u32 s56, s56, 31744\n"
        "buffer_load_dwordx4 v64, s[44:47], s56 offen offset:2048 lds\n"
        "ds_read_b64 v[72:73], v13 offset:1024\n"
        "s_add_u32 s56, s56, 31744\n"
        "buffer_load_dwordx4 v64, s[44:47], s56 offen offset:3072 lds\n"
        "ds_read_b64 v[74:75], v13 offset:1536\n"
        "s_add_u32 m0, s67, 40960\n"
        "s_add_u32 s56, s65, 112640\n"
        "buffer_load_dwordx4 v65, s[52:55], s56 offen sc1 lds\n"
        "ds_read_b64 v[76:77], v13 offset:2048\n"
        "buffer_load_dwordx4 v65, s[52:55], s56 offen offset:1024 sc1 lds\n"
        "buffer_load_dwordx4 v65, s[52:55], s56 offen offset:2048 sc1 lds\n"
        "buffer_load_dwordx4 v65, s[52:55], s56 offen offset:3072 sc1 lds\n"
        "s_add_u32 m0, s67, 45056\n"
        "s_add_u32 s56, s56, 4096\n"
        "buffer_load_dwordx4 v65, s[52:55], s56 offen sc1 lds\n"
        "s_waitcnt vmcnt(9)\n"
        "s_barrier\n"
        "s_add_u32 m0, s66, 0\n"
        "s_add_u32 s56, s64, 1536\n"
        "buffer_load_dwordx4 v64, s[44:47], s56 offen lds\n"
        "ds_read_b64 v[78:79], v13 offset:2560\n"
        "s_add_u32 s56, s56, 31744\n"
        "buffer_load_dwordx4 v64, s[44:47], s56 offen offset:1024 lds\n"
        "ds_read_b64 v[80:81], v13 offset:3072\n"
        "s_add_u32 s56, s56, 31744\n"
        "buffer_load_dwordx4 v64, s[44:47], s56 offen offset:2048 lds\n"
        "ds_read_b64 v[82:83], v13 offset:3584\n"
        "s_add_u32 s56, s56, 31744\n"
        "buffer_load_dwordx4 v64, s[44:47], s56 offen offset:3072 lds\n"
        "ds_read_b64 v[84:85], v13 offset:4096\n"
        "s_add_u32 m0, s67, 0\n"
        "s_add_u32 s56, s65, 122880\n"
        "buffer_load_dwordx4 v65, s[52:55], s56 offen sc1 lds\n"
        "ds_read_b64 v[86:87], v13 offset:4608\n"
        "buffer_load_dwordx4 v65, s[52:55], s56 offen offset:1024 sc1 lds\n"
        "buffer_load_dwordx4 v65, s[52:55], s56 offen offset:2048 sc1 lds\n"
        "buffer_load_dwordx4 v65, s[52:55], s56 offen offset:3072 sc1 lds\n"
        "s_add_u32 m0, s67, 4096\n"
        "s_add_u32 s56, s56, 4096\n"
        "buffer_load_dwordx4 v65, s[52:55], s56 offen sc1 lds\n"
        "s_waitcnt vmcnt(9)\n"
        "s_barrier\n"
        "s_add_u32 m0, s66, 16384\n"
        "s_add_u32 s56, s64, 1664\n"
        "buffer_load_dwordx4 v64, s[44:47], s56 offen lds\n"
        "s_add_u32 s57, s77, 0\n"
        "s_and_b32 s57, s57, 3\n"
        "s_mul_i32 s57, s57, 20480\n"
        "s_add_u32 s56, s56, 31744\n"
        "buffer_load_dwordx4 v64, s[44:47], s56 offen offset:1024 lds\n"
        "s_add_u32 s57, s57, s79\n"
        "v_add_u32 v88, s57, v65\n"
        "s_add_u32 s57, s77, 1\n"
        "s_add_u32 s56, s56, 31744\n"
        "buffer_load_dwordx4 v64, s[44:47], s56 offen offset:2048 lds\n"
        "s_and_b32 s57, s57, 3\n"
        "s_mul_i32 s57, s57, 20480\n"
        "s_add_u32 s57, s57, s79\n"
        ".Lk_pad5:\n"
        "s_cbranch_execz .Lk_ret\n"
        "s_add_u32 s56, s56, 31744\n"
        "buffer_load_dwordx4 v64, s[44:47], s56 offen offset:3072 lds\n"
        "v_add_u32 v89, s57, v65\n"
        "s_add_u32 s57, s77, 2\n"
        "s_and_b32 s57, s57, 3\n"
        "s_add_u32 m0, s67, 20480\n"
        "s_add_u32 s56, s65, 133120\n"
        "buffer_load_dwordx4 v65, s[52:55], s56 offen sc1 lds\n"
        "s_mul_i32 s57, s57, 20480\n"
        "s_add_u32 s57, s57, s79\n"
        "v_add_u32 v90, s57, v65\n"
        "buffer_load_dwordx4 v65, s[52:55], s56 offen offset:1024 sc1 lds\n"
        "s_add_u32 s57, s77, 3\n"
        "s_and_b32 s57, s57, 3\n"
        "s_mul_i32 s57, s57, 20480\n"
        "buffer_load_dwordx4 v65, s[52:55], s56 offen offset:2048 sc1 lds\n"
        "s_add_u32 s57, s57, s79\n"
        "v_add_u32 v91, s57, v65\n"
        "buffer_load_dwordx4 v65, s[52:55], s56 offen offset:3072 sc1 lds\n"
        "s_add_u32 m0, s67, 24576\n"
        "s_add_u32 s56, s56, 4096\n"
        "buffer_load_dwordx4 v65, s[52:55], s56 offen sc1 lds\n"
        "s_waitcnt vmcnt(9)\n"
        "s_barrier\n"
        "s_add_u32 m0, s66, 32768\n"
        "s_add_u32 s56, s64, 1792\n"
        "buffer_load_dwordx4 v64, s[44:47], s56 offen lds\n"
        "s_add_u32 s56, s56, 31744\n"
        "buffer_load_dwordx4 v64, s[44:47], s56 offen offset:1024 lds\n"
        "s_add_u32 s56, s56, 31744\n"
        "buffer_load_dwordx4 v64, s[44:47], s56 offen offset:2048 lds\n"
        "s_add_u32 s56, s56, 31744\n"
        "buffer_load_dwordx4 v64, s[44:47], s56 offen offset:3072 lds\n"
        "s_add_u32 m0, s67, 40960\n"
        "s_add_u32 s56, s65, 143360\n"
        "buffer_load_dwordx4 v65, s[52:55], s56 offen sc1 lds\n"
        "buffer_load_dwordx4 v65, s[52:55], s56 offen offset:1024 sc1 lds\n"
        "buffer_load_dwordx4 v65, s[52:55], s56 offen offset:2048 sc1 lds\n"
        "buffer_load_dwordx4 v65, s[52:55], s56 offen offset:3072 sc1 lds\n"
        "s_add_u32 m0, s67, 45056\n"
        "s_add_u32 s56, s56, 4096\n"
        "buffer_load_dwordx4 v65, s[52:55], s56 offen sc1 lds\n"
        "s_mov_b64 exec, 0x7ff\n"
        "buffer_load_dwordx2 v[56:57], v10, s[52:55], 0 offen sc1\n"
        "s_mov_b64 exec, -1\n"
        "s_waitcnt vmcnt(10)\n"
        "s_barrier\n"
        "s_add_u32 m0, s66, 0\n"
        "s_add_u32 s56, s64, 1920\n"
        "buffer_load_dwordx4 v64, s[44:47], s56 offen lds\n"
        "s_add_u32 s56, s56, 31744\n"
        "buffer_load_dwordx4 v64, s[44:47], s56 offen offset:1024 lds\n"
        "s_add_u32 s56, s56, 31744\n"
        "buffer_load_dwordx4 v64, s[44:47], s56 offen offset:2048 lds\n"
        "s_add_u32 s56, s56, 31744\n"
        "buffer_load_dwordx4 v64, s[44:47], s56 offen offset:3072 lds\n"
        "s_add_u32 m0, s67, 0\n"
        "s_add_u32 s56, s65, 153600\n"
        "buffer_load_dwordx4 v65, s[52:55], s56 offen sc1 lds\n"
        "buffer_load_dwordx4 v65, s[52:55], s56 offen offset:1024 sc1 lds\n"
        "buffer_load_dwordx4 v65, s[52:55], s56 offen offset:2048 sc1 lds\n"
        "buffer_load_dwordx4 v65, s[52:55], s56 offen offset:3072 sc1 lds\n"
        "s_add_u32 m0, s67, 4096\n"
        "s_add_u32 s56, s56, 4096\n"
        "buffer_load_dwordx4 v65, s[52:55], s56 offen sc1 lds\n"
        "s_waitcnt vmcnt(10)\n"
        "s_barrier\n"
        "s_waitcnt vmcnt(9)\n"
        "s_mov_b64 exec, 0x7ff\n"
        "s_mov_b32 s58, 0\n"
        "5:\n"
        "v_cmp_eq_u64 vcc, s[60:61], v[56:57]\n"
        "s_nop 1\n"
        "s_cmp_eq_u64 vcc, exec\n"
        "s_cbranch_scc1 6f\n"
        "s_sleep 2\n"
        "buffer_load_dwordx2 v[56:57], v10, s[52:55], 0 offen sc1\n"
        "s_add_u32 s58, s58, 1\n"
        "s_cmp_lt_u32 s58, 262144\n"
        "s_waitcnt vmcnt(0)\n"
        "s_cbranch_scc1 5b\n"
        "6:\n"
        "s_mov_b64 exec, -1\n"
        "s_add_u32 s56, s77, 0\n"
        "s_and_b32 s56, s56, 3\n"
        "s_mul_i32 s56, s56, 20480\n"
        "s_add_u32 s56, s56, s74\n"
        "buffer_load_dwordx4 v[96:99], v11, s[52:55], s56 offen sc1\n"
        "buffer_load_dwordx4 v[112:115], v11, s[52:55], s56 offen offset:1280 sc1\n"
        "buffer_load_dwordx4 v[128:131], v11, s[52:55], s56 offen offset:2560 sc1\n"
        "buffer_load_dwordx4 v[144:147], v11, s[52:55], s56 offen offset:3840 sc1\n"
        "buffer_load_dwordx2 v[160:161], v12, s[52:55], s56 offen sc1\n"
        "buffer_load_dwordx2 v[162:163], v12, s[52:55], s56 offen offset:640 sc1\n"
        "s_add_u32 s56, s77, 1\n"
        "s_and_b32 s56, s56, 3\n"
        "s_mul_i32 s56, s56, 20480\n"
        "s_add_u32 s56, s56, s74\n"
        "buffer_load_dwordx4 v[100:103], v11, s[52:55], s56 offen sc1\n"
        "buffer_load_dwordx4 v[116:119], v11, s[52:55], s56 offen offset:1280 sc1\n"
        "buffer_load_dwordx4 v[132:135], v11, s[52:55], s56 offen offset:2560 sc1\n"
        "buffer_load_dwordx4 v[148:151], v11, s[52:55], s56 offen offset:3840 sc1\n"
        "buffer_load_dwordx2 v[164:165], v12, s[52:55], s56 offen sc1\n"
        "buffer_load_dwordx2 v[166:167], v12, s[52:55], s56 offen offset:640 sc1\n"
        "s_waitcnt vmcnt(12)\n"
        "s_barrier\n"
        "s_add_u32 s56, s77, 2\n"
        "s_and_b32 s56, s56, 3\n"
        "s_mul_i32 s56, s56, 20480\n"
        "s_add_u32 s56, s56, s74\n"
        "buffer_load_dwordx4 v[104:107], v11, s[52:55], s56 offen sc1\n"
        "buffer_load_dwordx4 v[120:123], v11, s[52:55], s56 offen offset:1280 sc1\n"
        "buffer_load_dwordx4 v[136:139], v11, s[52:55], s56 offen offset:2560 sc1\n"
        "buffer_load_dwordx4 v[152:155], v11, s[52:55], s56 offen offset:3840 sc1\n"
        "buffer_load_dwordx2 v[168:169], v12, s[52:55], s56 offen sc1\n"
        "buffer_load_dwordx2 v[170:171], v12, s[52:55], s56 offen offset:640 sc1\n"
        "s_add_u32 s56, s77, 3\n"
        "s_and_b32 s56, s56, 3\n"
        "s_mul_i32 s56, s56, 20480\n"
        "s_add_u32 s56, s56, s74\n"
        "buffer_load_dwordx4 v[108:111], v11, s[52:55], s56 offen sc1\n"
        "buffer_load_dwordx4 v[124:127], v11, s[52:55], s56 offen offset:1280 sc1\n"
        "buffer_load_dwordx4 v[140:143], v11, s[52:55], s56 offen offset:2560 sc1\n"
        "buffer_load_dwordx4 v[156:159], v11, s[52:55], s56 offen offset:3840 sc1\n"
        "buffer_load_dwordx2 v[172:173], v12, s[52:55], s56 offen sc1\n"
        "buffer_load_dwordx2 v[174:175], v12, s[52:55], s56 offen offset:640 sc1\n"
        "s_mov_b32 m0, s80\n"
        "s_waitcnt lgkmcnt(0)\n"
        "s_barrier\n"
        "s_waitcnt vmcnt(18)\n"
        "v_mfma_f32_16x16x32_bf16 v[16:19], v[176:179], v[96:99], 0\n"
        "v_mfma_f32_16x16x32_bf16 v[20:23], v[180:183], v[96:99], 0\n"
        "v_mfma_f32_16x16x32_bf16 v[24:27], v[184:187], v[96:99], 0\n"
        "v_mfma_f32_16x16x32_bf16 v[28:31], v[188:191], v[96:99], 0\n"
        "v_mfma_f32_16x16x32_bf16 v[32:35], v[192:195], v[96:99], 0\n"
        "v_mfma_f32_16x16x32_bf16 v[16:19], v[196:199], v[112:115], v[16:19]\n"
        "v_mfma_f32_16x16x32_bf16 v[20:23], v[200:203], v[112:115], v[20:23]\n"
        "v_mfma_f32_16x16x32_bf16 v[24:27], v[204:207], v[112:115], v[24:27]\n"
        "v_mfma_f32_16x16x32_bf16 v[28:31], v[208:211], v[112:115], v[28:31]\n"
        "v_mfma_f32_16x16x32_bf16 v[32:35], v[212:215], v[112:115], v[32:35]\n"
        "v_mfma_f32_16x16x32_bf16 v[16:19], v[216:219], v[128:131], v[16:19]\n"
        "v_mfma_f32_16x16x32_bf16 v[20:23], v[220:223], v[128:131], v[20:23]\n"
        "v_mfma_f32_16x16x32_bf16 v[24:27], v[224:227], v[128:131], v[24:27]\n"
        "v_mfma_f32_16x16x32_bf16 v[28:31], v[228:231], v[128:131], v[28:31]\n"
        "v_mfma_f32_16x16x32_bf16 v[32:35], v[232:235], v[128:131], v[32:35]\n"
        "v_mfma_f32_16x16x32_bf16 v[16:19], v[68:71], v[144:147], v[16:19]\n"
        "v_mfma_f32_16x16x32_bf16 v[20:23], v[72:75], v[144:147], v[20:23]\n"
        "v_mfma_f32_16x16x32_bf16 v[24:27], v[76:79], v[144:147], v[24:27]\n"
        "v_mfma_f32_16x16x32_bf16 v[28:31], v[80:83], v[144:147], v[28:31]\n"
        "v_mfma_f32_16x16x32_bf16 v[32:35], v[84:87], v[144:147], v[32:35]\n"
        "v_mfma_f32_16x16x32_bf16 v[16:19], v[236:239], v[160:163], v[16:19]\n"
        "v_mfma_f32_16x16x32_bf16 v[20:23], v[240:243], v[160:163], v[20:23]\n"
        "v_mfma_f32_16x16x32_bf16 v[24:27], v[244:247], v[160:163], v[24:27]\n"
        "v_mfma_f32_16x16x32_bf16 v[28:31], v[248:251], v[160:163], v[28:31]\n"
        "v_mfma_f32_16x16x32_bf16 v[32:35], v[252:255], v[160:163], v[32:35]\n"
        "s_waitcnt vmcnt(12)\n"
        "v_mfma_f32_16x16x32_bf16 v[36:39], v[176:179], v[100:103], 0\n"
        "v_mfma_f32_16x16x32_bf16 v[40:43], v[180:183], v[100:103], 0\n"
        "v_mfma_f32_16x16x32_bf16 v[44:47], v[184:187], v[100:103], 0\n"
        "v_mfma_f32_16x16x32_bf16 v[48:51], v[188:191], v[100:103], 0\n"
        "v_mfma_f32_16x16x32_bf16 v[52:55], v[192:195], v[100:103], 0\n"
        "v_mfma_f32_16x16x32_bf16 v[36:39], v[196:199], v[116:119], v[36:39]\n"
        "v_mfma_f32_16x16x32_bf16 v[40:43], v[200:203], v[116:119], v[40:43]\n"
        "v_mfma_f32_16x16x32_bf16 v[44:47], v[204:207], v[116:119], v[44:47]\n"
        "v_mfma_f32_16x16x32_bf16 v[48:51], v[208:211], v[116:119], v[48:51]\n"
        "ds_write_b128 v88, v[16:19]\n"
        "v_mfma_f32_16x16x32_bf16 v[52:55], v[212:215], v[116:119], v[52:55]\n"
        "v_mfma_f32_16x16x32_bf16 v[36:39], v[216:219], v[132:135], v[36:39]\n"
        "v_mfma_f32_16x16x32_bf16 v[40:43], v[220:223], v[132:135], v[40:43]\n"
        "ds_write_b128 v88, v[20:23] offset:4096\n"
        "v_mfma_f32_16x16x32_bf16 v[44:47], v[224:227], v[132:135], v[44:47]\n"
        "v_mfma_f32_16x16x32_bf16 v[48:51], v[228:231], v[132:135], v[48:51]\n"
        "v_mfma_f32_16x16x32_bf16 v[52:55], v[232:235], v[132:135], v[52:55]\n"
        "ds_write_b128 v88, v[24:27] offset:8192\n"
        "v_mfma_f32_16x16x32_bf16 v[36:39], v[68:71], v[148:151], v[36:39]\n"
        "v_mfma_f32_16x16x32_bf16 v[40:43], v[72:75], v[148:151], v[40:43]\n"
        "v_mfma_f32_16x16x32_bf16 v[44:47], v[76:79], v[148:151], v[44:47]\n"
        "ds_write_b128 v88, v[28:31] offset:12288\n"
        "v_mfma_f32_16x16x32_bf16 v[48:51], v[80:83], v[148:151], v[48:51]\n"
        "v_mfma_f32_16x16x32_bf16 v[52:55], v[84:87], v[148:151], v[52:55]\n"
        "v_mfma_f32_16x16x32_bf16 v[36:39], v[236:239], v[164:167], v[36:39]\n"
        "ds_write_b128 v88, v[32:35] offset:16384\n"
        "v_mfma_f32_16x16x32_bf16 v[40:43], v[240:243], v[164:167], v[40:43]\n"
        "v_mfma_f32_16x16x32_bf16 v[44:47], v[244:247], v[164:167], v[44:47]\n"
        "v_mfma_f32_16x16x32_bf16 v[48:51], v[248:251], v[164:167], v[48:51]\n"
        "v_mfma_f32_16x16x32_bf16 v[52:55], v[252:255], v[164:167], v[52:55]\n"
        "s_waitcnt vmcnt(6)\n"
        "v_mfma_f32_16x16x32_bf16 v[16:19], v[176:179], v[104:107], 0\n"
        "v_mfma_f32_16x16x32_bf16 v[20:23], v[180:183], v[104:107], 0\n"
        "v_mfma_f32_16x16x32_bf16 v[24:27], v[184:187], v[104:107], 0\n"
        "v_mfma_f32_16x16x32_bf16 v[28:31], v[188:191], v[104:107], 0\n"
        "v_mfma_f32_16x16x32_bf16 v[32:35], v[192:195], v[104:107], 0\n"
        "v_mfma_f32_16x16x32_bf16 v[16:19], v[196:199], v[120:123], v[16:19]\n"
        "v_mfma_f32_16x16x32_bf16 v[20:23], v[200:203], v[120:123], v[20:23]\n"
        "v_mfma_f32_16x16x32_bf16 v[24:27], v[204:207], v[120:123], v[24:27]\n"
        "v_mfma_f32_16x16x32_bf16 v[28:31], v[208:211], v[120:123], v[28:31]\n"
        "ds_write_b128 v89, v[36:39]\n"
        "v_mfma_f32_16x16x32_bf16 v[32:35], v[212:215], v[120:123], v[32:35]\n"
        "v_mfma_f32_16x16x32_bf16 v[16:19], v[216:219], v[136:139], v[16:19]\n"
        "v_mfma_f32_16x16x32_bf16 v[20:23], v[220:223], v[136:139], v[20:23]\n"
        "ds_write_b128 v89, v[40:43] offset:4096\n"
        "v_mfma_f32_16x16x32_bf16 v[24:27], v[224:227], v[136:139], v[24:27]\n"
        "v_mfma_f32_16x16x32_bf16 v[28:31], v[228:231], v[136:139], v[28:31]\n"
        "v_mfma_f32_16x16x32_bf16 v[32:35], v[232:235], v[136:139], v[32:35]\n"
        "ds_write_b128 v89, v[44:47] offset:8192\n"
        "v_mfma_f32_16x16x32_bf16 v[16:19], v[68:71], v[152:155], v[16:19]\n"
        "v_mfma_f32_16x16x32_bf16 v[20:23], v[72:75], v[152:155], v[20:23]\n"
        "v_mfma_f32_16x16x32_bf16 v[24:27], v[76:79], v[152:155], v[24:27]\n"
        "ds_write_b128 v89, v[48:51] offset:12288\n"
        "v_mfma_f32_16x16x32_bf16 v[28:31], v[80:83], v[152:155], v[28:31]\n"
        "v_mfma_f32_16x16x32_bf16 v[32:35], v[84:87], v[152:155], v[32:35]\n"
        "v_mfma_f32_16x16x32_bf16 v[16:19], v[236:239], v[168:171], v[16:19]\n"
        "ds_write_b128 v89, v[52:55] offset:16384\n"
        "v_mfma_f32_16x16x32_bf16 v[20:23], v[240:243], v[168:171], v[20:23]\n"
        "v_mfma_f32_16x16x32_bf16 v[24:27], v[244:247], v[168:171], v[24:27]\n"
        "v_mfma_f32_16x16x32_bf16 v[28:31], v[248:251], v[168:171], v[28:31]\n"
        "v_mfma_f32_16x16x32_bf16 v[32:35], v[252:255], v[168:171], v[32:35]\n"
        "s_waitcnt vmcnt(0)\n"
        "v_mfma_f32_16x16x32_bf16 v[36:39], v[176:179], v[108:111], 0\n"
        "v_mfma_f32_16x16x32_bf16 v[40:43], v[180:183], v[108:111], 0\n"
        "v_mfma_f32_16x16x32_bf16 v[44:47], v[184:187], v[108:111], 0\n"
        "v_mfma_f32_16x16x32_bf16 v[48:51], v[188:191], v[108:111], 0\n"
        "v_mfma_f32_16x16x32_bf16 v[52:55], v[192:195], v[108:111], 0\n"
        "v_mfma_f32_16x16x32_bf16 v[36:39], v[196:199], v[124:127], v[36:39]\n"
        "v_mfma_f32_16x16x32_bf16 v[40:43], v[200:203], v[124:127], v[40:43]\n"
        "v_mfma_f32_16x16x32_bf16 v[44:47], v[204:207], v[124:127], v[44:47]\n"
        "v_mfma_f32_16x16x32_bf16 v[48:51], v[208:211], v[124:127], v[48:51]\n"
        "ds_write_b128 v90, v[16:19]\n"
        "v_mfma_f32_16x16x32_bf16 v[52:55], v[212:215], v[124:127], v[52:55]\n"
        "v_mfma_f32_16x16x32_bf16 v[36:39], v[216:219], v[140:143], v[36:39]\n"
        "v_mfma_f32_16x16x32_bf16 v[40:43], v[220:223], v[140:143], v[40:43]\n"
        "ds_write_b128 v90, v[20:23] offset:4096\n"
        "v_mfma_f32_16x16x32_bf16 v[44:47], v[224:227], v[140:143], v[44:47]\n"
        "v_mfma_f32_16x16x32_bf16 v[48:51], v[228:231], v[140:143], v[48:51]\n"
        "v_mfma_f32_16x16x32_bf16 v[52:55], v[232:235], v[140:143], v[52:55]\n"
        "ds_write_b128 v90, v[24:27] offset:8192\n"
        "v_mfma_f32_16x16x32_bf16 v[36:39], v[68:71], v[156:159], v[36:39]\n"
        "v_mfma_f32_16x16x32_bf16 v[40:43], v[72:75], v[156:159], v[40:43]\n"
        "v_mfma_f32_16x16x32_bf16 v[44:47], v[76:79], v[156:159], v[44:47]\n"
        "ds_write_b128 v90, v[28:31] offset:12288\n"
        "v_mfma_f32_16x16x32_bf16 v[48:51], v[80:83], v[156:159], v[48:51]\n"
        "v_mfma_f32_16x16x32_bf16 v[52:55], v[84:87], v[156:159], v[52:55]\n"
        "v_mfma_f32_16x16x32_bf16 v[36:39], v[236:239], v[172:175], v[36:39]\n"
        "ds_write_b128 v90, v[32:35] offset:16384\n"
        "v_mfma_f32_16x16x32_bf16 v[40:43], v[240:243], v[172:175], v[40:43]\n"
        "v_mfma_f32_16x16x32_bf16 v[44:47], v[244:247], v[172:175], v[44:47]\n"
        "v_mfma_f32_16x16x32_bf16 v[48:51], v[248:251], v[172:175], v[48:51]\n"
        "v_mfma_f32_16x16x32_bf16 v[52:55], v[252:255], v[172:175], v[52:55]\n"
        "s_nop 7\n"
        "ds_write_b128 v91, v[36:39]\n"
        "ds_write_b128 v91, v[40:43] offset:4096\n"
        "ds_write_b128 v91, v[44:47] offset:8192\n"
        "ds_write_b128 v91, v[48:51] offset:12288\n"
        "ds_write_b128 v91, v[52:55] offset:16384\n"
        "s_waitcnt lgkmcnt(0)\n"
        "s_barrier\n"
        : "+{v[156:171]}"(vat)
        : "{v[8:15]}"(v3), "{v[176:207]}"(wf0), "{v[208:239]}"(wf1), "{v[240:255]}"(wf2), "{s[64:79]}"(sat), "{s[60:61]}"(epoch),
          "{s[44:47]}"(rd), "{s[52:55]}"(rw)
        : "v16", "v17", "v18", "v19", "v20", "v21", "v22", "v23", "v24", "v25", "v26", "v27", "v28", "v29", "v30", "v31", "v32", "v33", "v34", "v35", "v36", "v37", "v38", "v39", "v40", "v41", "v42", "v43", "v44", "v45", "v46", "v47", "v48", "v49", "v50", "v51", "v52", "v53", "v54", "v55", "v56", "v57", "v58", "v59", "v60", "v61", "v62", "v63", "v64", "v65", "v66", "v67", "v68", "v69", "v70", "v71", "v72", "v73", "v74", "v75", "v76", "v77", "v78", "v79", "v80", "v81", "v82", "v83", "v84", "v85", "v86", "v87", "v88", "v89", "v90", "v91", "v92", "v93", "v94", "v95", "v96", "v97", "v98", "v99", "v100", "v101", "v102", "v103", "v104", "v105", "v106", "v107", "v108", "v109", "v110", "v111", "v112", "v113", "v114", "v115", "v116", "v117", "v118", "v119", "v120", "v121", "v122", "v123", "v124", "v125", "v126", "v127", "v128", "v129", "v130", "v131", "v132", "v133", "v134", "v135", "v136", "v137", "v138", "v139", "v140", "v141", "v142", "v143", "v144", "v145", "v146", "v147", "v148", "v149", "v150", "v151", "v152", "v153", "v154", "v155", "v172", "v173", "v174", "v175", "s56", "s57", "s58", "s59", "s62", "s63", "s80", "scc", "vcc", "memory");
    }
}
}

extern "C" void kernel_launch(void* const* d_in, const int* in_sizes, int n_in, void* d_out, int out_size, void* d_ws, size_t ws_size, hipStream_t stream) {
    k_fused<<<256, 512, 0, stream>>>((const char*)d_in[0], (const char*)d_in[6], (char*)d_ws, (const char*)d_in[3], (const char*)d_in[4],
                                     (const char*)d_in[1], (const char*)d_in[2], (const char*)d_in[7], (const char*)d_in[8], (char*)d_out);
}
```
